```python
import torch

def _softmax(x, axis=-1):
    return torch.softmax(x, dim=axis)
def _gelu(x):
    return torch.nn.functional.gelu(x, approximate="tanh")
def _silu(x):
    return torch.nn.functional.silu(x)
def _sigmoid(x):
    return torch.sigmoid(x)
def _layernorm(x, axis=-1, eps=1e-5):
    m = x.mean(dim=axis, keepdim=True)
    v = x.var(dim=axis, keepdim=True, unbiased=False)
    return (x - m) / torch.sqrt(v + eps)
def _scatter_add(tgt, idx, src, axis=0):
    return tgt.clone().index_add_(axis, idx, src)
def _segment_sum(src, seg, n):
    out = torch.zeros((n,) + src.shape[1:], dtype=src.dtype)
    return out.index_add_(0, seg, src)
def _pad(x, pw):
    flat = tuple(v for pair in reversed(pw) for v in pair)
    return torch.nn.functional.pad(x, flat)

kernel_name = 'standalone__mha_softmax__near_ridge'

def setup_inputs():

    _w = (lambda a: (lambda t: t.to(torch.bfloat16) if t.is_floating_point() else t)(torch.as_tensor(a)))
    B, H, Sq, Sk, D = 8, 32, 496, 512, 192
    return {
        "q": _w((lambda *s: torch.randn(*s, dtype=torch.bfloat16) * 0.8)(B, H, Sq, D)),
        "k": _w((lambda *s: torch.randn(*s, dtype=torch.bfloat16) * 0.8)(B, H, Sk, D)),
        "v": _w((lambda *s: torch.randn(*s, dtype=torch.bfloat16) * 0.05)(B, H, Sk, D)),
    }

def reference(q, k, v):
    scale = q.shape[-1] ** -0.5
    scores = torch.einsum("bhqd,bhkd->bhqk", q, k) * scale
    p = _softmax(scores, axis=-1)
    return torch.einsum("bhqk,bhkd->bhqd", p, v)
```

```cpp
#include <hip/hip_runtime.h>
#include <hip/hip_bf16.h>

typedef float    f32x16 __attribute__((ext_vector_type(16)));
typedef float    f32x2  __attribute__((ext_vector_type(2)));
typedef __bf16   bf16x8 __attribute__((ext_vector_type(8)));
typedef __bf16   bf16x4 __attribute__((ext_vector_type(4)));
typedef __bf16   bf16x2 __attribute__((ext_vector_type(2)));
typedef unsigned u32x4  __attribute__((ext_vector_type(4)));
typedef unsigned u32x2  __attribute__((ext_vector_type(2)));
#define DI __device__ __forceinline__

constexpr int NH = 256, SQ = 496, SK = 512, D = 192;
constexpr int ROWB = D * 2;
constexpr int TILEB = 64 * ROWB;

DI unsigned cvtpk(float lo, float hi) { f32x2 v = {lo, hi}; bf16x2 b = __builtin_convertvector(v, bf16x2); return __builtin_bit_cast(unsigned, b); }
template <int OFF> DI bf16x4 tr_read(unsigned vb) { bf16x4 r; asm volatile("ds_read_b64_tr_b16 %0, %1 offset:%2" : "=&v"(r) : "v"(vb), "i"(OFF) : "memory"); return r; }

template <int KS, int DB> DI bf16x8 vfrag(unsigned vb) {
  bf16x4 lo = tr_read<1024 * (3 * (2 * KS + 0) + (DB >> 1)) + 512 * (DB & 1)>(vb);
  bf16x4 hi = tr_read<1024 * (3 * (2 * KS + 1) + (DB >> 1)) + 512 * (DB & 1)>(vb);
  asm volatile("s_waitcnt lgkmcnt(0)" : "+v"(lo), "+v"(hi) :: "memory");
  return __builtin_shufflevector(lo, hi, 0, 1, 2, 3, 4, 5, 6, 7);
}
template <int DB> DI void pv_db(f32x16& o, unsigned vb, const bf16x8* P) {
  bf16x4 l0 = tr_read<1024 * (3 * 0 + (DB >> 1)) + 512 * (DB & 1)>(vb), h0 = tr_read<1024 * (3 * 1 + (DB >> 1)) + 512 * (DB & 1)>(vb);
  bf16x4 l1 = tr_read<1024 * (3 * 2 + (DB >> 1)) + 512 * (DB & 1)>(vb), h1 = tr_read<1024 * (3 * 3 + (DB >> 1)) + 512 * (DB & 1)>(vb);
  bf16x4 l2 = tr_read<1024 * (3 * 4 + (DB >> 1)) + 512 * (DB & 1)>(vb), h2 = tr_read<1024 * (3 * 5 + (DB >> 1)) + 512 * (DB & 1)>(vb);
  bf16x4 l3 = tr_read<1024 * (3 * 6 + (DB >> 1)) + 512 * (DB & 1)>(vb), h3 = tr_read<1024 * (3 * 7 + (DB >> 1)) + 512 * (DB & 1)>(vb);
  asm volatile("s_waitcnt lgkmcnt(0)" : "+v"(l0), "+v"(h0), "+v"(l1), "+v"(h1), "+v"(l2), "+v"(h2), "+v"(l3), "+v"(h3) :: "memory");
#define PK(L, H) __builtin_shufflevector(L, H, 0, 1, 2, 3, 4, 5, 6, 7)
  o = __builtin_amdgcn_mfma_f32_32x32x16_bf16(PK(l0, h0), P[0], o, 0, 0, 0);
  o = __builtin_amdgcn_mfma_f32_32x32x16_bf16(PK(l1, h1), P[1], o, 0, 0, 0);
  o = __builtin_amdgcn_mfma_f32_32x32x16_bf16(PK(l2, h2), P[2], o, 0, 0, 0);
  o = __builtin_amdgcn_mfma_f32_32x32x16_bf16(PK(l3, h3), P[3], o, 0, 0, 0);
#undef PK
}

__global__ __launch_bounds__(512, 1)
void attn_v0(const unsigned short* __restrict__ Q, const unsigned short* __restrict__ K, const unsigned short* __restrict__ V, unsigned short* __restrict__ O) {
  __shared__ __attribute__((aligned(16))) char lds[4 * TILEB];
  const unsigned tid = threadIdx.x, lane = tid & 63u, r = lane & 31u, h = lane >> 5;
  const unsigned wid = __builtin_amdgcn_readfirstlane(tid >> 6);
  const unsigned b = blockIdx.x;
  const unsigned head = (b >> 4) * 8u + (b & 7u), half = (b >> 3) & 1u;
  const unsigned ldsb = (unsigned)(size_t)lds;
  __amdgpu_buffer_rsrc_t srdQ = __builtin_amdgcn_make_buffer_rsrc((void*)((const char*)Q + (size_t)head * SQ * ROWB), (short)0, SQ * ROWB, 0x00020000);
  __amdgpu_buffer_rsrc_t srdK = __builtin_amdgcn_make_buffer_rsrc((void*)((const char*)K + (size_t)head * SK * ROWB), (short)0, SK * ROWB, 0x00020000);
  __amdgpu_buffer_rsrc_t srdV = __builtin_amdgcn_make_buffer_rsrc((void*)((const char*)V + (size_t)head * SK * ROWB), (short)0, SK * ROWB, 0x00020000);
  __amdgpu_buffer_rsrc_t srdO = __builtin_amdgcn_make_buffer_rsrc((void*)((char*)O + (size_t)head * SQ * ROWB), (short)0, SQ * ROWB, 0x00020000);
  const unsigned row = 256u * half + 32u * wid + r;
  const unsigned ksrc = (lane >> 3) * ROWB + 16u * ((lane & 7u) ^ (4u * (wid & 1u) + (lane >> 4)));
  const unsigned vsrc = ((lane >> 2) & 7u) * ROWB + 64u * h + 16u * (lane & 3u);
  auto dma = [&](int t) {
    const unsigned slot = (unsigned)(t & 1) * TILEB;
#pragma unroll
    for (int cb = 0; cb < 3; ++cb) {
      unsigned m0v = ldsb + slot + cb * 8192u + wid * 1024u;
      unsigned so = (unsigned)t * TILEB + wid * 8u * ROWB + 128u * cb;
      asm volatile("s_mov_b32 m0, %0\n\ts_nop 0\n\tbuffer_load_dwordx4 %1, %2, %3 offen lds" :: "s"(m0v), "v"(ksrc), "s"(srdK), "s"(so) : "m0", "memory");
    }
#pragma unroll
    for (int cb = 0; cb < 3; ++cb) {
      unsigned m0v = ldsb + 2 * TILEB + slot + 1024u * (3u * wid + cb);
      unsigned so = (unsigned)t * TILEB + wid * 8u * ROWB + 128u * cb;
      asm volatile("s_mov_b32 m0, %0\n\ts_nop 0\n\tbuffer_load_dwordx4 %1, %2, %3 offen lds" :: "s"(m0v), "v"(vsrc), "s"(srdV), "s"(so) : "m0", "memory");
    }
  };
  dma(0);
  bf16x8 q[12];
#pragma unroll
  for (int s = 0; s < 12; ++s) q[s] = __builtin_bit_cast(bf16x8, __builtin_amdgcn_raw_buffer_load_b128(srdQ, row * ROWB + 32u * s + 16u * h, 0, 0));
  unsigned kb4[4];
#pragma unroll
  for (int j = 0; j < 4; ++j) kb4[j] = ldsb + r * 128u + 16u * ((2u * j + h) ^ ((r >> 1) & 7u));
  const unsigned vb0 = ldsb + 2 * TILEB + (((lane & 3u) << 3) | (((lane >> 2) & 3u) << 6) | (((lane >> 4) & 1u) << 5) | (h << 8));
  f32x16 o[6];
#pragma unroll
  for (int i = 0; i < 6; ++i) o[i] = f32x16{};
  float mc = -1e30f, l = 0.f;
  const float C = 0.07216878364870322f * 1.4426950408889634f;
  for (int t = 0; t < 8; ++t) {
    asm volatile("s_waitcnt vmcnt(0) lgkmcnt(0)\n\ts_barrier" ::: "memory");
    if (t + 1 < 8) dma(t + 1);
    const unsigned slot = (unsigned)(t & 1) * TILEB;
    f32x16 s0 = f32x16{}, s1 = f32x16{};
#pragma unroll
    for (int s = 0; s < 12; ++s) {
      const __attribute__((address_space(3))) char* p = (const __attribute__((address_space(3))) char*)(size_t)(kb4[s & 3] + slot);
      bf16x8 k0 = *(const __attribute__((address_space(3))) bf16x8*)(p + (s >> 2) * 8192);
      bf16x8 k1 = *(const __attribute__((address_space(3))) bf16x8*)(p + (s >> 2) * 8192 + 4096);
      s0 = __builtin_amdgcn_mfma_f32_32x32x16_bf16(k0, q[s], s0, 0, 0, 0);
      s1 = __builtin_amdgcn_mfma_f32_32x32x16_bf16(k1, q[s], s1, 0, 0, 0);
    }
    float pm = s0[0];
#pragma unroll
    for (int i = 1; i < 16; ++i) pm = fmaxf(pm, s0[i]);
#pragma unroll
    for (int i = 0; i < 16; ++i) pm = fmaxf(pm, s1[i]);
    { auto rr = __builtin_amdgcn_permlane32_swap(__float_as_uint(pm), __float_as_uint(pm), false, false);
      pm = fmaxf(__uint_as_float(rr[0]), __uint_as_float(rr[1])); }
    pm *= C;
    if (!__all(pm - mc <= 8.f)) {
      float mn = fmaxf(mc, pm); float a = __builtin_amdgcn_exp2f(mc - mn); mc = mn; l *= a;
#pragma unroll
      for (int i = 0; i < 6; ++i) o[i] *= a;
    }
    float ps = 0.f;
#pragma unroll
    for (int i = 0; i < 16; ++i) { s0[i] = __builtin_amdgcn_exp2f(fmaf(s0[i], C, -mc)); ps += s0[i]; }
#pragma unroll
    for (int i = 0; i < 16; ++i) { s1[i] = __builtin_amdgcn_exp2f(fmaf(s1[i], C, -mc)); ps += s1[i]; }
    l += ps;
    bf16x8 P[4];
#pragma unroll
    for (int ks = 0; ks < 2; ++ks) {
      u32x4 w0, w1;
#pragma unroll
      for (int e = 0; e < 4; ++e) { w0[e] = cvtpk(s0[8 * ks + 2 * e], s0[8 * ks + 2 * e + 1]); w1[e] = cvtpk(s1[8 * ks + 2 * e], s1[8 * ks + 2 * e + 1]); }
      P[ks] = __builtin_bit_cast(bf16x8, w0); P[2 + ks] = __builtin_bit_cast(bf16x8, w1);
    }
    const unsigned vb = vb0 + slot;
    pv_db<0>(o[0], vb, P); pv_db<1>(o[1], vb, P); pv_db<2>(o[2], vb, P);
    pv_db<3>(o[3], vb, P); pv_db<4>(o[4], vb, P); pv_db<5>(o[5], vb, P);
  }
  { auto rr = __builtin_amdgcn_permlane32_swap(__float_as_uint(l), __float_as_uint(l), false, false);
    l = __uint_as_float(rr[0]) + __uint_as_float(rr[1]); }
  const float il = 1.f / l;
#pragma unroll
  for (int db = 0; db < 6; ++db) {
#pragma unroll
    for (int g = 0; g < 4; g += 2) {
      unsigned a0 = cvtpk(o[db][4 * g] * il, o[db][4 * g + 1] * il), a1 = cvtpk(o[db][4 * g + 2] * il, o[db][4 * g + 3] * il);
      unsigned b0 = cvtpk(o[db][4 * g + 4] * il, o[db][4 * g + 5] * il), b1 = cvtpk(o[db][4 * g + 6] * il, o[db][4 * g + 7] * il);
      auto r0 = __builtin_amdgcn_permlane32_swap(a0, b0, false, false);
      auto r1 = __builtin_amdgcn_permlane32_swap(a1, b1, false, false);
      u32x4 w = {r0[0], r1[0], r0[1], r1[1]};
      __builtin_amdgcn_raw_buffer_store_b128(w, srdO, row * ROWB + 64u * db + 16u * g + 16u * h, 0, 0);
    }
  }
}

extern "C" void kernel_launch(void* const* d_in, const int* in_sizes, int n_in, void* d_out, int out_size, void* d_ws, size_t ws_size, hipStream_t stream) {
  hipLaunchKernelGGL(attn_v0, dim3(512), dim3(512), 0, stream, (const unsigned short*)d_in[0], (const unsigned short*)d_in[1], (const unsigned short*)d_in[2], (unsigned short*)d_out);
}
```

```cpp
#include <hip/hip_runtime.h>
#include <hip/hip_bf16.h>

typedef float    f32x16 __attribute__((ext_vector_type(16)));
typedef float    f32x2  __attribute__((ext_vector_type(2)));
typedef __bf16   bf16x8 __attribute__((ext_vector_type(8)));
typedef __bf16   bf16x4 __attribute__((ext_vector_type(4)));
typedef __bf16   bf16x2 __attribute__((ext_vector_type(2)));
typedef unsigned u32x4  __attribute__((ext_vector_type(4)));
typedef unsigned u32x2  __attribute__((ext_vector_type(2)));
#define DI __device__ __forceinline__

constexpr int NH = 256, SQ = 496, SK = 512, D = 192;
constexpr int ROWB = D * 2;
constexpr int TILEB = 64 * ROWB;

DI unsigned cvtpk(float lo, float hi) { f32x2 v = {lo, hi}; bf16x2 b = __builtin_convertvector(v, bf16x2); return __builtin_bit_cast(unsigned, b); }
template <int OFF> DI bf16x4 tr_read(unsigned vb) { bf16x4 r; asm volatile("ds_read_b64_tr_b16 %0, %1 offset:%2" : "=&v"(r) : "v"(vb), "i"(OFF) : "memory"); return r; }

template <int KS, int DB> DI bf16x8 vfrag(unsigned vb) {
  bf16x4 lo = tr_read<1024 * (3 * (2 * KS + 0) + (DB >> 1)) + 512 * (DB & 1)>(vb);
  bf16x4 hi = tr_read<1024 * (3 * (2 * KS + 1) + (DB >> 1)) + 512 * (DB & 1)>(vb);
  asm volatile("s_waitcnt lgkmcnt(0)" : "+v"(lo), "+v"(hi) :: "memory");
  return __builtin_shufflevector(lo, hi, 0, 1, 2, 3, 4, 5, 6, 7);
}
template <int DB> DI void pv_db(f32x16& o, unsigned vb, const bf16x8* P) {
  bf16x4 l0 = tr_read<1024 * (3 * 0 + (DB >> 1)) + 512 * (DB & 1)>(vb), h0 = tr_read<1024 * (3 * 1 + (DB >> 1)) + 512 * (DB & 1)>(vb);
  bf16x4 l1 = tr_read<1024 * (3 * 2 + (DB >> 1)) + 512 * (DB & 1)>(vb), h1 = tr_read<1024 * (3 * 3 + (DB >> 1)) + 512 * (DB & 1)>(vb);
  bf16x4 l2 = tr_read<1024 * (3 * 4 + (DB >> 1)) + 512 * (DB & 1)>(vb), h2 = tr_read<1024 * (3 * 5 + (DB >> 1)) + 512 * (DB & 1)>(vb);
  bf16x4 l3 = tr_read<1024 * (3 * 6 + (DB >> 1)) + 512 * (DB & 1)>(vb), h3 = tr_read<1024 * (3 * 7 + (DB >> 1)) + 512 * (DB & 1)>(vb);
  asm volatile("s_waitcnt lgkmcnt(0)" : "+v"(l0), "+v"(h0), "+v"(l1), "+v"(h1), "+v"(l2), "+v"(h2), "+v"(l3), "+v"(h3) :: "memory");
#define PK(L, H) __builtin_shufflevector(L, H, 0, 1, 2, 3, 4, 5, 6, 7)
  o = __builtin_amdgcn_mfma_f32_32x32x16_bf16(PK(l0, h0), P[0], o, 0, 0, 0);
  o = __builtin_amdgcn_mfma_f32_32x32x16_bf16(PK(l1, h1), P[1], o, 0, 0, 0);
  o = __builtin_amdgcn_mfma_f32_32x32x16_bf16(PK(l2, h2), P[2], o, 0, 0, 0);
  o = __builtin_amdgcn_mfma_f32_32x32x16_bf16(PK(l3, h3), P[3], o, 0, 0, 0);
#undef PK
}

__global__ __launch_bounds__(512, 1)
void attn_v0(const unsigned short* __restrict__ Q, const unsigned short* __restrict__ K, const unsigned short* __restrict__ V, unsigned short* __restrict__ O) {
  __shared__ __attribute__((aligned(16))) char lds[4 * TILEB];
  const unsigned tid = threadIdx.x, lane = tid & 63u, r = lane & 31u, h = lane >> 5;
  const unsigned wid = __builtin_amdgcn_readfirstlane(tid >> 6);
  const unsigned b = blockIdx.x;
  const unsigned head = (b >> 4) * 8u + (b & 7u), half = (b >> 3) & 1u;
  const unsigned ldsb = (unsigned)(size_t)lds;
  __amdgpu_buffer_rsrc_t srdQ = __builtin_amdgcn_make_buffer_rsrc((void*)((const char*)Q + (size_t)head * SQ * ROWB), (short)0, SQ * ROWB, 0x00020000);
  __amdgpu_buffer_rsrc_t srdK = __builtin_amdgcn_make_buffer_rsrc((void*)((const char*)K + (size_t)head * SK * ROWB), (short)0, SK * ROWB, 0x00020000);
  __amdgpu_buffer_rsrc_t srdV = __builtin_amdgcn_make_buffer_rsrc((void*)((const char*)V + (size_t)head * SK * ROWB), (short)0, SK * ROWB, 0x00020000);
  __amdgpu_buffer_rsrc_t srdO = __builtin_amdgcn_make_buffer_rsrc((void*)((char*)O + (size_t)head * SQ * ROWB), (short)0, SQ * ROWB, 0x00020000);
  const unsigned row = 256u * half + 32u * wid + r;
  const unsigned ksrc = (lane >> 3) * ROWB + 16u * ((lane & 7u) ^ (4u * (wid & 1u) + (lane >> 4)));
  const unsigned vsrc = ((lane >> 2) & 7u) * ROWB + 64u * h + 16u * (lane & 3u);
  auto dma = [&](int t) {
    const unsigned slot = (unsigned)(t & 1) * TILEB;
#pragma unroll
    for (int cb = 0; cb < 3; ++cb) {
      unsigned m0v = ldsb + slot + cb * 8192u + wid * 1024u;
      unsigned so = (unsigned)t * TILEB + wid * 8u * ROWB + 128u * cb;
      asm volatile("s_mov_b32 m0, %0\n\ts_nop 0\n\tbuffer_load_dwordx4 %1, %2, %3 offen lds" :: "s"(m0v), "v"(ksrc), "s"(srdK), "s"(so) : "m0", "memory");
    }
#pragma unroll
    for (int cb = 0; cb < 3; ++cb) {
      unsigned m0v = ldsb + 2 * TILEB + slot + 1024u * (3u * wid + cb);
      unsigned so = (unsigned)t * TILEB + wid * 8u * ROWB + 128u * cb;
      asm volatile("s_mov_b32 m0, %0\n\ts_nop 0\n\tbuffer_load_dwordx4 %1, %2, %3 offen lds" :: "s"(m0v), "v"(vsrc), "s"(srdV), "s"(so) : "m0", "memory");
    }
  };
  dma(0);
  bf16x8 q[12];
#pragma unroll
  for (int s = 0; s < 12; ++s) q[s] = __builtin_bit_cast(bf16x8, __builtin_amdgcn_raw_buffer_load_b128(srdQ, row * ROWB + 32u * s + 16u * h, 0, 0));
  unsigned kb4[4];
#pragma unroll
  for (int j = 0; j < 4; ++j) kb4[j] = ldsb + r * 128u + 16u * ((2u * j + h) ^ ((r >> 1) & 7u));
  const unsigned vb0 = ldsb + 2 * TILEB + (((lane & 3u) << 3) | (((lane >> 2) & 3u) << 6) | (((lane >> 4) & 1u) << 5) | (h << 8));
  f32x16 o[6];
#pragma unroll
  for (int i = 0; i < 6; ++i) o[i] = f32x16{};
  float mc = -1e30f, l = 0.f;
  const float C = 0.07216878364870322f * 1.4426950408889634f;
  for (int t = 0; t < 8; ++t) {
    asm volatile("s_waitcnt vmcnt(0) lgkmcnt(0)\n\ts_barrier" ::: "memory");
    if (t + 1 < 8) dma(t + 1);
    const unsigned slot = (unsigned)(t & 1) * TILEB;
    f32x16 s0 = f32x16{}, s1 = f32x16{};
#pragma unroll
    for (int s = 0; s < 12; ++s) {
      const __attribute__((address_space(3))) char* p = (const __attribute__((address_space(3))) char*)(size_t)(kb4[s & 3] + slot);
      bf16x8 k0 = *(const __attribute__((address_space(3))) bf16x8*)(p + (s >> 2) * 8192);
      bf16x8 k1 = *(const __attribute__((address_space(3))) bf16x8*)(p + (s >> 2) * 8192 + 4096);
      s0 = __builtin_amdgcn_mfma_f32_32x32x16_bf16(k0, q[s], s0, 0, 0, 0);
      s1 = __builtin_amdgcn_mfma_f32_32x32x16_bf16(k1, q[s], s1, 0, 0, 0);
    }
    float pm = s0[0];
#pragma unroll
    for (int i = 1; i < 16; ++i) pm = fmaxf(pm, s0[i]);
#pragma unroll
    for (int i = 0; i < 16; ++i) pm = fmaxf(pm, s1[i]);
    { auto rr = __builtin_amdgcn_permlane32_swap(__float_as_uint(pm), __float_as_uint(pm), false, false);
      pm = fmaxf(__uint_as_float(rr[0]), __uint_as_float(rr[1])); }
    pm *= C;
    if (!__all(pm - mc <= 8.f)) {
      float mn = fmaxf(mc, pm); float a = __builtin_amdgcn_exp2f(mc - mn); mc = mn; l *= a;
#pragma unroll
      for (int i = 0; i < 6; ++i) o[i] *= a;
    }
    float ps = 0.f;
#pragma unroll
    for (int i = 0; i < 16; ++i) { s0[i] = __builtin_amdgcn_exp2f(fmaf(s0[i], C, -mc)); ps += s0[i]; }
#pragma unroll
    for (int i = 0; i < 16; ++i) { s1[i] = __builtin_amdgcn_exp2f(fmaf(s1[i], C, -mc)); ps += s1[i]; }
    l += ps;
    bf16x8 P[4];
#pragma unroll
    for (int ks = 0; ks < 2; ++ks) {
      u32x4 w0, w1;
#pragma unroll
      for (int e = 0; e < 4; ++e) { w0[e] = cvtpk(s0[8 * ks + 2 * e], s0[8 * ks + 2 * e + 1]); w1[e] = cvtpk(s1[8 * ks + 2 * e], s1[8 * ks + 2 * e + 1]); }
      P[ks] = __builtin_bit_cast(bf16x8, w0); P[2 + ks] = __builtin_bit_cast(bf16x8, w1);
    }
    const unsigned vb = vb0 + slot;
    pv_db<0>(o[0], vb, P); pv_db<1>(o[1], vb, P); pv_db<2>(o[2], vb, P);
    pv_db<3>(o[3], vb, P); pv_db<4>(o[4], vb, P); pv_db<5>(o[5], vb, P);
  }
  { auto rr = __builtin_amdgcn_permlane32_swap(__float_as_uint(l), __float_as_uint(l), false, false);
    l = __uint_as_float(rr[0]) + __uint_as_float(rr[1]); }
  const float il = 1.f / l;
#pragma unroll
  for (int db = 0; db < 6; ++db) {
#pragma unroll
    for (int g = 0; g < 4; g += 2) {
      unsigned a0 = cvtpk(o[db][4 * g] * il, o[db][4 * g + 1] * il), a1 = cvtpk(o[db][4 * g + 2] * il, o[db][4 * g + 3] * il);
      unsigned b0 = cvtpk(o[db][4 * g + 4] * il, o[db][4 * g + 5] * il), b1 = cvtpk(o[db][4 * g + 6] * il, o[db][4 * g + 7] * il);
      auto r0 = __builtin_amdgcn_permlane32_swap(a0, b0, false, false);
      auto r1 = __builtin_amdgcn_permlane32_swap(a1, b1, false, false);
      u32x4 w = {r0[0], r1[0], r0[1], r1[1]};
      __builtin_amdgcn_raw_buffer_store_b128(w, srdO, row * ROWB + 64u * db + 16u * g + 16u * h, 0, 0);
    }
  }
}

#define INFL 24
#define PAIR 1
#define QFRAG 0
#define AUXQ ""
#define AUXKV ""
#define AUXO 0
#define STR2(x) #x
#define STR(x) STR2(x)
__global__ __launch_bounds__(256, 1)
void memfloor(const unsigned short* __restrict__ Q, const unsigned short* __restrict__ K, const unsigned short* __restrict__ V, unsigned short* __restrict__ O, unsigned long long* ts) {
  __shared__ __attribute__((aligned(16))) char lds[160 * 1024];
  const unsigned tid = threadIdx.x, lane = tid & 63u;
  const unsigned wid = __builtin_amdgcn_readfirstlane(tid >> 6);
  const unsigned b = blockIdx.x;
  const unsigned ldsb = (unsigned)(size_t)lds;
  unsigned long long t0 = __builtin_amdgcn_s_memrealtime();
  unsigned long long tq = 0;
  unsigned ring = 0;
  u32x4 acc = {0,0,0,0};
  for (int item = 0; item < 2; ++item) {
    const unsigned head = item * 128u + (b >> 4) * 8u + (b & 7u), half = ((b >> 3) & 1u) ^ item;
    __amdgpu_buffer_rsrc_t srdQ = __builtin_amdgcn_make_buffer_rsrc((void*)((const char*)Q + (size_t)head * SQ * ROWB), (short)0, SQ * ROWB, 0x00020000);
    __amdgpu_buffer_rsrc_t srdK = __builtin_amdgcn_make_buffer_rsrc((void*)((const char*)K + (size_t)head * SK * ROWB), (short)0, SK * ROWB, 0x00020000);
    __amdgpu_buffer_rsrc_t srdV = __builtin_amdgcn_make_buffer_rsrc((void*)((const char*)V + (size_t)head * SK * ROWB), (short)0, SK * ROWB, 0x00020000);
    __amdgpu_buffer_rsrc_t srdO = __builtin_amdgcn_make_buffer_rsrc((void*)((char*)O + (size_t)head * SQ * ROWB), (short)0, SQ * ROWB, 0x00020000);
    const unsigned src = (lane >> 3) * ROWB + 16u * (lane & 7u);
    for (int i = 0; i < 24; ++i) {
      unsigned m0v = ldsb + wid * 40960u + (ring % 40u) * 1024u; ring++;
      unsigned so = (256u * half + 64u * wid + 8u * (i / 3)) * ROWB + 128u * (i % 3);
      asm volatile("s_mov_b32 m0, %0\n\ts_nop 0\n\tbuffer_load_dwordx4 %1, %2, %3 offen" AUXQ " lds\n\ts_waitcnt vmcnt(" STR(INFL) ")" :: "s"(m0v), "v"(src), "s"(srdQ), "s"(so) : "m0", "memory");
    }
    for (int t = 0; t < 8; ++t) {
      for (int i = 0; i < 6; ++i) {
        unsigned m0v = ldsb + wid * 40960u + (ring % 40u) * 1024u; ring++;
        unsigned so = t * TILEB + (16u * wid + 8u * (i / 3)) * ROWB + 128u * (i % 3);
        asm volatile("s_mov_b32 m0, %0\n\ts_nop 0\n\tbuffer_load_dwordx4 %1, %2, %3 offen" AUXKV " lds\n\ts_waitcnt vmcnt(" STR(INFL) ")" :: "s"(m0v), "v"(src), "s"(srdK), "s"(so) : "m0", "memory");
      }
      for (int i = 0; i < 6; ++i) {
        unsigned m0v = ldsb + wid * 40960u + (ring % 40u) * 1024u; ring++;
        unsigned so = t * TILEB + (16u * wid + 8u * (i / 3)) * ROWB + 128u * (i % 3);
        asm volatile("s_mov_b32 m0, %0\n\ts_nop 0\n\tbuffer_load_dwordx4 %1, %2, %3 offen" AUXKV " lds\n\ts_waitcnt vmcnt(" STR(INFL) ")" :: "s"(m0v), "v"(src), "s"(srdV), "s"(so) : "m0", "memory");
      }
      if (item == 0 && t == 0) { asm volatile("s_waitcnt vmcnt(6)" ::: "memory"); tq = __builtin_amdgcn_s_memrealtime(); }
    }
    for (int i = 0; i < 24; ++i) {
      unsigned so = (256u * half + 64u * wid + 8u * (i / 3)) * ROWB + 128u * (i % 3);
      __builtin_amdgcn_raw_buffer_store_b128(acc, srdO, src, so, AUXO);
      asm volatile("s_waitcnt vmcnt(" STR(INFL) ")" ::: "memory");
    }
  }
  asm volatile("s_waitcnt vmcnt(0)" ::: "memory");
  unsigned long long t1 = __builtin_amdgcn_s_memrealtime();
  if (tid == 0) { ts[b * 4] = t0; ts[b * 4 + 1] = tq; ts[b * 4 + 2] = t1; }
}

extern "C" void kernel_launch(void* const* d_in, const int* in_sizes, int n_in, void* d_out, int out_size, void* d_ws, size_t ws_size, hipStream_t stream) {
  hipLaunchKernelGGL(memfloor, dim3(256), dim3(256), 0, stream, (const unsigned short*)d_in[0], (const unsigned short*)d_in[1], (const unsigned short*)d_in[2], (unsigned short*)d_out, (unsigned long long*)d_ws);
  hipLaunchKernelGGL(attn_v0, dim3(512), dim3(512), 0, stream, (const unsigned short*)d_in[0], (const unsigned short*)d_in[1], (const unsigned short*)d_in[2], (unsigned short*)d_out);
}
```

```cpp
#include <hip/hip_runtime.h>

__global__ __launch_bounds__(256, 1)
void attn_fwd(const void* __restrict__ Q, const void* __restrict__ K, const void* __restrict__ V, void* __restrict__ O, void* __restrict__ W) {
  __shared__ __attribute__((aligned(16))) char lds[163840];
  const unsigned wid = __builtin_amdgcn_readfirstlane(threadIdx.x >> 6);
  const unsigned bid = blockIdx.x;
  const unsigned ldsb = (unsigned)(size_t)lds;
  asm volatile(
    "s_load_dwordx8 s[4:11], %0, 0x0\n"
    "v_mbcnt_lo_u32_b32 v70, -1, 0\n"
    "v_mbcnt_hi_u32_b32 v70, -1, v70\n"
    "s_getpc_b64 s[84:85]\n"
    "s_lshr_b32 s70, %1, 3\n"
    "s_lshl_b32 s70, s70, 2\n"
    "s_add_u32 s70, s70, %2\n"
    "s_mul_i32 s70, s70, 288\n"
    "s_add_u32 s84, s84, s70\n"
    "s_addc_u32 s85, s85, 0\n"
    "s_mov_b32 s86, 288\n"
    "s_mov_b32 s87, 0x20000\n"
    "v_lshlrev_b32 v129, 7, v70\n"
    "buffer_load_dword v128, v129, s[84:87], 0 offen\n"
    "s_lshr_b32 s70, %1, 4\n"
    "s_lshl_b32 s70, s70, 3\n"
    "s_and_b32 s71, %1, 7\n"
    "s_or_b32 s70, s70, s71\n"
    "s_bfe_u32 s71, %1, 0x10003\n"
    "s_lshl_b32 s72, %2, 6\n"
    "s_lshl_b32 s73, s71, 8\n"
    "s_add_u32 s73, s73, s72\n"
    "s_mul_i32 s39, s70, 992\n"
    "s_add_u32 s38, s39, s73\n"
    "s_mul_i32 s38, s38, 384\n"
    "s_sub_u32 s30, 496, s73\n"
    "s_mul_i32 s30, s30, 384\n"
    "s_mov_b32 s31, 0x20000\n"
    "v_lshrrev_b32 v71, 3, v70\n"
    "v_mul_u32_u24 v72, 0x180, v71\n"
    "v_and_b32 v73, 7, v70\n"
    "v_lshrrev_b32 v74, 4, v70\n"
    "v_xor_b32 v74, v73, v74\n"
    "v_lshl_add_u32 v228, v74, 4, v72\n"
    "v_xor_b32 v74, 4, v74\n"
    "v_lshl_add_u32 v229, v74, 4, v72\n"
    "v_add_u32 v229, 0x800, v229\n"
    "s_lshl_b32 s75, %2, 14\n"
    "s_add_u32 s75, s75, 0x18000\n"
    "s_add_u32 s48, s75, %3\n"
    "s_movk_i32 s42, 0x1800\n"
    "s_movk_i32 s43, 0x3000\n"
    "s_movk_i32 s44, 0x4800\n"
    "s_mul_i32 s45, s70, 393216\n"
    "s_mov_b32 s22, 393216\n"
    "s_mov_b32 s23, 0x20000\n"
    "s_mul_i32 s36, %2, 6144\n"
    "s_lshl_b32 s52, %2, 11\n"
    "s_add_u32 s52, s52, %3\n"
    "s_add_u32 s53, s52, 8064\n"
    "s_add_u32 s54, s52, 16128\n"
    "s_waitcnt lgkmcnt(0)\n"
    "s_mov_b64 s[28:29], s[4:5]\n"
    "s_add_u32 s28, s28, s38\n"
    "s_addc_u32 s29, s29, 0\n"
    "s_mov_b64 s[20:21], s[6:7]\n"
    "s_add_u32 s20, s20, s45\n"
    "s_addc_u32 s21, s21, 0\n"
    "s_and_b32 s76, %1, 7\n"
    "s_lshl_b32 s76, s76, 2\n"
    "s_lshr_b32 s76, 0x04040404, s76\n"
    "s_and_b32 s76, s76, 15\n"
    "LSG0_%=:\n"
    "s_sub_u32 s76, s76, 1\n"
    "s_cbranch_scc1 LSG1_%=\n"
    "s_sleep 8\n"
    "s_branch LSG0_%=\n"
    "LSG1_%=:\n"
    "s_add_u32 s71, s36, 24576\n"
    "s_add_u32 s19, s36, %3\n"
    "s_add_u32 s19, s19, 73728\n"
    "s_mov_b32 m0, s52\n"
    "s_nop 0\n"
    "buffer_load_dwordx4 v228, s[20:23], s36 offen offset:0 nt lds\n"
    "buffer_load_dwordx4 v229, s[20:23], s36 offen offset:1024 nt lds\n"
    "s_mov_b32 m0, s53\n"
    "s_nop 0\n"
    "buffer_load_dwordx4 v228, s[20:23], s36 offen offset:128 nt lds\n"
    "buffer_load_dwordx4 v229, s[20:23], s36 offen offset:1152 nt lds\n"
    "s_mov_b32 m0, s54\n"
    "s_nop 0\n"
    "buffer_load_dwordx4 v228, s[20:23], s36 offen offset:256 nt lds\n"
    "buffer_load_dwordx4 v229, s[20:23], s36 offen offset:1280 nt lds\n"
    "s_add_u32 m0, s48, 0\n"
    "s_nop 0\n"
    "buffer_load_dwordx4 v228, s[28:31], 0 offen offset:0 nt lds\n"
    "buffer_load_dwordx4 v229, s[28:31], 0 offen offset:1024 nt lds\n"
    "s_add_u32 m0, s48, 1920\n"
    "s_nop 0\n"
    "buffer_load_dwordx4 v228, s[28:31], 0 offen offset:128 nt lds\n"
    "buffer_load_dwordx4 v229, s[28:31], 0 offen offset:1152 nt lds\n"
    "s_add_u32 m0, s48, 3840\n"
    "s_nop 0\n"
    "buffer_load_dwordx4 v228, s[28:31], 0 offen offset:256 nt lds\n"
    "buffer_load_dwordx4 v229, s[28:31], 0 offen offset:1280 nt lds\n"
    "s_add_u32 m0, s52, 24576\n"
    "s_nop 0\n"
    "buffer_load_dwordx4 v228, s[20:23], s71 offen offset:0 nt lds\n"
    "buffer_load_dwordx4 v229, s[20:23], s71 offen offset:1024 nt lds\n"
    "s_add_u32 m0, s53, 24576\n"
    "s_nop 0\n"
    "buffer_load_dwordx4 v228, s[20:23], s71 offen offset:128 nt lds\n"
    "buffer_load_dwordx4 v229, s[20:23], s71 offen offset:1152 nt lds\n"
    "s_add_u32 m0, s54, 24576\n"
    "s_nop 0\n"
    "buffer_load_dwordx4 v228, s[20:23], s71 offen offset:256 nt lds\n"
    "buffer_load_dwordx4 v229, s[20:23], s71 offen offset:1280 nt lds\n"
    "s_add_u32 s36, s36, 49152\n"
    "s_mov_b64 s[24:25], s[8:9]\n"
    "s_add_u32 s24, s24, s45\n"
    "s_addc_u32 s25, s25, 0\n"
    "s_mov_b32 s26, 393216\n"
    "s_mov_b32 s27, 0x20000\n"
    "s_mov_b64 s[80:81], s[10:11]\n"
    "s_add_u32 s80, s80, s38\n"
    "s_addc_u32 s81, s81, 0\n"
    "s_mov_b32 s82, s30\n"
    "s_mov_b32 s83, 0x20000\n"
    "s_xor_b32 s73, s73, 0x100\n"
    "s_add_u32 s38, s39, s73\n"
    "s_add_u32 s38, s38, 496\n"
    "s_mul_i32 s38, s38, 384\n"
    "s_mov_b64 s[76:77], s[4:5]\n"
    "s_add_u32 s67, s76, s38\n"
    "s_addc_u32 s68, s77, 0\n"
    "s_sub_u32 s69, 496, s73\n"
    "s_mul_i32 s69, s69, 384\n"
    "s_mov_b64 s[76:77], s[10:11]\n"
    "s_add_u32 s64, s76, s38\n"
    "s_addc_u32 s65, s77, 0\n"
    "s_mov_b32 s66, s69\n"
    "s_mul_i32 s37, %2, 6144\n"
    "s_add_u32 s58, s37, %3\n"
    "s_add_u32 s58, s58, 0xc000\n"
    "s_add_u32 s59, s58, 896\n"
    "s_add_u32 s60, s58, 1792\n"
    "s_add_u32 s55, s52, 24576\n"
    "s_add_u32 s61, s58, 24576\n"
    "s_add_u32 s56, s53, 24576\n"
    "s_add_u32 s62, s59, 24576\n"
    "s_add_u32 s57, s54, 24576\n"
    "s_add_u32 s63, s60, 24576\n"
    "s_mov_b32 s40, 0x3dd53b95\n"
    "s_mov_b32 s47, 0xbdd53b95\n"
    "s_mov_b32 s41, 0x53800000\n"
    "s_mov_b64 s[50:51], 0\n"
    "s_mov_b32 s46, 0\n"
    "s_mov_b32 s49, s48\n"
    "s_mov_b64 s[84:85], s[10:11]\n"
    "s_mov_b32 s86, 0\n"
    "s_mov_b32 s87, 0x20000\n"
    "s_mov_b32 s16, -1\n"
    "v_xor_b32 v74, v73, v71\n"
    "v_lshl_add_u32 v250, v74, 4, v72\n"
    "v_and_b32 v71, 15, v70\n"
    "v_lshlrev_b32 v71, 7, v71\n"
    "v_bfe_u32 v72, v70, 1, 3\n"
    "v_lshrrev_b32 v74, 4, v70\n"
    "v_xor_b32 v72, v72, v74\n"
    "v_xor_b32 v74, 0, v72\n"
    "v_lshl_add_u32 v74, v74, 4, v71\n"
    "v_add_u32 v224, %3, v74\n"
    "v_add_u32 v253, s48, v74\n"
    "v_xor_b32 v74, 4, v72\n"
    "v_lshl_add_u32 v74, v74, 4, v71\n"
    "v_add_u32 v225, %3, v74\n"
    "v_add_u32 v254, s48, v74\n"
    "v_lshrrev_b32 v72, 5, v70\n"
    "v_xor_b32 v74, v73, v72\n"
    "v_lshl_add_u32 v71, v74, 4, v71\n"
    "v_bfe_u32 v74, v70, 4, 1\n"
    "v_lshl_add_u32 v71, v74, 3, v71\n"
    "v_add_u32 v248, s48, v71\n"
    "v_bfe_u32 v71, v70, 2, 3\n"
    "v_mul_u32_u24 v230, 0x180, v71\n"
    "v_lshl_add_u32 v230, v72, 6, v230\n"
    "v_and_b32 v73, 3, v70\n"
    "v_lshlrev_b32 v231, 1, v74\n"
    "v_xor_b32 v231, v231, v73\n"
    "v_lshl_add_u32 v230, v231, 4, v230\n"
    "v_mul_u32_u24 v231, 0xc00, v72\n"
    "v_lshl_add_u32 v231, v71, 6, v231\n"
    "v_lshl_add_u32 v231, v74, 5, v231\n"
    "v_lshl_add_u32 v231, v73, 3, v231\n"
    "s_add_u32 s70, %3, 0xc000\n"
    "v_add_u32 v226, s70, v231\n"
    "v_xor_b32 v231, 32, v231\n"
    "v_add_u32 v227, s70, v231\n"
    "v_lshlrev_b32 v74, 4, v70\n"
    "v_add_u32 v249, s48, v74\n"
    "v_and_b32 v74, 3, v70\n"
    "v_cmp_eq_u32 vcc, 0, v74\n"
    "v_mov_b32 v74, 0x3f803f80\n"
    "v_cndmask_b32 v240, 0, v74, vcc\n"
    "v_cndmask_b32 v241, 0, v74, vcc\n"
    "v_cndmask_b32 v242, 0, v74, vcc\n"
    "v_cndmask_b32 v243, 0, v74, vcc\n"
    "s_cmp_lt_u32 s82, 24576\n"
    "s_cselect_b32 s14, 0xf149f2ca, 0\n"
    "s_cmp_lt_u32 s66, 24576\n"
    "s_cselect_b32 s15, 0xf149f2ca, 0\n"
    "s_sub_u32 s70, s19, s48\n"
    "v_add_u32 v246, s70, v253\n"
    "v_add_u32 v247, s70, v254\n"
    "s_waitcnt vmcnt(16)\n"
    "s_add_u32 m0, s48, 6144\n"
    "s_nop 0\n"
    "buffer_load_dwordx4 v228, s[28:31], s42 offen offset:0 nt lds\n"
    "buffer_load_dwordx4 v229, s[28:31], s42 offen offset:1024 nt lds\n"
    "s_add_u32 m0, s48, 8064\n"
    "s_nop 0\n"
    "buffer_load_dwordx4 v228, s[28:31], s42 offen offset:128 nt lds\n"
    "buffer_load_dwordx4 v229, s[28:31], s42 offen offset:1152 nt lds\n"
    "s_add_u32 m0, s48, 9984\n"
    "s_nop 0\n"
    "buffer_load_dwordx4 v228, s[28:31], s42 offen offset:256 nt lds\n"
    "buffer_load_dwordx4 v229, s[28:31], s42 offen offset:1280 nt lds\n"
    "s_waitcnt vmcnt(12)\n"
    "s_barrier\n"
    "ds_read_b128 a[96:99], v224 offset:0\n"
    "ds_read_b128 a[0:3], v253 offset:0\n"
    "ds_read_b128 a[100:103], v225 offset:0\n"
    "ds_read_b128 a[4:7], v254 offset:0\n"
    "ds_read_b128 a[104:107], v224 offset:8192\n"
    "ds_read_b128 a[8:11], v253 offset:2048\n"
    "ds_read_b128 a[108:111], v225 offset:8192\n"
    "ds_read_b128 a[12:15], v254 offset:2048\n"
    "ds_read_b128 a[112:115], v224 offset:16384\n"
    "ds_read_b128 a[16:19], v253 offset:4096\n"
    "ds_read_b128 a[116:119], v225 offset:16384\n"
    "ds_read_b128 a[20:23], v254 offset:4096\n"
    "ds_read_b128 a[120:123], v224 offset:2048\n"
    "ds_read_b128 a[124:127], v225 offset:2048\n"
    "ds_read_b128 a[128:131], v224 offset:10240\n"
    "ds_read_b128 a[132:135], v225 offset:10240\n"
    "s_waitcnt lgkmcnt(4)\n"
    "v_mfma_f32_16x16x32_bf16 v[0:3], a[96:99], a[0:3], 0\n"
    "ds_read_b128 a[136:139], v224 offset:18432\n"
    "ds_read_b128 a[140:143], v225 offset:18432\n"
    "v_mfma_f32_16x16x32_bf16 v[0:3], a[100:103], a[4:7], v[0:3]\n"
    "ds_read_b128 a[144:147], v224 offset:4096\n"
    "ds_read_b128 a[148:151], v225 offset:4096\n"
    "v_mfma_f32_16x16x32_bf16 v[0:3], a[104:107], a[8:11], v[0:3]\n"
    "ds_read_b128 a[152:155], v224 offset:12288\n"
    "ds_read_b128 a[156:159], v225 offset:12288\n"
    "v_mfma_f32_16x16x32_bf16 v[0:3], a[108:111], a[12:15], v[0:3]\n"
    "ds_read_b128 a[160:163], v224 offset:20480\n"
    "ds_read_b128 a[164:167], v225 offset:20480\n"
    "v_mfma_f32_16x16x32_bf16 v[0:3], a[112:115], a[16:19], v[0:3]\n"
    "ds_read_b128 a[168:171], v224 offset:6144\n"
    "ds_read_b128 a[172:175], v225 offset:6144\n"
    "v_mfma_f32_16x16x32_bf16 v[0:3], a[116:119], a[20:23], v[0:3]\n"
    "ds_read_b128 a[176:179], v224 offset:14336\n"
    "ds_read_b128 a[180:183], v225 offset:14336\n"
    "s_waitcnt lgkmcnt(10)\n"
    "v_mfma_f32_16x16x32_bf16 v[4:7], a[120:123], a[0:3], 0\n"
    "ds_read_b128 a[184:187], v224 offset:22528\n"
    "ds_read_b128 a[188:191], v225 offset:22528\n"
    "v_mfma_f32_16x16x32_bf16 v[4:7], a[124:127], a[4:7], v[4:7]\n"
    "v_mfma_f32_16x16x32_bf16 v[4:7], a[128:131], a[8:11], v[4:7]\n"
    "s_waitcnt vmcnt(6)\n"
    "s_barrier\n"
    "v_max3_f32 v236, v0, v1, v2\n"
    "v_mfma_f32_16x16x32_bf16 v[4:7], a[132:135], a[12:15], v[4:7]\n"
    "ds_read_b128 v[128:131], v224 offset:24576\n"
    "ds_read_b128 v[132:135], v225 offset:24576\n"
    "v_mfma_f32_16x16x32_bf16 v[4:7], a[136:139], a[16:19], v[4:7]\n"
    "ds_read_b128 v[136:139], v224 offset:32768\n"
    "ds_read_b128 v[140:143], v225 offset:32768\n"
    "s_add_u32 m0, s19, 0\n"
    "v_mfma_f32_16x16x32_bf16 v[4:7], a[140:143], a[20:23], v[4:7]\n"
    "s_waitcnt lgkmcnt(10)\n"
    "ds_read_b128 v[144:147], v224 offset:40960\n"
    "ds_read_b128 v[148:151], v225 offset:40960\n"
    "v_mfma_f32_16x16x32_bf16 v[8:11], a[144:147], a[0:3], 0\n"
    "ds_read_b128 v[152:155], v224 offset:26624\n"
    "ds_read_b128 v[156:159], v225 offset:26624\n"
    "v_mfma_f32_16x16x32_bf16 v[8:11], a[148:151], a[4:7], v[8:11]\n"
    "ds_read_b128 v[160:163], v224 offset:34816\n"
    "ds_read_b128 v[164:167], v225 offset:34816\n"
    "v_max3_f32 v236, v236, v3, v4\n"
    "v_max3_f32 v236, v236, v5, v6\n"
    "v_mfma_f32_16x16x32_bf16 v[8:11], a[152:155], a[8:11], v[8:11]\n"
    "ds_read_b128 v[168:171], v224 offset:43008\n"
    "ds_read_b128 v[172:175], v225 offset:43008\n"
    "v_mfma_f32_16x16x32_bf16 v[8:11], a[156:159], a[12:15], v[8:11]\n"
    "ds_read_b128 v[176:179], v224 offset:28672\n"
    "buffer_load_dwordx4 v228, s[28:31], s43 offen offset:0 nt lds\n"
    "v_mfma_f32_16x16x32_bf16 v[8:11], a[160:163], a[16:19], v[8:11]\n"
    "ds_read_b128 v[180:183], v225 offset:28672\n"
    "v_mfma_f32_16x16x32_bf16 v[8:11], a[164:167], a[20:23], v[8:11]\n"
    "s_waitcnt lgkmcnt(14)\n"
    "ds_read_b128 v[184:187], v224 offset:36864\n"
    "v_mfma_f32_16x16x32_bf16 v[12:15], a[168:171], a[0:3], 0\n"
    "ds_read_b128 v[188:191], v225 offset:36864\n"
    "v_mfma_f32_16x16x32_bf16 v[12:15], a[172:175], a[4:7], v[12:15]\n"
    "ds_read_b128 v[192:195], v224 offset:45056\n"
    "buffer_load_dwordx4 v229, s[28:31], s43 offen offset:1024 nt lds\n"
    "v_mfma_f32_16x16x32_bf16 v[12:15], a[176:179], a[8:11], v[12:15]\n"
    "ds_read_b128 v[196:199], v225 offset:45056\n"
    "v_max3_f32 v236, v236, v7, v8\n"
    "v_max3_f32 v236, v236, v9, v10\n"
    "v_mfma_f32_16x16x32_bf16 v[12:15], a[180:183], a[12:15], v[12:15]\n"
    "ds_read_b128 v[200:203], v224 offset:30720\n"
    "s_add_u32 m0, s19, 1920\n"
    "v_mfma_f32_16x16x32_bf16 v[12:15], a[184:187], a[16:19], v[12:15]\n"
    "ds_read_b128 v[204:207], v225 offset:30720\n"
    "v_mfma_f32_16x16x32_bf16 v[12:15], a[188:191], a[20:23], v[12:15]\n"
    "ds_read_b128 v[208:211], v224 offset:38912\n"
    "s_waitcnt lgkmcnt(15)\n"
    "v_mfma_f32_16x16x32_bf16 v[64:67], v[128:131], a[0:3], 0\n"
    "ds_read_b128 v[212:215], v225 offset:38912\n"
    "buffer_load_dwordx4 v228, s[28:31], s43 offen offset:128 nt lds\n"
    "v_mfma_f32_16x16x32_bf16 v[64:67], v[132:135], a[4:7], v[64:67]\n"
    "ds_read_b128 v[216:219], v224 offset:47104\n"
    "v_mfma_f32_16x16x32_bf16 v[64:67], v[136:139], a[8:11], v[64:67]\n"
    "ds_read_b128 v[220:223], v225 offset:47104\n"
    "v_max3_f32 v236, v236, v11, v12\n"
    "v_max3_f32 v236, v236, v13, v14\n"
    "v_max_f32 v236, v236, v15\n"
    "v_mov_b32 v231, v236\n"
    "v_mfma_f32_16x16x32_bf16 v[64:67], v[140:143], a[12:15], v[64:67]\n"
    "v_mfma_f32_16x16x32_bf16 v[64:67], v[144:147], a[16:19], v[64:67]\n"
    "buffer_load_dwordx4 v229, s[28:31], s43 offen offset:1152 nt lds\n"
    "v_permlane16_swap_b32 v236, v231\n"
    "v_max_f32 v236, v236, v231\n"
    "v_mov_b32 v231, v236\n"
    "v_mfma_f32_16x16x32_bf16 v[64:67], v[148:151], a[20:23], v[64:67]\n"
    "s_waitcnt lgkmcnt(12)\n"
    "v_mfma_f32_16x16x32_bf16 v[68:71], v[152:155], a[0:3], 0\n"
    "s_add_u32 m0, s19, 3840\n"
    "v_permlane32_swap_b32 v236, v231\n"
    "v_max_f32 v236, v236, v231\n"
    "v_mul_f32 v232, s47, v236\n"
    "v_fma_f32 v0, v0, s40, v232\n"
    "v_exp_f32 v0, v0\n"
    "v_fma_f32 v1, v1, s40, v232\n"
    "v_mfma_f32_16x16x32_bf16 v[68:71], v[156:159], a[4:7], v[68:71]\n"
    "v_exp_f32 v1, v1\n"
    "v_fma_f32 v2, v2, s40, v232\n"
    "v_mfma_f32_16x16x32_bf16 v[68:71], v[160:163], a[8:11], v[68:71]\n"
    "buffer_load_dwordx4 v228, s[28:31], s43 offen offset:256 nt lds\n"
    "v_fma_f32 v3, v3, s40, v232\n"
    "v_mfma_f32_16x16x32_bf16 v[68:71], v[164:167], a[12:15], v[68:71]\n"
    "v_exp_f32 v2, v2\n"
    "v_fma_f32 v4, v4, s40, v232\n"
    "v_mfma_f32_16x16x32_bf16 v[68:71], v[168:171], a[16:19], v[68:71]\n"
    "v_exp_f32 v3, v3\n"
    "v_fma_f32 v5, v5, s40, v232\n"
    "v_mfma_f32_16x16x32_bf16 v[68:71], v[172:175], a[20:23], v[68:71]\n"
    "s_waitcnt lgkmcnt(6)\n"
    "v_exp_f32 v4, v4\n"
    "v_mfma_f32_16x16x32_bf16 v[72:75], v[176:179], a[0:3], 0\n"
    "buffer_load_dwordx4 v229, s[28:31], s43 offen offset:1280 nt lds\n"
    "v_exp_f32 v5, v5\n"
    "v_mfma_f32_16x16x32_bf16 v[72:75], v[180:183], a[4:7], v[72:75]\n"
    "v_fma_f32 v6, v6, s40, v232\n"
    "v_exp_f32 v6, v6\n"
    "v_mfma_f32_16x16x32_bf16 v[72:75], v[184:187], a[8:11], v[72:75]\n"
    "v_fma_f32 v7, v7, s40, v232\n"
    "v_exp_f32 v7, v7\n"
    "v_mfma_f32_16x16x32_bf16 v[72:75], v[188:191], a[12:15], v[72:75]\n"
    "s_waitcnt vmcnt(6)\n"
    "ds_read_b128 a[24:27], v253 offset:6144\n"
    "ds_read_b128 a[28:31], v254 offset:6144\n"
    "v_fma_f32 v8, v8, s40, v232\n"
    "v_mfma_f32_16x16x32_bf16 v[72:75], v[192:195], a[16:19], v[72:75]\n"
    "ds_read_b128 a[32:35], v253 offset:8192\n"
    "ds_read_b128 a[36:39], v254 offset:8192\n"
    "v_fma_f32 v9, v9, s40, v232\n"
    "v_mfma_f32_16x16x32_bf16 v[72:75], v[196:199], a[20:23], v[72:75]\n"
    "s_waitcnt lgkmcnt(5)\n"
    "ds_read_b128 a[40:43], v253 offset:10240\n"
    "ds_read_b128 a[44:47], v254 offset:10240\n"
    "v_cvt_pk_bf16_f32 v0, v0, v1\n"
    "v_mfma_f32_16x16x32_bf16 v[76:79], v[200:203], a[0:3], 0\n"
    "v_exp_f32 v8, v8\n"
    "v_exp_f32 v9, v9\n"
    "v_mfma_f32_16x16x32_bf16 v[76:79], v[204:207], a[4:7], v[76:79]\n"
    "v_fma_f32 v10, v10, s40, v232\n"
    "v_exp_f32 v10, v10\n"
    "v_mfma_f32_16x16x32_bf16 v[76:79], v[208:211], a[8:11], v[76:79]\n"
    "v_fma_f32 v11, v11, s40, v232\n"
    "v_exp_f32 v11, v11\n"
    "v_mfma_f32_16x16x32_bf16 v[76:79], v[212:215], a[12:15], v[76:79]\n"
    "v_cvt_pk_bf16_f32 v1, v2, v3\n"
    "v_fma_f32 v12, v12, s40, v232\n"
    "v_fma_f32 v13, v13, s40, v232\n"
    "v_mfma_f32_16x16x32_bf16 v[76:79], v[216:219], a[16:19], v[76:79]\n"
    "s_waitcnt lgkmcnt(6)\n"
    "s_barrier\n"
    "v_cvt_pk_bf16_f32 v2, v4, v5\n"
    "v_mfma_f32_16x16x32_bf16 v[76:79], v[220:223], a[20:23], v[76:79]\n"
    "v_exp_f32 v12, v12\n"
    "v_fma_f32 v14, v14, s40, v232\n"
    "s_waitcnt lgkmcnt(0)\n"
    "v_mfma_f32_16x16x32_bf16 v[16:19], a[96:99], a[24:27], 0\n"
    "v_exp_f32 v13, v13\n"
    "v_fma_f32 v15, v15, s40, v232\n"
    "v_mfma_f32_16x16x32_bf16 v[16:19], a[100:103], a[28:31], v[16:19]\n"
    "v_exp_f32 v14, v14\n"
    "v_exp_f32 v15, v15\n"
    "v_mfma_f32_16x16x32_bf16 v[16:19], a[104:107], a[32:35], v[16:19]\n"
    "v_cvt_pk_bf16_f32 v3, v6, v7\n"
    "v_cvt_pk_bf16_f32 v8, v8, v9\n"
    "v_cvt_pk_bf16_f32 v9, v10, v11\n"
    "v_mfma_f32_16x16x32_bf16 v[16:19], a[108:111], a[36:39], v[16:19]\n"
    "v_cvt_pk_bf16_f32 v10, v12, v13\n"
    "v_cvt_pk_bf16_f32 v11, v14, v15\n"
    "v_fma_f32 v64, v64, s40, v232\n"
    "v_mfma_f32_16x16x32_bf16 v[16:19], a[112:115], a[40:43], v[16:19]\n"
    "v_exp_f32 v64, v64\n"
    "v_fma_f32 v65, v65, s40, v232\n"
    "v_mfma_f32_16x16x32_bf16 v[16:19], a[116:119], a[44:47], v[16:19]\n"
    "v_exp_f32 v65, v65\n"
    "v_fma_f32 v66, v66, s40, v232\n"
    "v_mfma_f32_16x16x32_bf16 v[20:23], a[120:123], a[24:27], 0\n"
    "v_exp_f32 v66, v66\n"
    "v_fma_f32 v67, v67, s40, v232\n"
    "v_mfma_f32_16x16x32_bf16 v[20:23], a[124:127], a[28:31], v[20:23]\n"
    "v_exp_f32 v67, v67\n"
    "v_fma_f32 v68, v68, s40, v232\n"
    "v_fma_f32 v69, v69, s40, v232\n"
    "v_mfma_f32_16x16x32_bf16 v[20:23], a[128:131], a[32:35], v[20:23]\n"
    "v_max3_f32 v237, v16, v17, v18\n"
    "v_exp_f32 v68, v68\n"
    "v_mfma_f32_16x16x32_bf16 v[20:23], a[132:135], a[36:39], v[20:23]\n"
    "v_exp_f32 v69, v69\n"
    "v_fma_f32 v70, v70, s40, v232\n"
    "v_mfma_f32_16x16x32_bf16 v[20:23], a[136:139], a[40:43], v[20:23]\n"
    "s_add_u32 m0, s48, 0\n"
    "v_exp_f32 v70, v70\n"
    "v_mfma_f32_16x16x32_bf16 v[20:23], a[140:143], a[44:47], v[20:23]\n"
    "v_fma_f32 v71, v71, s40, v232\n"
    "v_exp_f32 v71, v71\n"
    "v_mfma_f32_16x16x32_bf16 v[24:27], a[144:147], a[24:27], 0\n"
    "buffer_load_dwordx4 v228, s[28:31], s44 offen offset:0 nt lds\n"
    "v_fma_f32 v72, v72, s40, v232\n"
    "v_fma_f32 v73, v73, s40, v232\n"
    "v_mfma_f32_16x16x32_bf16 v[24:27], a[148:151], a[28:31], v[24:27]\n"
    "v_exp_f32 v72, v72\n"
    "v_cvt_pk_bf16_f32 v64, v64, v65\n"
    "v_mfma_f32_16x16x32_bf16 v[24:27], a[152:155], a[32:35], v[24:27]\n"
    "v_max3_f32 v237, v237, v19, v20\n"
    "v_max3_f32 v237, v237, v21, v22\n"
    "v_fma_f32 v74, v74, s40, v232\n"
    "v_mfma_f32_16x16x32_bf16 v[24:27], a[156:159], a[36:39], v[24:27]\n"
    "v_exp_f32 v73, v73\n"
    "v_fma_f32 v75, v75, s40, v232\n"
    "v_mfma_f32_16x16x32_bf16 v[24:27], a[160:163], a[40:43], v[24:27]\n"
    "buffer_load_dwordx4 v229, s[28:31], s44 offen offset:1024 nt lds\n"
    "v_exp_f32 v74, v74\n"
    "v_mfma_f32_16x16x32_bf16 v[24:27], a[164:167], a[44:47], v[24:27]\n"
    "v_exp_f32 v75, v75\n"
    "v_cvt_pk_bf16_f32 v65, v66, v67\n"
    "v_mfma_f32_16x16x32_bf16 v[28:31], a[168:171], a[24:27], 0\n"
    "s_add_u32 m0, s48, 1920\n"
    "v_fma_f32 v76, v76, s40, v232\n"
    "v_exp_f32 v76, v76\n"
    "v_mfma_f32_16x16x32_bf16 v[28:31], a[172:175], a[28:31], v[28:31]\n"
    "v_fma_f32 v77, v77, s40, v232\n"
    "v_exp_f32 v77, v77\n"
    "v_mfma_f32_16x16x32_bf16 v[28:31], a[176:179], a[32:35], v[28:31]\n"
    "buffer_load_dwordx4 v228, s[28:31], s44 offen offset:128 nt lds\n"
    "v_max3_f32 v237, v237, v23, v24\n"
    "v_max3_f32 v237, v237, v25, v26\n"
    "v_cvt_pk_bf16_f32 v66, v68, v69\n"
    "v_fma_f32 v78, v78, s40, v232\n"
    "v_mfma_f32_16x16x32_bf16 v[28:31], a[180:183], a[36:39], v[28:31]\n"
    "v_exp_f32 v78, v78\n"
    "v_fma_f32 v79, v79, s40, v232\n"
    "v_mfma_f32_16x16x32_bf16 v[28:31], a[184:187], a[40:43], v[28:31]\n"
    "v_exp_f32 v79, v79\n"
    "v_cvt_pk_bf16_f32 v67, v70, v71\n"
    "v_cvt_pk_bf16_f32 v72, v72, v73\n"
    "v_mfma_f32_16x16x32_bf16 v[28:31], a[188:191], a[44:47], v[28:31]\n"
    "v_cvt_pk_bf16_f32 v73, v74, v75\n"
    "v_cvt_pk_bf16_f32 v74, v76, v77\n"
    "v_cvt_pk_bf16_f32 v75, v78, v79\n"
    "v_mfma_f32_16x16x32_bf16 v[80:83], v[128:131], a[24:27], 0\n"
    "buffer_load_dwordx4 v229, s[28:31], s44 offen offset:1152 nt lds\n"
    "v_mfma_f32_16x16x32_bf16 v[80:83], v[132:135], a[28:31], v[80:83]\n"
    "v_mfma_f32_16x16x32_bf16 v[80:83], v[136:139], a[32:35], v[80:83]\n"
    "s_add_u32 m0, s48, 3840\n"
    "v_max3_f32 v237, v237, v27, v28\n"
    "v_max3_f32 v237, v237, v29, v30\n"
    "v_max_f32 v237, v237, v31\n"
    "v_mov_b32 v231, v237\n"
    "v_mfma_f32_16x16x32_bf16 v[80:83], v[140:143], a[36:39], v[80:83]\n"
    "v_mfma_f32_16x16x32_bf16 v[80:83], v[144:147], a[40:43], v[80:83]\n"
    "buffer_load_dwordx4 v228, s[28:31], s44 offen offset:256 nt lds\n"
    "v_permlane16_swap_b32 v237, v231\n"
    "v_max_f32 v237, v237, v231\n"
    "v_mov_b32 v231, v237\n"
    "v_mfma_f32_16x16x32_bf16 v[80:83], v[148:151], a[44:47], v[80:83]\n"
    "v_mfma_f32_16x16x32_bf16 v[84:87], v[152:155], a[24:27], 0\n"
    "v_permlane32_swap_b32 v237, v231\n"
    "v_max_f32 v237, v237, v231\n"
    "v_mul_f32 v233, s47, v237\n"
    "v_fma_f32 v16, v16, s40, v233\n"
    "v_exp_f32 v16, v16\n"
    "v_fma_f32 v17, v17, s40, v233\n"
    "v_mfma_f32_16x16x32_bf16 v[84:87], v[156:159], a[28:31], v[84:87]\n"
    "v_exp_f32 v17, v17\n"
    "v_fma_f32 v18, v18, s40, v233\n"
    "v_mfma_f32_16x16x32_bf16 v[84:87], v[160:163], a[32:35], v[84:87]\n"
    "buffer_load_dwordx4 v229, s[28:31], s44 offen offset:1280 nt lds\n"
    "v_fma_f32 v19, v19, s40, v233\n"
    "v_mfma_f32_16x16x32_bf16 v[84:87], v[164:167], a[36:39], v[84:87]\n"
    "s_mov_b32 s28, s67\n"
    "v_exp_f32 v18, v18\n"
    "v_fma_f32 v20, v20, s40, v233\n"
    "v_mfma_f32_16x16x32_bf16 v[84:87], v[168:171], a[40:43], v[84:87]\n"
    "s_mov_b32 s29, s68\n"
    "v_exp_f32 v19, v19\n"
    "v_mfma_f32_16x16x32_bf16 v[84:87], v[172:175], a[44:47], v[84:87]\n"
    "s_mov_b32 s30, s69\n"
    "v_exp_f32 v20, v20\n"
    "v_mfma_f32_16x16x32_bf16 v[88:91], v[176:179], a[24:27], 0\n"
    "v_fma_f32 v21, v21, s40, v233\n"
    "v_exp_f32 v21, v21\n"
    "v_fma_f32 v22, v22, s40, v233\n"
    "v_mfma_f32_16x16x32_bf16 v[88:91], v[180:183], a[28:31], v[88:91]\n"
    "v_exp_f32 v22, v22\n"
    "v_fma_f32 v23, v23, s40, v233\n"
    "v_mfma_f32_16x16x32_bf16 v[88:91], v[184:187], a[32:35], v[88:91]\n"
    "v_exp_f32 v23, v23\n"
    "v_fma_f32 v24, v24, s40, v233\n"
    "v_mfma_f32_16x16x32_bf16 v[88:91], v[188:191], a[36:39], v[88:91]\n"
    "s_waitcnt vmcnt(6)\n"
    "ds_read_b128 a[48:51], v246 offset:0\n"
    "ds_read_b128 a[52:55], v247 offset:0\n"
    "v_exp_f32 v24, v24\n"
    "v_mfma_f32_16x16x32_bf16 v[88:91], v[192:195], a[40:43], v[88:91]\n"
    "ds_read_b128 a[56:59], v246 offset:2048\n"
    "ds_read_b128 a[60:63], v247 offset:2048\n"
    "v_fma_f32 v25, v25, s40, v233\n"
    "v_mfma_f32_16x16x32_bf16 v[88:91], v[196:199], a[44:47], v[88:91]\n"
    "ds_read_b128 a[64:67], v246 offset:4096\n"
    "ds_read_b128 a[68:71], v247 offset:4096\n"
    "v_cvt_pk_bf16_f32 v16, v16, v17\n"
    "v_mfma_f32_16x16x32_bf16 v[92:95], v[200:203], a[24:27], 0\n"
    "v_exp_f32 v25, v25\n"
    "v_fma_f32 v26, v26, s40, v233\n"
    "v_fma_f32 v27, v27, s40, v233\n"
    "v_mfma_f32_16x16x32_bf16 v[92:95], v[204:207], a[28:31], v[92:95]\n"
    "v_exp_f32 v26, v26\n"
    "v_cvt_pk_bf16_f32 v17, v18, v19\n"
    "v_mfma_f32_16x16x32_bf16 v[92:95], v[208:211], a[32:35], v[92:95]\n"
    "v_exp_f32 v27, v27\n"
    "v_fma_f32 v28, v28, s40, v233\n"
    "v_mfma_f32_16x16x32_bf16 v[92:95], v[212:215], a[36:39], v[92:95]\n"
    "v_exp_f32 v28, v28\n"
    "v_fma_f32 v29, v29, s40, v233\n"
    "v_cvt_pk_bf16_f32 v18, v20, v21\n"
    "v_mfma_f32_16x16x32_bf16 v[92:95], v[216:219], a[40:43], v[92:95]\n"
    "v_exp_f32 v29, v29\n"
    "v_fma_f32 v30, v30, s40, v233\n"
    "v_mfma_f32_16x16x32_bf16 v[92:95], v[220:223], a[44:47], v[92:95]\n"
    "v_exp_f32 v30, v30\n"
    "v_fma_f32 v31, v31, s40, v233\n"
    "s_waitcnt lgkmcnt(0)\n"
    "v_mfma_f32_16x16x32_bf16 v[32:35], a[96:99], a[48:51], 0\n"
    "v_exp_f32 v31, v31\n"
    "v_cvt_pk_bf16_f32 v19, v22, v23\n"
    "v_cvt_pk_bf16_f32 v24, v24, v25\n"
    "v_mfma_f32_16x16x32_bf16 v[32:35], a[100:103], a[52:55], v[32:35]\n"
    "v_cvt_pk_bf16_f32 v25, v26, v27\n"
    "v_cvt_pk_bf16_f32 v26, v28, v29\n"
    "v_cvt_pk_bf16_f32 v27, v30, v31\n"
    "v_mfma_f32_16x16x32_bf16 v[32:35], a[104:107], a[56:59], v[32:35]\n"
    "s_mov_b32 m0, s58\n"
    "v_fma_f32 v80, v80, s40, v233\n"
    "v_fma_f32 v81, v81, s40, v233\n"
    "v_mfma_f32_16x16x32_bf16 v[32:35], a[108:111], a[60:63], v[32:35]\n"
    "v_exp_f32 v80, v80\n"
    "v_exp_f32 v81, v81\n"
    "v_mfma_f32_16x16x32_bf16 v[32:35], a[112:115], a[64:67], v[32:35]\n"
    "buffer_load_dwordx4 v230, s[24:27], s37 offen offset:0 nt lds\n"
    "v_fma_f32 v82, v82, s40, v233\n"
    "v_mfma_f32_16x16x32_bf16 v[32:35], a[116:119], a[68:71], v[32:35]\n"
    "s_mov_b32 s39, s49\n"
    "v_exp_f32 v82, v82\n"
    "v_mfma_f32_16x16x32_bf16 v[36:39], a[120:123], a[48:51], 0\n"
    "s_mov_b32 s38, 0\n"
    "v_fma_f32 v83, v83, s40, v233\n"
    "v_exp_f32 v83, v83\n"
    "v_mfma_f32_16x16x32_bf16 v[36:39], a[124:127], a[52:55], v[36:39]\n"
    "s_mov_b32 s78, 0\n"
    "v_max3_f32 v238, v32, v33, v34\n"
    "v_fma_f32 v84, v84, s40, v233\n"
    "v_mfma_f32_16x16x32_bf16 v[36:39], a[128:131], a[56:59], v[36:39]\n"
    "s_mov_b32 s79, s43\n"
    "v_exp_f32 v84, v84\n"
    "v_mfma_f32_16x16x32_bf16 v[36:39], a[132:135], a[60:63], v[36:39]\n"
    "buffer_load_dwordx4 v230, s[24:27], s37 offen offset:3072 nt lds\n"
    "v_fma_f32 v85, v85, s40, v233\n"
    "v_fma_f32 v86, v86, s40, v233\n"
    "v_mfma_f32_16x16x32_bf16 v[36:39], a[136:139], a[64:67], v[36:39]\n"
    "s_add_u32 s17, s49, 6144\n"
    "v_exp_f32 v85, v85\n"
    "v_mfma_f32_16x16x32_bf16 v[36:39], a[140:143], a[68:71], v[36:39]\n"
    "s_mov_b32 s18, s44\n"
    "v_exp_f32 v86, v86\n"
    "v_mfma_f32_16x16x32_bf16 v[40:43], a[144:147], a[48:51], 0\n"
    "s_mov_b32 m0, s59\n"
    "v_fma_f32 v87, v87, s40, v233\n"
    "v_exp_f32 v87, v87\n"
    "v_mfma_f32_16x16x32_bf16 v[40:43], a[148:151], a[52:55], v[40:43]\n"
    "v_fma_f32 v88, v88, s40, v233\n"
    "v_exp_f32 v88, v88\n"
    "v_mfma_f32_16x16x32_bf16 v[40:43], a[152:155], a[56:59], v[40:43]\n"
    "buffer_load_dwordx4 v230, s[24:27], s37 offen offset:128 nt lds\n"
    "v_max3_f32 v238, v238, v35, v36\n"
    "v_max3_f32 v238, v238, v37, v38\n"
    "v_fma_f32 v89, v89, s40, v233\n"
    "v_cvt_pk_bf16_f32 v80, v80, v81\n"
    "v_mfma_f32_16x16x32_bf16 v[40:43], a[156:159], a[60:63], v[40:43]\n"
    "v_exp_f32 v89, v89\n"
    "v_fma_f32 v90, v90, s40, v233\n"
    "v_mfma_f32_16x16x32_bf16 v[40:43], a[160:163], a[64:67], v[40:43]\n"
    "v_exp_f32 v90, v90\n"
    "v_fma_f32 v91, v91, s40, v233\n"
    "v_mfma_f32_16x16x32_bf16 v[40:43], a[164:167], a[68:71], v[40:43]\n"
    "v_exp_f32 v91, v91\n"
    "v_cvt_pk_bf16_f32 v81, v82, v83\n"
    "v_fma_f32 v92, v92, s40, v233\n"
    "v_mfma_f32_16x16x32_bf16 v[44:47], a[168:171], a[48:51], 0\n"
    "v_exp_f32 v92, v92\n"
    "v_fma_f32 v93, v93, s40, v233\n"
    "v_mfma_f32_16x16x32_bf16 v[44:47], a[172:175], a[52:55], v[44:47]\n"
    "buffer_load_dwordx4 v230, s[24:27], s37 offen offset:3200 nt lds\n"
    "v_max3_f32 v238, v238, v39, v40\n"
    "v_max3_f32 v238, v238, v41, v42\n"
    "v_cvt_pk_bf16_f32 v82, v84, v85\n"
    "v_mfma_f32_16x16x32_bf16 v[44:47], a[176:179], a[56:59], v[44:47]\n"
    "v_exp_f32 v93, v93\n"
    "v_fma_f32 v94, v94, s40, v233\n"
    "v_fma_f32 v95, v95, s40, v233\n"
    "v_mfma_f32_16x16x32_bf16 v[44:47], a[180:183], a[60:63], v[44:47]\n"
    "v_exp_f32 v94, v94\n"
    "v_cvt_pk_bf16_f32 v83, v86, v87\n"
    "v_mfma_f32_16x16x32_bf16 v[44:47], a[184:187], a[64:67], v[44:47]\n"
    "s_mov_b32 m0, s60\n"
    "v_exp_f32 v95, v95\n"
    "v_cvt_pk_bf16_f32 v88, v88, v89\n"
    "v_mfma_f32_16x16x32_bf16 v[44:47], a[188:191], a[68:71], v[44:47]\n"
    "v_cvt_pk_bf16_f32 v89, v90, v91\n"
    "v_cvt_pk_bf16_f32 v90, v92, v93\n"
    "v_cvt_pk_bf16_f32 v91, v94, v95\n"
    "v_mfma_f32_16x16x32_bf16 v[96:99], v[128:131], a[48:51], 0\n"
    "buffer_load_dwordx4 v230, s[24:27], s37 offen offset:256 nt lds\n"
    "v_mfma_f32_16x16x32_bf16 v[96:99], v[132:135], a[52:55], v[96:99]\n"
    "v_mfma_f32_16x16x32_bf16 v[96:99], v[136:139], a[56:59], v[96:99]\n"
    "v_mfma_f32_16x16x32_bf16 v[96:99], v[140:143], a[60:63], v[96:99]\n"
    "v_max3_f32 v238, v238, v43, v44\n"
    "v_max3_f32 v238, v238, v45, v46\n"
    "v_max_f32 v238, v238, v47\n"
    "v_mov_b32 v231, v238\n"
    "v_mfma_f32_16x16x32_bf16 v[96:99], v[144:147], a[64:67], v[96:99]\n"
    "v_mfma_f32_16x16x32_bf16 v[96:99], v[148:151], a[68:71], v[96:99]\n"
    "buffer_load_dwordx4 v230, s[24:27], s37 offen offset:3328 nt lds\n"
    "v_permlane16_swap_b32 v238, v231\n"
    "v_max_f32 v238, v238, v231\n"
    "v_mov_b32 v231, v238\n"
    "v_mfma_f32_16x16x32_bf16 v[100:103], v[152:155], a[48:51], 0\n"
    "s_add_u32 s37, s37, 24576\n"
    "v_permlane32_swap_b32 v238, v231\n"
    "v_max_f32 v238, v238, v231\n"
    "v_mfma_f32_16x16x32_bf16 v[100:103], v[156:159], a[52:55], v[100:103]\n"
    "v_mul_f32 v234, s47, v238\n"
    "v_fma_f32 v32, v32, s40, v234\n"
    "v_exp_f32 v32, v32\n"
    "v_fma_f32 v33, v33, s40, v234\n"
    "v_fma_f32 v34, v34, s40, v234\n"
    "v_mfma_f32_16x16x32_bf16 v[100:103], v[160:163], a[56:59], v[100:103]\n"
    "v_exp_f32 v33, v33\n"
    "v_fma_f32 v35, v35, s40, v234\n"
    "v_mfma_f32_16x16x32_bf16 v[100:103], v[164:167], a[60:63], v[100:103]\n"
    "v_exp_f32 v34, v34\n"
    "v_fma_f32 v36, v36, s40, v234\n"
    "v_mfma_f32_16x16x32_bf16 v[100:103], v[168:171], a[64:67], v[100:103]\n"
    "v_exp_f32 v35, v35\n"
    "v_fma_f32 v37, v37, s40, v234\n"
    "v_mfma_f32_16x16x32_bf16 v[100:103], v[172:175], a[68:71], v[100:103]\n"
    "v_exp_f32 v36, v36\n"
    "v_fma_f32 v38, v38, s40, v234\n"
    "v_mfma_f32_16x16x32_bf16 v[104:107], v[176:179], a[48:51], 0\n"
    "v_exp_f32 v37, v37\n"
    "v_fma_f32 v39, v39, s40, v234\n"
    "v_mfma_f32_16x16x32_bf16 v[104:107], v[180:183], a[52:55], v[104:107]\n"
    "s_mov_b32 m0, s52\n"
    "v_exp_f32 v38, v38\n"
    "v_mfma_f32_16x16x32_bf16 v[104:107], v[184:187], a[56:59], v[104:107]\n"
    "v_exp_f32 v39, v39\n"
    "v_fma_f32 v40, v40, s40, v234\n"
    "v_mfma_f32_16x16x32_bf16 v[104:107], v[188:191], a[60:63], v[104:107]\n"
    "buffer_load_dwordx4 v228, s[20:23], s36 offen offset:0 nt lds\n"
    "s_waitcnt vmcnt(11)\n"
    "ds_read_b128 a[72:75], v253 offset:0\n"
    "ds_read_b128 a[76:79], v254 offset:0\n"
    "v_fma_f32 v41, v41, s40, v234\n"
    "v_mfma_f32_16x16x32_bf16 v[104:107], v[192:195], a[64:67], v[104:107]\n"
    "v_exp_f32 v40, v40\n"
    "v_cvt_pk_bf16_f32 v32, v32, v33\n"
    "v_mfma_f32_16x16x32_bf16 v[104:107], v[196:199], a[68:71], v[104:107]\n"
    "v_exp_f32 v41, v41\n"
    "v_fma_f32 v42, v42, s40, v234\n"
    "v_mfma_f32_16x16x32_bf16 v[108:111], v[200:203], a[48:51], 0\n"
    "v_exp_f32 v42, v42\n"
    "v_fma_f32 v43, v43, s40, v234\n"
    "v_cvt_pk_bf16_f32 v33, v34, v35\n"
    "v_mfma_f32_16x16x32_bf16 v[108:111], v[204:207], a[52:55], v[108:111]\n"
    "v_exp_f32 v43, v43\n"
    "v_fma_f32 v44, v44, s40, v234\n"
    "v_mfma_f32_16x16x32_bf16 v[108:111], v[208:211], a[56:59], v[108:111]\n"
    "v_exp_f32 v44, v44\n"
    "v_fma_f32 v45, v45, s40, v234\n"
    "v_mfma_f32_16x16x32_bf16 v[108:111], v[212:215], a[60:63], v[108:111]\n"
    "buffer_load_dwordx4 v229, s[20:23], s36 offen offset:1024 nt lds\n"
    "v_cvt_pk_bf16_f32 v34, v36, v37\n"
    "v_mfma_f32_16x16x32_bf16 v[108:111], v[216:219], a[64:67], v[108:111]\n"
    "v_exp_f32 v45, v45\n"
    "v_fma_f32 v46, v46, s40, v234\n"
    "v_mfma_f32_16x16x32_bf16 v[108:111], v[220:223], a[68:71], v[108:111]\n"
    "v_exp_f32 v46, v46\n"
    "v_fma_f32 v47, v47, s40, v234\n"
    "s_waitcnt lgkmcnt(0)\n"
    "v_mfma_f32_16x16x32_bf16 v[48:51], a[96:99], a[72:75], 0\n"
    "v_exp_f32 v47, v47\n"
    "v_cvt_pk_bf16_f32 v35, v38, v39\n"
    "v_mfma_f32_16x16x32_bf16 v[48:51], a[100:103], a[76:79], v[48:51]\n"
    "s_mov_b32 m0, s53\n"
    "v_cvt_pk_bf16_f32 v40, v40, v41\n"
    "v_cvt_pk_bf16_f32 v41, v42, v43\n"
    "v_mfma_f32_16x16x32_bf16 v[52:55], a[120:123], a[72:75], 0\n"
    "v_cvt_pk_bf16_f32 v42, v44, v45\n"
    "v_cvt_pk_bf16_f32 v43, v46, v47\n"
    "v_fma_f32 v96, v96, s40, v234\n"
    "v_mfma_f32_16x16x32_bf16 v[52:55], a[124:127], a[76:79], v[52:55]\n"
    "buffer_load_dwordx4 v228, s[20:23], s36 offen offset:128 nt lds\n"
    "v_fma_f32 v97, v97, s40, v234\n"
    "v_mfma_f32_16x16x32_bf16 v[56:59], a[144:147], a[72:75], 0\n"
    "v_exp_f32 v96, v96\n"
    "v_fma_f32 v98, v98, s40, v234\n"
    "v_mfma_f32_16x16x32_bf16 v[56:59], a[148:151], a[76:79], v[56:59]\n"
    "v_exp_f32 v97, v97\n"
    "v_exp_f32 v98, v98\n"
    "v_mfma_f32_16x16x32_bf16 v[60:63], a[168:171], a[72:75], 0\n"
    "v_fma_f32 v99, v99, s40, v234\n"
    "v_exp_f32 v99, v99\n"
    "v_mfma_f32_16x16x32_bf16 v[60:63], a[172:175], a[76:79], v[60:63]\n"
    "v_fma_f32 v100, v100, s40, v234\n"
    "v_exp_f32 v100, v100\n"
    "v_mfma_f32_16x16x32_bf16 v[112:115], v[128:131], a[72:75], 0\n"
    "v_fma_f32 v101, v101, s40, v234\n"
    "v_exp_f32 v101, v101\n"
    "v_mfma_f32_16x16x32_bf16 v[112:115], v[132:135], a[76:79], v[112:115]\n"
    "buffer_load_dwordx4 v229, s[20:23], s36 offen offset:1152 nt lds\n"
    "v_fma_f32 v102, v102, s40, v234\n"
    "s_waitcnt vmcnt(12)\n"
    "ds_read_b128 a[80:83], v253 offset:2048\n"
    "ds_read_b128 a[84:87], v254 offset:2048\n"
    "v_mfma_f32_16x16x32_bf16 v[116:119], v[152:155], a[72:75], 0\n"
    "v_exp_f32 v102, v102\n"
    "v_fma_f32 v103, v103, s40, v234\n"
    "v_mfma_f32_16x16x32_bf16 v[116:119], v[156:159], a[76:79], v[116:119]\n"
    "v_exp_f32 v103, v103\n"
    "v_fma_f32 v104, v104, s40, v234\n"
    "v_mfma_f32_16x16x32_bf16 v[120:123], v[176:179], a[72:75], 0\n"
    "v_exp_f32 v104, v104\n"
    "v_fma_f32 v105, v105, s40, v234\n"
    "v_mfma_f32_16x16x32_bf16 v[120:123], v[180:183], a[76:79], v[120:123]\n"
    "s_mov_b32 m0, s54\n"
    "v_exp_f32 v105, v105\n"
    "v_mfma_f32_16x16x32_bf16 v[124:127], v[200:203], a[72:75], 0\n"
    "v_cvt_pk_bf16_f32 v96, v96, v97\n"
    "v_fma_f32 v106, v106, s40, v234\n"
    "v_fma_f32 v107, v107, s40, v234\n"
    "v_mfma_f32_16x16x32_bf16 v[124:127], v[204:207], a[76:79], v[124:127]\n"
    "buffer_load_dwordx4 v228, s[20:23], s36 offen offset:256 nt lds\n"
    "s_waitcnt lgkmcnt(0)\n"
    "v_cvt_pk_bf16_f32 v97, v98, v99\n"
    "v_mfma_f32_16x16x32_bf16 v[48:51], a[104:107], a[80:83], v[48:51]\n"
    "v_exp_f32 v106, v106\n"
    "v_fma_f32 v108, v108, s40, v234\n"
    "v_mfma_f32_16x16x32_bf16 v[48:51], a[108:111], a[84:87], v[48:51]\n"
    "v_exp_f32 v107, v107\n"
    "v_exp_f32 v108, v108\n"
    "v_mfma_f32_16x16x32_bf16 v[52:55], a[128:131], a[80:83], v[52:55]\n"
    "v_fma_f32 v109, v109, s40, v234\n"
    "v_exp_f32 v109, v109\n"
    "v_mfma_f32_16x16x32_bf16 v[52:55], a[132:135], a[84:87], v[52:55]\n"
    "v_cvt_pk_bf16_f32 v98, v100, v101\n"
    "v_fma_f32 v110, v110, s40, v234\n"
    "v_fma_f32 v111, v111, s40, v234\n"
    "v_mfma_f32_16x16x32_bf16 v[56:59], a[152:155], a[80:83], v[56:59]\n"
    "v_exp_f32 v110, v110\n"
    "v_cvt_pk_bf16_f32 v99, v102, v103\n"
    "v_mfma_f32_16x16x32_bf16 v[56:59], a[156:159], a[84:87], v[56:59]\n"
    "buffer_load_dwordx4 v229, s[20:23], s36 offen offset:1280 nt lds\n"
    "v_cvt_pk_bf16_f32 v104, v104, v105\n"
    "v_mfma_f32_16x16x32_bf16 v[60:63], a[176:179], a[80:83], v[60:63]\n"
    "s_add_u32 s36, s36, 24576\n"
    "v_exp_f32 v111, v111\n"
    "v_mfma_f32_16x16x32_bf16 v[60:63], a[180:183], a[84:87], v[60:63]\n"
    "v_cvt_pk_bf16_f32 v105, v106, v107\n"
    "v_cvt_pk_bf16_f32 v106, v108, v109\n"
    "v_cvt_pk_bf16_f32 v107, v110, v111\n"
    "v_mfma_f32_16x16x32_bf16 v[112:115], v[136:139], a[80:83], v[112:115]\n"
    "v_mfma_f32_16x16x32_bf16 v[112:115], v[140:143], a[84:87], v[112:115]\n"
    "s_waitcnt vmcnt(12)\n"
    "ds_read_b128 a[88:91], v253 offset:4096\n"
    "ds_read_b128 a[92:95], v254 offset:4096\n"
    "v_mfma_f32_16x16x32_bf16 v[116:119], v[160:163], a[80:83], v[116:119]\n"
    "v_mfma_f32_16x16x32_bf16 v[116:119], v[164:167], a[84:87], v[116:119]\n"
    "v_mfma_f32_16x16x32_bf16 v[120:123], v[184:187], a[80:83], v[120:123]\n"
    "v_mfma_f32_16x16x32_bf16 v[120:123], v[188:191], a[84:87], v[120:123]\n"
    "v_mfma_f32_16x16x32_bf16 v[124:127], v[208:211], a[80:83], v[124:127]\n"
    "v_mfma_f32_16x16x32_bf16 v[124:127], v[212:215], a[84:87], v[124:127]\n"
    "s_waitcnt lgkmcnt(0)\n"
    "v_mfma_f32_16x16x32_bf16 v[48:51], a[112:115], a[88:91], v[48:51]\n"
    "v_mfma_f32_16x16x32_bf16 v[48:51], a[116:119], a[92:95], v[48:51]\n"
    "v_mfma_f32_16x16x32_bf16 v[52:55], a[136:139], a[88:91], v[52:55]\n"
    "v_mfma_f32_16x16x32_bf16 v[52:55], a[140:143], a[92:95], v[52:55]\n"
    "v_mfma_f32_16x16x32_bf16 v[56:59], a[160:163], a[88:91], v[56:59]\n"
    "v_mfma_f32_16x16x32_bf16 v[56:59], a[164:167], a[92:95], v[56:59]\n"
    "v_mfma_f32_16x16x32_bf16 v[60:63], a[184:187], a[88:91], v[60:63]\n"
    "v_mfma_f32_16x16x32_bf16 v[60:63], a[188:191], a[92:95], v[60:63]\n"
    "v_mfma_f32_16x16x32_bf16 v[112:115], v[144:147], a[88:91], v[112:115]\n"
    "s_waitcnt vmcnt(10)\n"
    "s_barrier\n"
    "v_max3_f32 v239, v48, v49, v50\n"
    "v_mfma_f32_16x16x32_bf16 v[112:115], v[148:151], a[92:95], v[112:115]\n"
    "ds_read_b64_tr_b16 a[192:193], v226 offset:0\n"
    "ds_read_b64_tr_b16 a[194:195], v226 offset:6144\n"
    "v_max3_f32 v239, v239, v51, v52\n"
    "v_max3_f32 v239, v239, v53, v54\n"
    "v_max3_f32 v239, v239, v55, v56\n"
    "v_max3_f32 v239, v239, v57, v58\n"
    "v_mfma_f32_16x16x32_bf16 v[116:119], v[168:171], a[88:91], v[116:119]\n"
    "ds_read_b64_tr_b16 a[196:197], v226 offset:12288\n"
    "ds_read_b64_tr_b16 a[198:199], v226 offset:18432\n"
    "v_max3_f32 v239, v239, v59, v60\n"
    "v_max3_f32 v239, v239, v61, v62\n"
    "v_mfma_f32_16x16x32_bf16 v[116:119], v[172:175], a[92:95], v[116:119]\n"
    "ds_read_b64_tr_b16 a[200:201], v227 offset:0\n"
    "ds_read_b64_tr_b16 a[202:203], v227 offset:6144\n"
    "v_max_f32 v239, v239, v63\n"
    "v_mfma_f32_16x16x32_bf16 v[120:123], v[192:195], a[88:91], v[120:123]\n"
    "ds_read_b64_tr_b16 a[204:205], v227 offset:12288\n"
    "ds_read_b64_tr_b16 a[206:207], v227 offset:18432\n"
    "v_mov_b32 v231, v239\n"
    "v_mfma_f32_16x16x32_bf16 v[120:123], v[196:199], a[92:95], v[120:123]\n"
    "ds_read_b64_tr_b16 a[208:209], v226 offset:512\n"
    "ds_read_b64_tr_b16 a[210:211], v226 offset:6656\n"
    "v_permlane16_swap_b32 v239, v231\n"
    "v_mfma_f32_16x16x32_bf16 v[124:127], v[216:219], a[88:91], v[124:127]\n"
    "ds_read_b64_tr_b16 a[212:213], v226 offset:12800\n"
    "ds_read_b64_tr_b16 a[214:215], v226 offset:18944\n"
    "v_max_f32 v239, v239, v231\n"
    "v_mov_b32 v231, v239\n"
    "v_mfma_f32_16x16x32_bf16 v[124:127], v[220:223], a[92:95], v[124:127]\n"
    "s_nop 0\n"
    "v_permlane32_swap_b32 v239, v231\n"
    "v_max_f32 v239, v239, v231\n"
    "v_mul_f32 v235, s47, v239\n"
    "v_add_f32 v235, s14, v235\n"
    "v_mov_b32 v251, v249\n"
    "s_waitcnt lgkmcnt(0)\n"
    "LB1_%=:\n"
    "v_mfma_f32_16x16x32_bf16 v[128:131], a[192:195], v[0:3], 0\n"
    "v_fma_f32 v48, v48, s40, v235\n"
    "v_exp_f32 v48, v48\n"
    "v_fma_f32 v49, v49, s40, v235\n"
    "v_mfma_f32_16x16x32_bf16 v[128:131], a[196:199], v[8:11], v[128:131]\n"
    "s_mov_b32 m0, s61\n"
    "v_exp_f32 v49, v49\n"
    "v_fma_f32 v50, v50, s40, v235\n"
    "v_mfma_f32_16x16x32_bf16 v[176:179], a[192:195], v[16:19], 0\n"
    "ds_read_b64_tr_b16 a[216:217], v227 offset:512\n"
    "v_exp_f32 v50, v50\n"
    "v_fma_f32 v51, v51, s40, v235\n"
    "v_mfma_f32_16x16x32_bf16 v[176:179], a[196:199], v[24:27], v[176:179]\n"
    "buffer_load_dwordx4 v230, s[24:27], s37 offen offset:0 nt lds\n"
    "v_exp_f32 v51, v51\n"
    "v_mfma_f32_16x16x32_bf16 a[96:99], a[192:195], v[32:35], 0\n"
    "ds_read_b64_tr_b16 a[218:219], v227 offset:6656\n"
    "v_fma_f32 v52, v52, s40, v235\n"
    "v_exp_f32 v52, v52\n"
    "v_mfma_f32_16x16x32_bf16 a[96:99], a[196:199], v[40:43], a[96:99]\n"
    "v_fma_f32 v53, v53, s40, v235\n"
    "v_exp_f32 v53, v53\n"
    "v_fma_f32 v54, v54, s40, v235\n"
    "v_mfma_f32_16x16x32_bf16 v[132:135], a[200:203], v[0:3], 0\n"
    "v_exp_f32 v54, v54\n"
    "v_fma_f32 v55, v55, s40, v235\n"
    "v_fma_f32 v56, v56, s40, v235\n"
    "v_mfma_f32_16x16x32_bf16 v[132:135], a[204:207], v[8:11], v[132:135]\n"
    "ds_read_b64_tr_b16 a[220:221], v227 offset:12800\n"
    "v_exp_f32 v55, v55\n"
    "v_fma_f32 v57, v57, s40, v235\n"
    "v_mfma_f32_16x16x32_bf16 v[180:183], a[200:203], v[16:19], 0\n"
    "buffer_load_dwordx4 v230, s[24:27], s37 offen offset:3072 nt lds\n"
    "v_exp_f32 v56, v56\n"
    "v_mfma_f32_16x16x32_bf16 v[180:183], a[204:207], v[24:27], v[180:183]\n"
    "v_exp_f32 v57, v57\n"
    "v_cvt_pk_bf16_f32 v48, v48, v49\n"
    "v_fma_f32 v58, v58, s40, v235\n"
    "v_mfma_f32_16x16x32_bf16 a[100:103], a[200:203], v[32:35], 0\n"
    "ds_read_b64_tr_b16 a[222:223], v227 offset:18944\n"
    "v_exp_f32 v58, v58\n"
    "v_fma_f32 v59, v59, s40, v235\n"
    "v_mfma_f32_16x16x32_bf16 a[100:103], a[204:207], v[40:43], a[100:103]\n"
    "s_mov_b32 m0, s62\n"
    "v_exp_f32 v59, v59\n"
    "v_cvt_pk_bf16_f32 v49, v50, v51\n"
    "v_mfma_f32_16x16x32_bf16 a[240:243], v[240:243], v[0:3], 0\n"
    "v_fma_f32 v60, v60, s40, v235\n"
    "v_exp_f32 v60, v60\n"
    "v_fma_f32 v61, v61, s40, v235\n"
    "v_mfma_f32_16x16x32_bf16 a[240:243], v[240:243], v[8:11], a[240:243]\n"
    "s_waitcnt vmcnt(10)\n"
    "v_exp_f32 v61, v61\n"
    "v_cvt_pk_bf16_f32 v50, v52, v53\n"
    "v_mfma_f32_16x16x32_bf16 v[136:139], a[208:211], v[0:3], 0\n"
    "s_barrier\n"
    "v_fma_f32 v62, v62, s40, v235\n"
    "v_exp_f32 v62, v62\n"
    "v_mfma_f32_16x16x32_bf16 v[136:139], a[212:215], v[8:11], v[136:139]\n"
    "buffer_load_dwordx4 v230, s[24:27], s37 offen offset:128 nt lds\n"
    "v_fma_f32 v63, v63, s40, v235\n"
    "v_cvt_pk_bf16_f32 v51, v54, v55\n"
    "v_mfma_f32_16x16x32_bf16 v[184:187], a[208:211], v[16:19], 0\n"
    "ds_read_b64_tr_b16 a[224:225], v226 offset:1024\n"
    "v_exp_f32 v63, v63\n"
    "v_cvt_pk_bf16_f32 v56, v56, v57\n"
    "v_mfma_f32_16x16x32_bf16 v[184:187], a[212:215], v[24:27], v[184:187]\n"
    "ds_read_b64_tr_b16 a[226:227], v226 offset:7168\n"
    "v_cvt_pk_bf16_f32 v57, v58, v59\n"
    "v_cvt_pk_bf16_f32 v58, v60, v61\n"
    "v_cvt_pk_bf16_f32 v59, v62, v63\n"
    "v_mfma_f32_16x16x32_bf16 a[104:107], a[208:211], v[32:35], 0\n"
    "ds_read_b64_tr_b16 a[228:229], v226 offset:13312\n"
    "v_fma_f32 v112, v112, s40, v235\n"
    "v_mfma_f32_16x16x32_bf16 a[104:107], a[212:215], v[40:43], a[104:107]\n"
    "v_exp_f32 v112, v112\n"
    "v_mfma_f32_16x16x32_bf16 a[144:147], a[192:195], v[48:51], 0\n"
    "buffer_load_dwordx4 v230, s[24:27], s37 offen offset:3200 nt lds\n"
    "v_mfma_f32_16x16x32_bf16 a[144:147], a[196:199], v[56:59], a[144:147]\n"
    "ds_read_b64_tr_b16 a[230:231], v226 offset:19456\n"
    "v_fma_f32 v113, v113, s40, v235\n"
    "v_mfma_f32_16x16x32_bf16 a[148:151], a[200:203], v[48:51], 0\n"
    "ds_read_b64_tr_b16 a[232:233], v227 offset:1024\n"
    "v_fma_f32 v114, v114, s40, v235\n"
    "v_mfma_f32_16x16x32_bf16 a[148:151], a[204:207], v[56:59], a[148:151]\n"
    "s_mov_b32 m0, s63\n"
    "v_fma_f32 v115, v115, s40, v235\n"
    "v_mfma_f32_16x16x32_bf16 a[152:155], a[208:211], v[48:51], 0\n"
    "ds_read_b64_tr_b16 a[234:235], v227 offset:7168\n"
    "v_fma_f32 v116, v116, s40, v235\n"
    "v_mfma_f32_16x16x32_bf16 a[152:155], a[212:215], v[56:59], a[152:155]\n"
    "s_waitcnt lgkmcnt(6)\n"
    "v_fma_f32 v117, v117, s40, v235\n"
    "v_mfma_f32_16x16x32_bf16 v[140:143], a[216:219], v[0:3], 0\n"
    "buffer_load_dwordx4 v230, s[24:27], s37 offen offset:256 nt lds\n"
    "v_mfma_f32_16x16x32_bf16 v[140:143], a[220:223], v[8:11], v[140:143]\n"
    "ds_read_b64_tr_b16 a[236:237], v227 offset:13312\n"
    "v_fma_f32 v118, v118, s40, v235\n"
    "v_mfma_f32_16x16x32_bf16 v[188:191], a[216:219], v[16:19], 0\n"
    "v_exp_f32 v113, v113\n"
    "v_mfma_f32_16x16x32_bf16 v[188:191], a[220:223], v[24:27], v[188:191]\n"
    "ds_read_b64_tr_b16 a[238:239], v227 offset:19456\n"
    "v_fma_f32 v119, v119, s40, v235\n"
    "v_mfma_f32_16x16x32_bf16 a[108:111], a[216:219], v[32:35], 0\n"
    "ds_read_b64_tr_b16 a[192:193], v226 offset:1536\n"
    "v_fma_f32 v120, v120, s40, v235\n"
    "v_mfma_f32_16x16x32_bf16 a[108:111], a[220:223], v[40:43], a[108:111]\n"
    "buffer_load_dwordx4 v230, s[24:27], s37 offen offset:3328 nt lds\n"
    "v_mfma_f32_16x16x32_bf16 a[156:159], a[216:219], v[48:51], 0\n"
    "s_add_u32 s37, s37, 24576\n"
    "v_fma_f32 v121, v121, s40, v235\n"
    "v_mfma_f32_16x16x32_bf16 a[156:159], a[220:223], v[56:59], a[156:159]\n"
    "s_waitcnt lgkmcnt(5)\n"
    "v_cvt_pk_bf16_f32 v112, v112, v113\n"
    "v_mfma_f32_16x16x32_bf16 v[144:147], a[224:227], v[0:3], 0\n"
    "ds_read_b64_tr_b16 a[194:195], v226 offset:7680\n"
    "v_fma_f32 v122, v122, s40, v235\n"
    "v_mfma_f32_16x16x32_bf16 v[144:147], a[228:231], v[8:11], v[144:147]\n"
    "ds_read_b64_tr_b16 a[196:197], v226 offset:13824\n"
    "v_fma_f32 v123, v123, s40, v235\n"
    "v_mfma_f32_16x16x32_bf16 v[192:195], a[224:227], v[16:19], 0\n"
    "v_exp_f32 v114, v114\n"
    "v_mfma_f32_16x16x32_bf16 v[192:195], a[228:231], v[24:27], v[192:195]\n"
    "ds_read_b64_tr_b16 a[198:199], v226 offset:19968\n"
    "v_mfma_f32_16x16x32_bf16 a[112:115], a[224:227], v[32:35], 0\n"
    "ds_read_b64_tr_b16 a[200:201], v227 offset:1536\n"
    "v_mfma_f32_16x16x32_bf16 a[112:115], a[228:231], v[40:43], a[112:115]\n"
    "v_exp_f32 v115, v115\n"
    "v_mfma_f32_16x16x32_bf16 a[160:163], a[224:227], v[48:51], 0\n"
    "ds_read_b64_tr_b16 a[202:203], v227 offset:7680\n"
    "v_cvt_pk_bf16_f32 v113, v114, v115\n"
    "v_mfma_f32_16x16x32_bf16 a[160:163], a[228:231], v[56:59], a[160:163]\n"
    "v_exp_f32 v116, v116\n"
    "v_mfma_f32_16x16x32_bf16 a[244:247], v[240:243], v[16:19], 0\n"
    "ds_read_b64_tr_b16 a[204:205], v227 offset:13824\n"
    "v_mfma_f32_16x16x32_bf16 a[244:247], v[240:243], v[24:27], a[244:247]\n"
    "s_waitcnt lgkmcnt(7)\n"
    "v_mfma_f32_16x16x32_bf16 v[148:151], a[232:235], v[0:3], 0\n"
    "v_exp_f32 v117, v117\n"
    "v_mfma_f32_16x16x32_bf16 v[148:151], a[236:239], v[8:11], v[148:151]\n"
    "s_waitcnt vmcnt(12)\n"
    "v_cvt_pk_bf16_f32 v114, v116, v117\n"
    "v_mfma_f32_16x16x32_bf16 v[196:199], a[232:235], v[16:19], 0\n"
    "s_barrier\n"
    "v_mfma_f32_16x16x32_bf16 v[196:199], a[236:239], v[24:27], v[196:199]\n"
    "ds_read_b64_tr_b16 a[206:207], v227 offset:19968\n"
    "v_mfma_f32_16x16x32_bf16 a[116:119], a[232:235], v[32:35], 0\n"
    "ds_read_b64_tr_b16 a[208:209], v226 offset:2048\n"
    "v_mfma_f32_16x16x32_bf16 a[116:119], a[236:239], v[40:43], a[116:119]\n"
    "ds_read_b64_tr_b16 a[210:211], v226 offset:8192\n"
    "v_mfma_f32_16x16x32_bf16 a[164:167], a[232:235], v[48:51], 0\n"
    "v_exp_f32 v118, v118\n"
    "v_mfma_f32_16x16x32_bf16 a[164:167], a[236:239], v[56:59], a[164:167]\n"
    "s_waitcnt lgkmcnt(6)\n"
    "v_mfma_f32_16x16x32_bf16 v[152:155], a[192:195], v[0:3], 0\n"
    "ds_read_b64_tr_b16 a[212:213], v226 offset:14336\n"
    "v_mfma_f32_16x16x32_bf16 v[152:155], a[196:199], v[8:11], v[152:155]\n"
    "v_exp_f32 v119, v119\n"
    "v_mfma_f32_16x16x32_bf16 v[200:203], a[192:195], v[16:19], 0\n"
    "ds_read_b64_tr_b16 a[214:215], v226 offset:20480\n"
    "v_cvt_pk_bf16_f32 v115, v118, v119\n"
    "v_mfma_f32_16x16x32_bf16 v[200:203], a[196:199], v[24:27], v[200:203]\n"
    "ds_read_b64_tr_b16 a[216:217], v227 offset:2048\n"
    "v_mfma_f32_16x16x32_bf16 a[120:123], a[192:195], v[32:35], 0\n"
    "ds_read_b64_tr_b16 a[218:219], v227 offset:8192\n"
    "v_mfma_f32_16x16x32_bf16 a[120:123], a[196:199], v[40:43], a[120:123]\n"
    "ds_read_b64_tr_b16 a[220:221], v227 offset:14336\n"
    "v_mfma_f32_16x16x32_bf16 a[168:171], a[192:195], v[48:51], 0\n"
    "s_mov_b32 m0, s55\n"
    "ds_read_b64_tr_b16 a[222:223], v227 offset:20480\n"
    "v_mfma_f32_16x16x32_bf16 a[168:171], a[196:199], v[56:59], a[168:171]\n"
    "s_waitcnt lgkmcnt(8)\n"
    "v_mfma_f32_16x16x32_bf16 v[156:159], a[200:203], v[0:3], 0\n"
    "buffer_load_dwordx4 v228, s[20:23], s36 offen offset:0 nt lds\n"
    "v_mfma_f32_16x16x32_bf16 v[156:159], a[204:207], v[8:11], v[156:159]\n"
    "ds_read_b64_tr_b16 a[224:225], v226 offset:2560\n"
    "v_mfma_f32_16x16x32_bf16 v[204:207], a[200:203], v[16:19], 0\n"
    "ds_read_b64_tr_b16 a[226:227], v226 offset:8704\n"
    "v_mfma_f32_16x16x32_bf16 v[204:207], a[204:207], v[24:27], v[204:207]\n"
    "ds_read_b64_tr_b16 a[228:229], v226 offset:14848\n"
    "v_mfma_f32_16x16x32_bf16 a[124:127], a[200:203], v[32:35], 0\n"
    "ds_read_b64_tr_b16 a[230:231], v226 offset:20992\n"
    "v_mfma_f32_16x16x32_bf16 a[124:127], a[204:207], v[40:43], a[124:127]\n"
    "buffer_load_dwordx4 v229, s[20:23], s36 offen offset:1024 nt lds\n"
    "v_mfma_f32_16x16x32_bf16 a[172:175], a[200:203], v[48:51], 0\n"
    "ds_read_b64_tr_b16 a[232:233], v227 offset:2560\n"
    "v_mfma_f32_16x16x32_bf16 a[172:175], a[204:207], v[56:59], a[172:175]\n"
    "ds_read_b64_tr_b16 a[234:235], v227 offset:8704\n"
    "v_mfma_f32_16x16x32_bf16 a[248:251], v[240:243], v[32:35], 0\n"
    "s_mov_b32 m0, s56\n"
    "ds_read_b64_tr_b16 a[236:237], v227 offset:14848\n"
    "v_mfma_f32_16x16x32_bf16 a[248:251], v[240:243], v[40:43], a[248:251]\n"
    "s_waitcnt lgkmcnt(11)\n"
    "ds_read_b64_tr_b16 a[238:239], v227 offset:20992\n"
    "v_mfma_f32_16x16x32_bf16 v[160:163], a[208:211], v[0:3], 0\n"
    "buffer_load_dwordx4 v228, s[20:23], s36 offen offset:128 nt lds\n"
    "v_mfma_f32_16x16x32_bf16 v[160:163], a[212:215], v[8:11], v[160:163]\n"
    "v_exp_f32 v120, v120\n"
    "v_mfma_f32_16x16x32_bf16 v[208:211], a[208:211], v[16:19], 0\n"
    "v_exp_f32 v121, v121\n"
    "v_mfma_f32_16x16x32_bf16 v[208:211], a[212:215], v[24:27], v[208:211]\n"
    "v_exp_f32 v122, v122\n"
    "v_mfma_f32_16x16x32_bf16 a[128:131], a[208:211], v[32:35], 0\n"
    "v_exp_f32 v123, v123\n"
    "v_mfma_f32_16x16x32_bf16 a[128:131], a[212:215], v[40:43], a[128:131]\n"
    "buffer_load_dwordx4 v229, s[20:23], s36 offen offset:1152 nt lds\n"
    "v_mfma_f32_16x16x32_bf16 a[176:179], a[208:211], v[48:51], 0\n"
    "v_cvt_pk_bf16_f32 v120, v120, v121\n"
    "v_cvt_pk_bf16_f32 v121, v122, v123\n"
    "v_mfma_f32_16x16x32_bf16 a[176:179], a[212:215], v[56:59], a[176:179]\n"
    "s_waitcnt lgkmcnt(8)\n"
    "v_mfma_f32_16x16x32_bf16 v[164:167], a[216:219], v[0:3], 0\n"
    "s_mov_b32 m0, s57\n"
    "v_mfma_f32_16x16x32_bf16 v[164:167], a[220:223], v[8:11], v[164:167]\n"
    "s_waitcnt vmcnt(10) lgkmcnt(0)\n"
    "v_mfma_f32_16x16x32_bf16 v[212:215], a[216:219], v[16:19], 0\n"
    "s_barrier\n"
    "v_mfma_f32_16x16x32_bf16 v[212:215], a[220:223], v[24:27], v[212:215]\n"
    "buffer_load_dwordx4 v228, s[20:23], s36 offen offset:256 nt lds\n"
    "v_mfma_f32_16x16x32_bf16 a[132:135], a[216:219], v[32:35], 0\n"
    "ds_read_b128 a[192:195], v224 offset:0\n"
    "v_mfma_f32_16x16x32_bf16 a[132:135], a[220:223], v[40:43], a[132:135]\n"
    "ds_read_b128 a[196:199], v225 offset:0\n"
    "v_mfma_f32_16x16x32_bf16 a[180:183], a[216:219], v[48:51], 0\n"
    "ds_read_b128 a[200:203], v224 offset:8192\n"
    "v_mfma_f32_16x16x32_bf16 a[180:183], a[220:223], v[56:59], a[180:183]\n"
    "ds_read_b128 a[204:207], v225 offset:8192\n"
    "v_mfma_f32_16x16x32_bf16 v[168:171], a[224:227], v[0:3], 0\n"
    "buffer_load_dwordx4 v229, s[20:23], s36 offen offset:1280 nt lds\n"
    "v_mfma_f32_16x16x32_bf16 v[168:171], a[228:231], v[8:11], v[168:171]\n"
    "s_add_u32 s36, s36, 24576\n"
    "v_mfma_f32_16x16x32_bf16 v[216:219], a[224:227], v[16:19], 0\n"
    "ds_read_b128 a[208:211], v224 offset:16384\n"
    "v_mfma_f32_16x16x32_bf16 v[216:219], a[228:231], v[24:27], v[216:219]\n"
    "s_mov_b32 s45, 2\n"
    "v_mfma_f32_16x16x32_bf16 a[136:139], a[224:227], v[32:35], 0\n"
    "ds_read_b128 a[212:215], v225 offset:16384\n"
    "v_mfma_f32_16x16x32_bf16 a[136:139], a[228:231], v[40:43], a[136:139]\n"
    "v_mfma_f32_16x16x32_bf16 a[184:187], a[224:227], v[48:51], 0\n"
    "ds_read_b128 a[216:219], v224 offset:2048\n"
    "v_mfma_f32_16x16x32_bf16 a[184:187], a[228:231], v[56:59], a[184:187]\n"
    "v_mfma_f32_16x16x32_bf16 a[252:255], v[240:243], v[48:51], 0\n"
    "ds_read_b128 a[220:223], v225 offset:2048\n"
    "v_mfma_f32_16x16x32_bf16 a[252:255], v[240:243], v[56:59], a[252:255]\n"
    "v_mfma_f32_16x16x32_bf16 v[172:175], a[232:235], v[0:3], 0\n"
    "v_mfma_f32_16x16x32_bf16 v[172:175], a[236:239], v[8:11], v[172:175]\n"
    "ds_read_b128 a[224:227], v224 offset:10240\n"
    "v_mfma_f32_16x16x32_bf16 v[220:223], a[232:235], v[16:19], 0\n"
    "v_mfma_f32_16x16x32_bf16 v[220:223], a[236:239], v[24:27], v[220:223]\n"
    "v_mfma_f32_16x16x32_bf16 a[140:143], a[232:235], v[32:35], 0\n"
    "v_mfma_f32_16x16x32_bf16 a[140:143], a[236:239], v[40:43], a[140:143]\n"
    "ds_read_b128 a[228:231], v225 offset:10240\n"
    "v_mfma_f32_16x16x32_bf16 a[188:191], a[232:235], v[48:51], 0\n"
    "v_mfma_f32_16x16x32_bf16 a[188:191], a[236:239], v[56:59], a[188:191]\n"
    "s_waitcnt lgkmcnt(4)\n"
    "LLOOP_%=:\n"
    "v_mfma_f32_16x16x32_bf16 v[0:3], a[192:195], a[0:3], 0\n"
    "v_fma_f32 v124, v124, s40, v235\n"
    "v_fma_f32 v125, v125, s40, v235\n"
    "v_mfma_f32_16x16x32_bf16 v[0:3], a[196:199], a[4:7], v[0:3]\n"
    "s_mov_b32 m0, s58\n"
    "v_fma_f32 v126, v126, s40, v235\n"
    "v_mfma_f32_16x16x32_bf16 v[0:3], a[200:203], a[8:11], v[0:3]\n"
    "v_exp_f32 v124, v124\n"
    "v_mfma_f32_16x16x32_bf16 v[0:3], a[204:207], a[12:15], v[0:3]\n"
    "buffer_load_dwordx4 v230, s[24:27], s37 offen offset:0 nt lds\n"
    "v_mfma_f32_16x16x32_bf16 v[0:3], a[208:211], a[16:19], v[0:3]\n"
    "ds_read_b128 a[232:235], v224 offset:18432\n"
    "v_fma_f32 v127, v127, s40, v235\n"
    "v_mfma_f32_16x16x32_bf16 v[0:3], a[212:215], a[20:23], v[0:3]\n"
    "v_exp_f32 v125, v125\n"
    "v_mfma_f32_16x16x32_bf16 v[16:19], a[192:195], a[24:27], 0\n"
    "v_exp_f32 v126, v126\n"
    "v_mfma_f32_16x16x32_bf16 v[16:19], a[196:199], a[28:31], v[16:19]\n"
    "v_exp_f32 v127, v127\n"
    "v_mfma_f32_16x16x32_bf16 v[16:19], a[200:203], a[32:35], v[16:19]\n"
    "buffer_load_dwordx4 v230, s[24:27], s37 offen offset:3072 nt lds\n"
    "v_mfma_f32_16x16x32_bf16 v[16:19], a[204:207], a[36:39], v[16:19]\n"
    "ds_read_b128 a[236:239], v225 offset:18432\n"
    "v_cvt_pk_bf16_f32 v122, v124, v125\n"
    "v_mfma_f32_16x16x32_bf16 v[16:19], a[208:211], a[40:43], v[16:19]\n"
    "v_cvt_pk_bf16_f32 v123, v126, v127\n"
    "v_fma_f32 v0, v0, s40, v232\n"
    "v_mfma_f32_16x16x32_bf16 v[16:19], a[212:215], a[44:47], v[16:19]\n"
    "s_mov_b32 m0, s59\n"
    "v_fma_f32 v1, v1, s40, v232\n"
    "v_mfma_f32_16x16x32_bf16 v[32:35], a[192:195], a[48:51], 0\n"
    "v_exp_f32 v0, v0\n"
    "v_mfma_f32_16x16x32_bf16 v[32:35], a[196:199], a[52:55], v[32:35]\n"
    "buffer_load_dwordx4 v230, s[24:27], s37 offen offset:128 nt lds\n"
    "v_mfma_f32_16x16x32_bf16 v[32:35], a[200:203], a[56:59], v[32:35]\n"
    "v_exp_f32 v1, v1\n"
    "v_mfma_f32_16x16x32_bf16 v[32:35], a[204:207], a[60:63], v[32:35]\n"
    "v_fma_f32 v2, v2, s40, v232\n"
    "v_fma_f32 v3, v3, s40, v232\n"
    "v_mfma_f32_16x16x32_bf16 v[32:35], a[208:211], a[64:67], v[32:35]\n"
    "v_exp_f32 v2, v2\n"
    "v_mfma_f32_16x16x32_bf16 v[32:35], a[212:215], a[68:71], v[32:35]\n"
    "v_exp_f32 v3, v3\n"
    "v_mfma_f32_16x16x32_bf16 v[48:51], a[192:195], a[72:75], 0\n"
    "buffer_load_dwordx4 v230, s[24:27], s37 offen offset:3200 nt lds\n"
    "v_mfma_f32_16x16x32_bf16 v[48:51], a[196:199], a[76:79], v[48:51]\n"
    "ds_read_b128 a[192:195], v224 offset:4096\n"
    "v_fma_f32 v16, v16, s40, v233\n"
    "v_mfma_f32_16x16x32_bf16 v[48:51], a[200:203], a[80:83], v[48:51]\n"
    "v_exp_f32 v16, v16\n"
    "v_mfma_f32_16x16x32_bf16 v[48:51], a[204:207], a[84:87], v[48:51]\n"
    "s_mov_b32 m0, s60\n"
    "v_fma_f32 v17, v17, s40, v233\n"
    "v_mfma_f32_16x16x32_bf16 v[48:51], a[208:211], a[88:91], v[48:51]\n"
    "ds_read_b128 a[196:199], v225 offset:4096\n"
    "v_fma_f32 v18, v18, s40, v233\n"
    "v_mfma_f32_16x16x32_bf16 v[48:51], a[212:215], a[92:95], v[48:51]\n"
    "s_waitcnt lgkmcnt(2)\n"
    "v_fma_f32 v19, v19, s40, v233\n"
    "v_mfma_f32_16x16x32_bf16 v[4:7], a[216:219], a[0:3], 0\n"
    "buffer_load_dwordx4 v230, s[24:27], s37 offen offset:256 nt lds\n"
    "v_mfma_f32_16x16x32_bf16 v[4:7], a[220:223], a[4:7], v[4:7]\n"
    "ds_read_b128 a[200:203], v224 offset:12288\n"
    "v_fma_f32 v32, v32, s40, v234\n"
    "v_mfma_f32_16x16x32_bf16 v[4:7], a[224:227], a[8:11], v[4:7]\n"
    "v_exp_f32 v17, v17\n"
    "v_mfma_f32_16x16x32_bf16 v[4:7], a[228:231], a[12:15], v[4:7]\n"
    "ds_read_b128 a[204:207], v225 offset:12288\n"
    "v_fma_f32 v33, v33, s40, v234\n"
    "v_mfma_f32_16x16x32_bf16 v[4:7], a[232:235], a[16:19], v[4:7]\n"
    "v_exp_f32 v18, v18\n"
    "v_mfma_f32_16x16x32_bf16 v[4:7], a[236:239], a[20:23], v[4:7]\n"
    "buffer_load_dwordx4 v230, s[24:27], s37 offen offset:3328 nt lds\n"
    "v_mfma_f32_16x16x32_bf16 v[20:23], a[216:219], a[24:27], 0\n"
    "s_add_u32 s37, s37, 24576\n"
    "v_cvt_pk_bf16_f32 v0, v0, v1\n"
    "v_mfma_f32_16x16x32_bf16 v[20:23], a[220:223], a[28:31], v[20:23]\n"
    "ds_read_b128 a[208:211], v224 offset:20480\n"
    "v_fma_f32 v34, v34, s40, v234\n"
    "v_mfma_f32_16x16x32_bf16 v[20:23], a[224:227], a[32:35], v[20:23]\n"
    "ds_read_b128 v[244:247], v251 offset:12288\n"
    "v_fma_f32 v35, v35, s40, v234\n"
    "v_mfma_f32_16x16x32_bf16 v[20:23], a[228:231], a[36:39], v[20:23]\n"
    "ds_read_b128 a[212:215], v225 offset:20480\n"
    "v_cvt_pk_bf16_f32 v1, v2, v3\n"
    "v_mfma_f32_16x16x32_bf16 v[20:23], a[232:235], a[40:43], v[20:23]\n"
    "v_exp_f32 v19, v19\n"
    "v_mfma_f32_16x16x32_bf16 v[20:23], a[236:239], a[44:47], v[20:23]\n"
    "v_exp_f32 v32, v32\n"
    "v_mfma_f32_16x16x32_bf16 v[36:39], a[216:219], a[48:51], 0\n"
    "v_exp_f32 v33, v33\n"
    "v_mfma_f32_16x16x32_bf16 v[36:39], a[220:223], a[52:55], v[36:39]\n"
    "v_exp_f32 v34, v34\n"
    "v_mfma_f32_16x16x32_bf16 v[36:39], a[224:227], a[56:59], v[36:39]\n"
    "v_exp_f32 v35, v35\n"
    "v_mfma_f32_16x16x32_bf16 v[36:39], a[228:231], a[60:63], v[36:39]\n"
    "v_fma_f32 v48, v48, s40, v235\n"
    "v_fma_f32 v49, v49, s40, v235\n"
    "v_mfma_f32_16x16x32_bf16 v[36:39], a[232:235], a[64:67], v[36:39]\n"
    "v_exp_f32 v48, v48\n"
    "v_mfma_f32_16x16x32_bf16 v[36:39], a[236:239], a[68:71], v[36:39]\n"
    "v_exp_f32 v49, v49\n"
    "v_mfma_f32_16x16x32_bf16 v[52:55], a[216:219], a[72:75], 0\n"
    "v_cvt_pk_bf16_f32 v16, v16, v17\n"
    "v_fma_f32 v50, v50, s40, v235\n"
    "v_mfma_f32_16x16x32_bf16 v[52:55], a[220:223], a[76:79], v[52:55]\n"
    "ds_read_b128 a[216:219], v224 offset:6144\n"
    "v_fma_f32 v51, v51, s40, v235\n"
    "v_mfma_f32_16x16x32_bf16 v[52:55], a[224:227], a[80:83], v[52:55]\n"
    "ds_read_b128 a[220:223], v225 offset:6144\n"
    "v_cvt_pk_bf16_f32 v17, v18, v19\n"
    "v_mfma_f32_16x16x32_bf16 v[52:55], a[228:231], a[84:87], v[52:55]\n"
    "ds_read_b128 a[224:227], v224 offset:14336\n"
    "v_fma_f32 v4, v4, s40, v232\n"
    "v_mfma_f32_16x16x32_bf16 v[52:55], a[232:235], a[88:91], v[52:55]\n"
    "v_exp_f32 v50, v50\n"
    "v_mfma_f32_16x16x32_bf16 v[52:55], a[236:239], a[92:95], v[52:55]\n"
    "s_waitcnt lgkmcnt(3)\n"
    "v_fma_f32 v5, v5, s40, v232\n"
    "v_mfma_f32_16x16x32_bf16 v[8:11], a[192:195], a[0:3], 0\n"
    "ds_read_b128 a[228:231], v225 offset:14336\n"
    "v_cvt_pk_bf16_f32 v32, v32, v33\n"
    "v_mfma_f32_16x16x32_bf16 v[8:11], a[196:199], a[4:7], v[8:11]\n"
    "buffer_store_dwordx4 v[244:247], v250, s[84:87], s79 offen offset:128 nt sc1\n"
    "v_mfma_f32_16x16x32_bf16 v[8:11], a[200:203], a[8:11], v[8:11]\n"
    "ds_read_b128 a[232:235], v224 offset:22528\n"
    "v_fma_f32 v6, v6, s40, v232\n"
    "v_mfma_f32_16x16x32_bf16 v[8:11], a[204:207], a[12:15], v[8:11]\n"
    "v_exp_f32 v51, v51\n"
    "v_mfma_f32_16x16x32_bf16 v[8:11], a[208:211], a[16:19], v[8:11]\n"
    "v_exp_f32 v4, v4\n"
    "v_mfma_f32_16x16x32_bf16 v[8:11], a[212:215], a[20:23], v[8:11]\n"
    "v_exp_f32 v5, v5\n"
    "v_mfma_f32_16x16x32_bf16 v[24:27], a[192:195], a[24:27], 0\n"
    "ds_read_b128 a[236:239], v225 offset:22528\n"
    "v_fma_f32 v7, v7, s40, v232\n"
    "v_mfma_f32_16x16x32_bf16 v[24:27], a[196:199], a[28:31], v[24:27]\n"
    "v_exp_f32 v6, v6\n"
    "v_mfma_f32_16x16x32_bf16 v[24:27], a[200:203], a[32:35], v[24:27]\n"
    "v_exp_f32 v7, v7\n"
    "v_mfma_f32_16x16x32_bf16 v[24:27], a[204:207], a[36:39], v[24:27]\n"
    "v_cvt_pk_bf16_f32 v33, v34, v35\n"
    "v_fma_f32 v20, v20, s40, v233\n"
    "v_mfma_f32_16x16x32_bf16 v[24:27], a[208:211], a[40:43], v[24:27]\n"
    "v_exp_f32 v20, v20\n"
    "v_mfma_f32_16x16x32_bf16 v[24:27], a[212:215], a[44:47], v[24:27]\n"
    "v_fma_f32 v21, v21, s40, v233\n"
    "v_cvt_pk_bf16_f32 v48, v48, v49\n"
    "v_mfma_f32_16x16x32_bf16 v[40:43], a[192:195], a[48:51], 0\n"
    "v_exp_f32 v21, v21\n"
    "v_mfma_f32_16x16x32_bf16 v[40:43], a[196:199], a[52:55], v[40:43]\n"
    "v_fma_f32 v22, v22, s40, v233\n"
    "v_fma_f32 v23, v23, s40, v233\n"
    "v_mfma_f32_16x16x32_bf16 v[40:43], a[200:203], a[56:59], v[40:43]\n"
    "v_exp_f32 v22, v22\n"
    "v_mfma_f32_16x16x32_bf16 v[40:43], a[204:207], a[60:63], v[40:43]\n"
    "v_exp_f32 v23, v23\n"
    "v_mfma_f32_16x16x32_bf16 v[40:43], a[208:211], a[64:67], v[40:43]\n"
    "v_cvt_pk_bf16_f32 v49, v50, v51\n"
    "v_fma_f32 v36, v36, s40, v234\n"
    "v_mfma_f32_16x16x32_bf16 v[40:43], a[212:215], a[68:71], v[40:43]\n"
    "s_waitcnt vmcnt(13) lgkmcnt(0)\n"
    "v_fma_f32 v37, v37, s40, v234\n"
    "v_mfma_f32_16x16x32_bf16 v[56:59], a[192:195], a[72:75], 0\n"
    "s_barrier\n"
    "v_cvt_pk_bf16_f32 v2, v4, v5\n"
    "v_mfma_f32_16x16x32_bf16 v[56:59], a[196:199], a[76:79], v[56:59]\n"
    "ds_read_b64_tr_b16 a[192:193], v226 offset:24576\n"
    "v_fma_f32 v38, v38, s40, v234\n"
    "v_mfma_f32_16x16x32_bf16 v[56:59], a[200:203], a[80:83], v[56:59]\n"
    "s_add_u32 m0, s39, 0\n"
    "v_fma_f32 v39, v39, s40, v234\n"
    "v_mfma_f32_16x16x32_bf16 v[56:59], a[204:207], a[84:87], v[56:59]\n"
    "ds_read_b64_tr_b16 a[194:195], v226 offset:30720\n"
    "v_cvt_pk_bf16_f32 v3, v6, v7\n"
    "v_mfma_f32_16x16x32_bf16 v[56:59], a[208:211], a[88:91], v[56:59]\n"
    "buffer_load_dwordx4 v228, s[28:31], s38 offen offset:0 nt lds\n"
    "v_mfma_f32_16x16x32_bf16 v[56:59], a[212:215], a[92:95], v[56:59]\n"
    "ds_read_b64_tr_b16 a[196:197], v226 offset:36864\n"
    "v_fma_f32 v52, v52, s40, v235\n"
    "v_mfma_f32_16x16x32_bf16 v[12:15], a[216:219], a[0:3], 0\n"
    "ds_read_b64_tr_b16 a[198:199], v226 offset:43008\n"
    "v_fma_f32 v53, v53, s40, v235\n"
    "v_mfma_f32_16x16x32_bf16 v[12:15], a[220:223], a[4:7], v[12:15]\n"
    "ds_read_b64_tr_b16 a[200:201], v227 offset:24576\n"
    "v_cvt_pk_bf16_f32 v18, v20, v21\n"
    "v_mfma_f32_16x16x32_bf16 v[12:15], a[224:227], a[8:11], v[12:15]\n"
    "ds_read_b64_tr_b16 a[202:203], v227 offset:30720\n"
    "v_fma_f32 v54, v54, s40, v235\n"
    "v_mfma_f32_16x16x32_bf16 v[12:15], a[228:231], a[12:15], v[12:15]\n"
    "buffer_load_dwordx4 v229, s[28:31], s38 offen offset:1024 nt lds\n"
    "v_mfma_f32_16x16x32_bf16 v[12:15], a[232:235], a[16:19], v[12:15]\n"
    "ds_read_b64_tr_b16 a[204:205], v227 offset:36864\n"
    "v_fma_f32 v55, v55, s40, v235\n"
    "v_mfma_f32_16x16x32_bf16 v[12:15], a[236:239], a[20:23], v[12:15]\n"
    "ds_read_b64_tr_b16 a[206:207], v227 offset:43008\n"
    "v_cvt_pk_bf16_f32 v19, v22, v23\n"
    "v_mfma_f32_16x16x32_bf16 v[28:31], a[216:219], a[24:27], 0\n"
    "ds_read_b64_tr_b16 a[208:209], v226 offset:25088\n"
    "v_mfma_f32_16x16x32_bf16 v[28:31], a[220:223], a[28:31], v[28:31]\n"
    "ds_read_b64_tr_b16 a[210:211], v226 offset:31232\n"
    "v_mfma_f32_16x16x32_bf16 v[28:31], a[224:227], a[32:35], v[28:31]\n"
    "v_exp_f32 v36, v36\n"
    "v_mfma_f32_16x16x32_bf16 v[28:31], a[228:231], a[36:39], v[28:31]\n"
    "ds_read_b64_tr_b16 a[212:213], v226 offset:37376\n"
    "v_mfma_f32_16x16x32_bf16 v[28:31], a[232:235], a[40:43], v[28:31]\n"
    "v_exp_f32 v37, v37\n"
    "v_mfma_f32_16x16x32_bf16 v[28:31], a[236:239], a[44:47], v[28:31]\n"
    "v_exp_f32 v38, v38\n"
    "v_mfma_f32_16x16x32_bf16 v[44:47], a[216:219], a[48:51], 0\n"
    "ds_read_b64_tr_b16 a[214:215], v226 offset:43520\n"
    "v_cvt_pk_bf16_f32 v34, v36, v37\n"
    "v_mfma_f32_16x16x32_bf16 v[44:47], a[220:223], a[52:55], v[44:47]\n"
    "v_exp_f32 v39, v39\n"
    "v_mfma_f32_16x16x32_bf16 v[44:47], a[224:227], a[56:59], v[44:47]\n"
    "v_exp_f32 v52, v52\n"
    "v_mfma_f32_16x16x32_bf16 v[44:47], a[228:231], a[60:63], v[44:47]\n"
    "v_exp_f32 v53, v53\n"
    "v_mfma_f32_16x16x32_bf16 v[44:47], a[232:235], a[64:67], v[44:47]\n"
    "v_exp_f32 v54, v54\n"
    "v_mfma_f32_16x16x32_bf16 v[44:47], a[236:239], a[68:71], v[44:47]\n"
    "v_exp_f32 v55, v55\n"
    "v_mfma_f32_16x16x32_bf16 v[60:63], a[216:219], a[72:75], 0\n"
    "v_cvt_pk_bf16_f32 v35, v38, v39\n"
    "v_cvt_pk_bf16_f32 v50, v52, v53\n"
    "v_mfma_f32_16x16x32_bf16 v[60:63], a[220:223], a[76:79], v[60:63]\n"
    "v_cvt_pk_bf16_f32 v51, v54, v55\n"
    "v_mfma_f32_16x16x32_bf16 v[60:63], a[224:227], a[80:83], v[60:63]\n"
    "v_mfma_f32_16x16x32_bf16 v[60:63], a[228:231], a[84:87], v[60:63]\n"
    "v_mfma_f32_16x16x32_bf16 v[60:63], a[232:235], a[88:91], v[60:63]\n"
    "s_cmp_eq_u32 s45, s16\n"
    "v_mfma_f32_16x16x32_bf16 v[60:63], a[236:239], a[92:95], v[60:63]\n"
    "s_waitcnt lgkmcnt(0)\n"
    "s_cbranch_scc1 LBD_%=\n"
    "v_mfma_f32_16x16x32_bf16 v[128:131], a[192:195], v[64:67], v[128:131]\n"
    "ds_read_b64_tr_b16 a[216:217], v227 offset:25088\n"
    "v_fma_f32 v8, v8, s40, v232\n"
    "v_mfma_f32_16x16x32_bf16 v[128:131], a[196:199], v[72:75], v[128:131]\n"
    "s_mov_b32 m0, s52\n"
    "v_fma_f32 v9, v9, s40, v232\n"
    "v_mfma_f32_16x16x32_bf16 v[176:179], a[192:195], v[80:83], v[176:179]\n"
    "ds_read_b64_tr_b16 a[218:219], v227 offset:31232\n"
    "v_fma_f32 v10, v10, s40, v232\n"
    "v_mfma_f32_16x16x32_bf16 v[176:179], a[196:199], v[88:91], v[176:179]\n"
    "buffer_load_dwordx4 v228, s[20:23], s36 offen offset:0 nt lds\n"
    "v_mfma_f32_16x16x32_bf16 a[96:99], a[192:195], v[96:99], a[96:99]\n"
    "ds_read_b64_tr_b16 a[220:221], v227 offset:37376\n"
    "v_fma_f32 v11, v11, s40, v232\n"
    "v_mfma_f32_16x16x32_bf16 a[96:99], a[196:199], v[104:107], a[96:99]\n"
    "ds_read_b64_tr_b16 a[222:223], v227 offset:43520\n"
    "v_fma_f32 v24, v24, s40, v233\n"
    "v_mfma_f32_16x16x32_bf16 a[144:147], a[192:195], v[112:115], a[144:147]\n"
    "ds_read_b64_tr_b16 a[224:225], v226 offset:25600\n"
    "v_fma_f32 v25, v25, s40, v233\n"
    "v_mfma_f32_16x16x32_bf16 a[144:147], a[196:199], v[120:123], a[144:147]\n"
    "ds_read_b64_tr_b16 a[226:227], v226 offset:31744\n"
    "v_fma_f32 v26, v26, s40, v233\n"
    "v_mfma_f32_16x16x32_bf16 v[132:135], a[200:203], v[64:67], v[132:135]\n"
    "buffer_load_dwordx4 v229, s[20:23], s36 offen offset:1024 nt lds\n"
    "v_mfma_f32_16x16x32_bf16 v[132:135], a[204:207], v[72:75], v[132:135]\n"
    "ds_read_b64_tr_b16 a[228:229], v226 offset:37888\n"
    "v_fma_f32 v27, v27, s40, v233\n"
    "v_mfma_f32_16x16x32_bf16 v[180:183], a[200:203], v[80:83], v[180:183]\n"
    "ds_read_b64_tr_b16 a[230:231], v226 offset:44032\n"
    "v_fma_f32 v40, v40, s40, v234\n"
    "v_mfma_f32_16x16x32_bf16 v[180:183], a[204:207], v[88:91], v[180:183]\n"
    "s_mov_b32 m0, s53\n"
    "v_fma_f32 v41, v41, s40, v234\n"
    "v_mfma_f32_16x16x32_bf16 a[100:103], a[200:203], v[96:99], a[100:103]\n"
    "ds_read_b64_tr_b16 a[232:233], v227 offset:25600\n"
    "v_fma_f32 v42, v42, s40, v234\n"
    "v_mfma_f32_16x16x32_bf16 a[100:103], a[204:207], v[104:107], a[100:103]\n"
    "buffer_load_dwordx4 v228, s[20:23], s36 offen offset:128 nt lds\n"
    "ds_read_b64_tr_b16 a[234:235], v227 offset:31744\n"
    "v_mfma_f32_16x16x32_bf16 a[148:151], a[200:203], v[112:115], a[148:151]\n"
    "ds_read_b64_tr_b16 a[236:237], v227 offset:37888\n"
    "v_fma_f32 v43, v43, s40, v234\n"
    "v_mfma_f32_16x16x32_bf16 a[148:151], a[204:207], v[120:123], a[148:151]\n"
    "ds_read_b64_tr_b16 a[238:239], v227 offset:44032\n"
    "v_fma_f32 v56, v56, s40, v235\n"
    "v_mfma_f32_16x16x32_bf16 a[240:243], v[240:243], v[64:67], a[240:243]\n"
    "ds_read_b64_tr_b16 a[192:193], v226 offset:26112\n"
    "v_fma_f32 v57, v57, s40, v235\n"
    "v_mfma_f32_16x16x32_bf16 a[240:243], v[240:243], v[72:75], a[240:243]\n"
    "ds_read_b64_tr_b16 a[194:195], v226 offset:32256\n"
    "v_fma_f32 v58, v58, s40, v235\n"
    "v_mfma_f32_16x16x32_bf16 v[136:139], a[208:211], v[64:67], v[136:139]\n"
    "buffer_load_dwordx4 v229, s[20:23], s36 offen offset:1152 nt lds\n"
    "v_mfma_f32_16x16x32_bf16 v[136:139], a[212:215], v[72:75], v[136:139]\n"
    "ds_read_b64_tr_b16 a[196:197], v226 offset:38400\n"
    "v_fma_f32 v59, v59, s40, v235\n"
    "v_mfma_f32_16x16x32_bf16 v[184:187], a[208:211], v[80:83], v[184:187]\n"
    "ds_read_b64_tr_b16 a[198:199], v226 offset:44544\n"
    "v_fma_f32 v12, v12, s40, v232\n"
    "v_mfma_f32_16x16x32_bf16 v[184:187], a[212:215], v[88:91], v[184:187]\n"
    "s_mov_b32 m0, s54\n"
    "v_fma_f32 v13, v13, s40, v232\n"
    "v_mfma_f32_16x16x32_bf16 a[104:107], a[208:211], v[96:99], a[104:107]\n"
    "ds_read_b64_tr_b16 a[200:201], v227 offset:26112\n"
    "v_fma_f32 v14, v14, s40, v232\n"
    "v_mfma_f32_16x16x32_bf16 a[104:107], a[212:215], v[104:107], a[104:107]\n"
    "buffer_load_dwordx4 v228, s[20:23], s36 offen offset:256 nt lds\n"
    "v_mfma_f32_16x16x32_bf16 a[152:155], a[208:211], v[112:115], a[152:155]\n"
    "ds_read_b64_tr_b16 a[202:203], v227 offset:32256\n"
    "v_fma_f32 v15, v15, s40, v232\n"
    "v_mfma_f32_16x16x32_bf16 a[152:155], a[212:215], v[120:123], a[152:155]\n"
    "s_waitcnt lgkmcnt(6)\n"
    "v_fma_f32 v28, v28, s40, v233\n"
    "v_mfma_f32_16x16x32_bf16 v[140:143], a[216:219], v[64:67], v[140:143]\n"
    "ds_read_b64_tr_b16 a[204:205], v227 offset:38400\n"
    "v_fma_f32 v29, v29, s40, v233\n"
    "v_mfma_f32_16x16x32_bf16 v[140:143], a[220:223], v[72:75], v[140:143]\n"
    "ds_read_b64_tr_b16 a[206:207], v227 offset:44544\n"
    "v_fma_f32 v30, v30, s40, v233\n"
    "v_mfma_f32_16x16x32_bf16 v[188:191], a[216:219], v[80:83], v[188:191]\n"
    "buffer_load_dwordx4 v229, s[20:23], s36 offen offset:1280 nt lds\n"
    "v_mfma_f32_16x16x32_bf16 v[188:191], a[220:223], v[88:91], v[188:191]\n"
    "s_add_u32 s36, s36, 24576\n"
    "v_mfma_f32_16x16x32_bf16 a[108:111], a[216:219], v[96:99], a[108:111]\n"
    "ds_read_b64_tr_b16 a[208:209], v226 offset:26624\n"
    "v_mfma_f32_16x16x32_bf16 a[108:111], a[220:223], v[104:107], a[108:111]\n"
    "ds_read_b64_tr_b16 a[210:211], v226 offset:32768\n"
    "v_mfma_f32_16x16x32_bf16 a[156:159], a[216:219], v[112:115], a[156:159]\n"
    "ds_read_b64_tr_b16 a[212:213], v226 offset:38912\n"
    "v_mfma_f32_16x16x32_bf16 a[156:159], a[220:223], v[120:123], a[156:159]\n"
    "ds_read_b64_tr_b16 a[214:215], v226 offset:45056\n"
    "v_mfma_f32_16x16x32_bf16 v[144:147], a[224:227], v[64:67], v[144:147]\n"
    "ds_read_b64_tr_b16 a[216:217], v227 offset:26624\n"
    "v_mfma_f32_16x16x32_bf16 v[144:147], a[228:231], v[72:75], v[144:147]\n"
    "ds_read_b64_tr_b16 a[218:219], v227 offset:32768\n"
    "v_mfma_f32_16x16x32_bf16 v[192:195], a[224:227], v[80:83], v[192:195]\n"
    "ds_read_b64_tr_b16 a[220:221], v227 offset:38912\n"
    "v_mfma_f32_16x16x32_bf16 v[192:195], a[228:231], v[88:91], v[192:195]\n"
    "v_exp_f32 v8, v8\n"
    "v_mfma_f32_16x16x32_bf16 a[112:115], a[224:227], v[96:99], a[112:115]\n"
    "ds_read_b64_tr_b16 a[222:223], v227 offset:45056\n"
    "v_fma_f32 v31, v31, s40, v233\n"
    "v_mfma_f32_16x16x32_bf16 a[112:115], a[228:231], v[104:107], a[112:115]\n"
    "v_exp_f32 v9, v9\n"
    "v_mfma_f32_16x16x32_bf16 a[160:163], a[224:227], v[112:115], a[160:163]\n"
    "v_exp_f32 v10, v10\n"
    "v_mfma_f32_16x16x32_bf16 a[160:163], a[228:231], v[120:123], a[160:163]\n"
    "v_exp_f32 v11, v11\n"
    "v_mfma_f32_16x16x32_bf16 a[244:247], v[240:243], v[80:83], a[244:247]\n"
    "ds_read_b64_tr_b16 a[224:225], v226 offset:27136\n"
    "v_cvt_pk_bf16_f32 v8, v8, v9\n"
    "v_mfma_f32_16x16x32_bf16 a[244:247], v[240:243], v[88:91], a[244:247]\n"
    "v_exp_f32 v24, v24\n"
    "v_mfma_f32_16x16x32_bf16 v[148:151], a[232:235], v[64:67], v[148:151]\n"
    "ds_read_b64_tr_b16 a[226:227], v226 offset:33280\n"
    "v_cvt_pk_bf16_f32 v9, v10, v11\n"
    "v_mfma_f32_16x16x32_bf16 v[148:151], a[236:239], v[72:75], v[148:151]\n"
    "v_exp_f32 v25, v25\n"
    "v_mfma_f32_16x16x32_bf16 v[196:199], a[232:235], v[80:83], v[196:199]\n"
    "v_exp_f32 v26, v26\n"
    "v_mfma_f32_16x16x32_bf16 v[196:199], a[236:239], v[88:91], v[196:199]\n"
    "ds_read_b64_tr_b16 a[228:229], v226 offset:39424\n"
    "v_cvt_pk_bf16_f32 v24, v24, v25\n"
    "v_mfma_f32_16x16x32_bf16 a[116:119], a[232:235], v[96:99], a[116:119]\n"
    "v_exp_f32 v27, v27\n"
    "v_mfma_f32_16x16x32_bf16 a[116:119], a[236:239], v[104:107], a[116:119]\n"
    "v_exp_f32 v40, v40\n"
    "v_mfma_f32_16x16x32_bf16 a[164:167], a[232:235], v[112:115], a[164:167]\n"
    "ds_read_b64_tr_b16 a[230:231], v226 offset:45568\n"
    "v_cvt_pk_bf16_f32 v25, v26, v27\n"
    "v_mfma_f32_16x16x32_bf16 a[164:167], a[236:239], v[120:123], a[164:167]\n"
    "s_waitcnt lgkmcnt(4)\n"
    "v_fma_f32 v44, v44, s40, v234\n"
    "v_mfma_f32_16x16x32_bf16 v[152:155], a[192:195], v[64:67], v[152:155]\n"
    "v_exp_f32 v41, v41\n"
    "v_mfma_f32_16x16x32_bf16 v[152:155], a[196:199], v[72:75], v[152:155]\n"
    "ds_read_b64_tr_b16 a[232:233], v227 offset:27136\n"
    "v_cvt_pk_bf16_f32 v40, v40, v41\n"
    "v_mfma_f32_16x16x32_bf16 v[200:203], a[192:195], v[80:83], v[200:203]\n"
    "v_exp_f32 v42, v42\n"
    "v_mfma_f32_16x16x32_bf16 v[200:203], a[196:199], v[88:91], v[200:203]\n"
    "v_exp_f32 v43, v43\n"
    "v_mfma_f32_16x16x32_bf16 a[120:123], a[192:195], v[96:99], a[120:123]\n"
    "v_exp_f32 v56, v56\n"
    "v_mfma_f32_16x16x32_bf16 a[120:123], a[196:199], v[104:107], a[120:123]\n"
    "ds_read_b64_tr_b16 a[234:235], v227 offset:33280\n"
    "v_cvt_pk_bf16_f32 v41, v42, v43\n"
    "v_mfma_f32_16x16x32_bf16 a[168:171], a[192:195], v[112:115], a[168:171]\n"
    "v_exp_f32 v57, v57\n"
    "v_mfma_f32_16x16x32_bf16 a[168:171], a[196:199], v[120:123], a[168:171]\n"
    "v_exp_f32 v58, v58\n"
    "v_mfma_f32_16x16x32_bf16 v[156:159], a[200:203], v[64:67], v[156:159]\n"
    "v_exp_f32 v59, v59\n"
    "v_mfma_f32_16x16x32_bf16 v[156:159], a[204:207], v[72:75], v[156:159]\n"
    "v_exp_f32 v12, v12\n"
    "v_mfma_f32_16x16x32_bf16 v[204:207], a[200:203], v[80:83], v[204:207]\n"
    "ds_read_b64_tr_b16 a[236:237], v227 offset:39424\n"
    "v_cvt_pk_bf16_f32 v56, v56, v57\n"
    "v_mfma_f32_16x16x32_bf16 v[204:207], a[204:207], v[88:91], v[204:207]\n"
    "ds_read_b128 v[244:247], v251 offset:0\n"
    "v_cvt_pk_bf16_f32 v57, v58, v59\n"
    "v_mfma_f32_16x16x32_bf16 a[124:127], a[200:203], v[96:99], a[124:127]\n"
    "v_exp_f32 v13, v13\n"
    "v_mfma_f32_16x16x32_bf16 a[124:127], a[204:207], v[104:107], a[124:127]\n"
    "v_exp_f32 v14, v14\n"
    "v_mfma_f32_16x16x32_bf16 a[172:175], a[200:203], v[112:115], a[172:175]\n"
    "v_exp_f32 v15, v15\n"
    "v_mfma_f32_16x16x32_bf16 a[172:175], a[204:207], v[120:123], a[172:175]\n"
    "v_exp_f32 v28, v28\n"
    "v_mfma_f32_16x16x32_bf16 a[248:251], v[240:243], v[96:99], a[248:251]\n"
    "ds_read_b64_tr_b16 a[238:239], v227 offset:45568\n"
    "v_fma_f32 v45, v45, s40, v234\n"
    "v_mfma_f32_16x16x32_bf16 a[248:251], v[240:243], v[104:107], a[248:251]\n"
    "v_exp_f32 v29, v29\n"
    "v_mfma_f32_16x16x32_bf16 v[160:163], a[208:211], v[64:67], v[160:163]\n"
    "v_exp_f32 v30, v30\n"
    "v_mfma_f32_16x16x32_bf16 v[160:163], a[212:215], v[72:75], v[160:163]\n"
    "v_exp_f32 v31, v31\n"
    "v_mfma_f32_16x16x32_bf16 v[208:211], a[208:211], v[80:83], v[208:211]\n"
    "v_exp_f32 v44, v44\n"
    "v_mfma_f32_16x16x32_bf16 v[208:211], a[212:215], v[88:91], v[208:211]\n"
    "v_exp_f32 v45, v45\n"
    "v_mfma_f32_16x16x32_bf16 a[128:131], a[208:211], v[96:99], a[128:131]\n"
    "v_cvt_pk_bf16_f32 v10, v12, v13\n"
    "v_fma_f32 v46, v46, s40, v234\n"
    "v_mfma_f32_16x16x32_bf16 a[128:131], a[212:215], v[104:107], a[128:131]\n"
    "v_exp_f32 v46, v46\n"
    "v_mfma_f32_16x16x32_bf16 a[176:179], a[208:211], v[112:115], a[176:179]\n"
    "v_fma_f32 v47, v47, s40, v234\n"
    "v_cvt_pk_bf16_f32 v11, v14, v15\n"
    "v_mfma_f32_16x16x32_bf16 a[176:179], a[212:215], v[120:123], a[176:179]\n"
    "v_exp_f32 v47, v47\n"
    "v_mfma_f32_16x16x32_bf16 v[164:167], a[216:219], v[64:67], v[164:167]\n"
    "v_cvt_pk_bf16_f32 v26, v28, v29\n"
    "v_cvt_pk_bf16_f32 v27, v30, v31\n"
    "v_mfma_f32_16x16x32_bf16 v[164:167], a[220:223], v[72:75], v[164:167]\n"
    "s_waitcnt vmcnt(15) lgkmcnt(0)\n"
    "v_cvt_pk_bf16_f32 v42, v44, v45\n"
    "v_mfma_f32_16x16x32_bf16 v[212:215], a[216:219], v[80:83], v[212:215]\n"
    "s_barrier\n"
    "v_cvt_pk_bf16_f32 v43, v46, v47\n"
    "v_mfma_f32_16x16x32_bf16 v[212:215], a[220:223], v[88:91], v[212:215]\n"
    "ds_read_b128 a[192:195], v224 offset:24576\n"
    "v_mfma_f32_16x16x32_bf16 a[132:135], a[216:219], v[96:99], a[132:135]\n"
    "ds_read_b128 a[196:199], v225 offset:24576\n"
    "v_mfma_f32_16x16x32_bf16 a[132:135], a[220:223], v[104:107], a[132:135]\n"
    "ds_read_b128 a[200:203], v224 offset:32768\n"
    "v_mfma_f32_16x16x32_bf16 a[180:183], a[216:219], v[112:115], a[180:183]\n"
    "ds_read_b128 a[204:207], v225 offset:32768\n"
    "v_mfma_f32_16x16x32_bf16 a[180:183], a[220:223], v[120:123], a[180:183]\n"
    "buffer_store_dwordx4 v[244:247], v250, s[84:87], s78 offen offset:256 nt sc1\n"
    "v_mfma_f32_16x16x32_bf16 v[168:171], a[224:227], v[64:67], v[168:171]\n"
    "ds_read_b128 a[208:211], v224 offset:40960\n"
    "v_mfma_f32_16x16x32_bf16 v[168:171], a[228:231], v[72:75], v[168:171]\n"
    "v_mfma_f32_16x16x32_bf16 v[216:219], a[224:227], v[80:83], v[216:219]\n"
    "v_mfma_f32_16x16x32_bf16 v[216:219], a[228:231], v[88:91], v[216:219]\n"
    "ds_read_b128 a[212:215], v225 offset:40960\n"
    "v_mfma_f32_16x16x32_bf16 a[136:139], a[224:227], v[96:99], a[136:139]\n"
    "v_mfma_f32_16x16x32_bf16 a[136:139], a[228:231], v[104:107], a[136:139]\n"
    "ds_read_b128 a[216:219], v224 offset:26624\n"
    "v_mfma_f32_16x16x32_bf16 a[184:187], a[224:227], v[112:115], a[184:187]\n"
    "v_mfma_f32_16x16x32_bf16 a[184:187], a[228:231], v[120:123], a[184:187]\n"
    "ds_read_b128 a[220:223], v225 offset:26624\n"
    "v_mfma_f32_16x16x32_bf16 a[252:255], v[240:243], v[112:115], a[252:255]\n"
    "v_mfma_f32_16x16x32_bf16 a[252:255], v[240:243], v[120:123], a[252:255]\n"
    "v_mfma_f32_16x16x32_bf16 v[172:175], a[232:235], v[64:67], v[172:175]\n"
    "ds_read_b128 a[224:227], v224 offset:34816\n"
    "v_mfma_f32_16x16x32_bf16 v[172:175], a[236:239], v[72:75], v[172:175]\n"
    "v_mfma_f32_16x16x32_bf16 v[220:223], a[232:235], v[80:83], v[220:223]\n"
    "v_mfma_f32_16x16x32_bf16 v[220:223], a[236:239], v[88:91], v[220:223]\n"
    "v_mfma_f32_16x16x32_bf16 a[140:143], a[232:235], v[96:99], a[140:143]\n"
    "v_mfma_f32_16x16x32_bf16 a[140:143], a[236:239], v[104:107], a[140:143]\n"
    "ds_read_b128 a[228:231], v225 offset:34816\n"
    "v_mfma_f32_16x16x32_bf16 a[188:191], a[232:235], v[112:115], a[188:191]\n"
    "v_mfma_f32_16x16x32_bf16 a[188:191], a[236:239], v[120:123], a[188:191]\n"
    "s_waitcnt lgkmcnt(4)\n"
    "v_mfma_f32_16x16x32_bf16 v[64:67], a[192:195], a[0:3], 0\n"
    "v_fma_f32 v60, v60, s40, v235\n"
    "v_fma_f32 v61, v61, s40, v235\n"
    "v_mfma_f32_16x16x32_bf16 v[64:67], a[196:199], a[4:7], v[64:67]\n"
    "s_mov_b32 m0, s61\n"
    "v_fma_f32 v62, v62, s40, v235\n"
    "v_mfma_f32_16x16x32_bf16 v[64:67], a[200:203], a[8:11], v[64:67]\n"
    "v_exp_f32 v60, v60\n"
    "v_mfma_f32_16x16x32_bf16 v[64:67], a[204:207], a[12:15], v[64:67]\n"
    "buffer_load_dwordx4 v230, s[24:27], s37 offen offset:0 nt lds\n"
    "v_mfma_f32_16x16x32_bf16 v[64:67], a[208:211], a[16:19], v[64:67]\n"
    "ds_read_b128 a[232:235], v224 offset:43008\n"
    "v_fma_f32 v63, v63, s40, v235\n"
    "v_mfma_f32_16x16x32_bf16 v[64:67], a[212:215], a[20:23], v[64:67]\n"
    "v_exp_f32 v61, v61\n"
    "v_mfma_f32_16x16x32_bf16 v[80:83], a[192:195], a[24:27], 0\n"
    "v_exp_f32 v62, v62\n"
    "v_mfma_f32_16x16x32_bf16 v[80:83], a[196:199], a[28:31], v[80:83]\n"
    "v_exp_f32 v63, v63\n"
    "v_mfma_f32_16x16x32_bf16 v[80:83], a[200:203], a[32:35], v[80:83]\n"
    "buffer_load_dwordx4 v230, s[24:27], s37 offen offset:3072 nt lds\n"
    "v_mfma_f32_16x16x32_bf16 v[80:83], a[204:207], a[36:39], v[80:83]\n"
    "ds_read_b128 a[236:239], v225 offset:43008\n"
    "v_cvt_pk_bf16_f32 v58, v60, v61\n"
    "v_mfma_f32_16x16x32_bf16 v[80:83], a[208:211], a[40:43], v[80:83]\n"
    "v_cvt_pk_bf16_f32 v59, v62, v63\n"
    "v_fma_f32 v64, v64, s40, v232\n"
    "v_mfma_f32_16x16x32_bf16 v[80:83], a[212:215], a[44:47], v[80:83]\n"
    "s_mov_b32 m0, s62\n"
    "v_fma_f32 v65, v65, s40, v232\n"
    "v_mfma_f32_16x16x32_bf16 v[96:99], a[192:195], a[48:51], 0\n"
    "v_exp_f32 v64, v64\n"
    "v_mfma_f32_16x16x32_bf16 v[96:99], a[196:199], a[52:55], v[96:99]\n"
    "buffer_load_dwordx4 v230, s[24:27], s37 offen offset:128 nt lds\n"
    "v_mfma_f32_16x16x32_bf16 v[96:99], a[200:203], a[56:59], v[96:99]\n"
    "v_exp_f32 v65, v65\n"
    "v_mfma_f32_16x16x32_bf16 v[96:99], a[204:207], a[60:63], v[96:99]\n"
    "v_fma_f32 v66, v66, s40, v232\n"
    "v_fma_f32 v67, v67, s40, v232\n"
    "v_mfma_f32_16x16x32_bf16 v[96:99], a[208:211], a[64:67], v[96:99]\n"
    "v_exp_f32 v66, v66\n"
    "v_mfma_f32_16x16x32_bf16 v[96:99], a[212:215], a[68:71], v[96:99]\n"
    "v_exp_f32 v67, v67\n"
    "v_mfma_f32_16x16x32_bf16 v[112:115], a[192:195], a[72:75], 0\n"
    "buffer_load_dwordx4 v230, s[24:27], s37 offen offset:3200 nt lds\n"
    "v_mfma_f32_16x16x32_bf16 v[112:115], a[196:199], a[76:79], v[112:115]\n"
    "ds_read_b128 a[192:195], v224 offset:28672\n"
    "v_fma_f32 v80, v80, s40, v233\n"
    "v_mfma_f32_16x16x32_bf16 v[112:115], a[200:203], a[80:83], v[112:115]\n"
    "v_exp_f32 v80, v80\n"
    "v_mfma_f32_16x16x32_bf16 v[112:115], a[204:207], a[84:87], v[112:115]\n"
    "s_mov_b32 m0, s63\n"
    "v_fma_f32 v81, v81, s40, v233\n"
    "v_mfma_f32_16x16x32_bf16 v[112:115], a[208:211], a[88:91], v[112:115]\n"
    "ds_read_b128 a[196:199], v225 offset:28672\n"
    "v_fma_f32 v82, v82, s40, v233\n"
    "v_mfma_f32_16x16x32_bf16 v[112:115], a[212:215], a[92:95], v[112:115]\n"
    "s_waitcnt lgkmcnt(2)\n"
    "v_fma_f32 v83, v83, s40, v233\n"
    "v_mfma_f32_16x16x32_bf16 v[68:71], a[216:219], a[0:3], 0\n"
    "buffer_load_dwordx4 v230, s[24:27], s37 offen offset:256 nt lds\n"
    "v_mfma_f32_16x16x32_bf16 v[68:71], a[220:223], a[4:7], v[68:71]\n"
    "ds_read_b128 a[200:203], v224 offset:36864\n"
    "v_fma_f32 v96, v96, s40, v234\n"
    "v_mfma_f32_16x16x32_bf16 v[68:71], a[224:227], a[8:11], v[68:71]\n"
    "v_exp_f32 v81, v81\n"
    "v_mfma_f32_16x16x32_bf16 v[68:71], a[228:231], a[12:15], v[68:71]\n"
    "ds_read_b128 a[204:207], v225 offset:36864\n"
    "v_fma_f32 v97, v97, s40, v234\n"
    "v_mfma_f32_16x16x32_bf16 v[68:71], a[232:235], a[16:19], v[68:71]\n"
    "v_exp_f32 v82, v82\n"
    "v_mfma_f32_16x16x32_bf16 v[68:71], a[236:239], a[20:23], v[68:71]\n"
    "buffer_load_dwordx4 v230, s[24:27], s37 offen offset:3328 nt lds\n"
    "v_mfma_f32_16x16x32_bf16 v[84:87], a[216:219], a[24:27], 0\n"
    "s_add_u32 s37, s37, 24576\n"
    "v_cvt_pk_bf16_f32 v64, v64, v65\n"
    "v_mfma_f32_16x16x32_bf16 v[84:87], a[220:223], a[28:31], v[84:87]\n"
    "ds_read_b128 a[208:211], v224 offset:45056\n"
    "v_fma_f32 v98, v98, s40, v234\n"
    "v_mfma_f32_16x16x32_bf16 v[84:87], a[224:227], a[32:35], v[84:87]\n"
    "ds_read_b128 v[244:247], v251 offset:4096\n"
    "v_fma_f32 v99, v99, s40, v234\n"
    "v_mfma_f32_16x16x32_bf16 v[84:87], a[228:231], a[36:39], v[84:87]\n"
    "ds_read_b128 a[212:215], v225 offset:45056\n"
    "v_cvt_pk_bf16_f32 v65, v66, v67\n"
    "v_mfma_f32_16x16x32_bf16 v[84:87], a[232:235], a[40:43], v[84:87]\n"
    "v_exp_f32 v83, v83\n"
    "v_mfma_f32_16x16x32_bf16 v[84:87], a[236:239], a[44:47], v[84:87]\n"
    "v_exp_f32 v96, v96\n"
    "v_mfma_f32_16x16x32_bf16 v[100:103], a[216:219], a[48:51], 0\n"
    "v_exp_f32 v97, v97\n"
    "v_mfma_f32_16x16x32_bf16 v[100:103], a[220:223], a[52:55], v[100:103]\n"
    "v_exp_f32 v98, v98\n"
    "v_mfma_f32_16x16x32_bf16 v[100:103], a[224:227], a[56:59], v[100:103]\n"
    "s_add_u32 m0, s39, 8064\n"
    "v_fma_f32 v112, v112, s40, v235\n"
    "v_mfma_f32_16x16x32_bf16 v[100:103], a[228:231], a[60:63], v[100:103]\n"
    "v_exp_f32 v99, v99\n"
    "v_mfma_f32_16x16x32_bf16 v[100:103], a[232:235], a[64:67], v[100:103]\n"
    "buffer_load_dwordx4 v228, s[28:31], s38 offen offset:128 nt lds\n"
    "v_mfma_f32_16x16x32_bf16 v[100:103], a[236:239], a[68:71], v[100:103]\n"
    "v_exp_f32 v112, v112\n"
    "v_mfma_f32_16x16x32_bf16 v[116:119], a[216:219], a[72:75], 0\n"
    "v_fma_f32 v113, v113, s40, v235\n"
    "v_cvt_pk_bf16_f32 v80, v80, v81\n"
    "v_mfma_f32_16x16x32_bf16 v[116:119], a[220:223], a[76:79], v[116:119]\n"
    "ds_read_b128 a[216:219], v224 offset:30720\n"
    "v_fma_f32 v114, v114, s40, v235\n"
    "v_mfma_f32_16x16x32_bf16 v[116:119], a[224:227], a[80:83], v[116:119]\n"
    "ds_read_b128 a[220:223], v225 offset:30720\n"
    "v_fma_f32 v115, v115, s40, v235\n"
    "v_mfma_f32_16x16x32_bf16 v[116:119], a[228:231], a[84:87], v[116:119]\n"
    "buffer_load_dwordx4 v229, s[28:31], s38 offen offset:1152 nt lds\n"
    "v_mfma_f32_16x16x32_bf16 v[116:119], a[232:235], a[88:91], v[116:119]\n"
    "s_add_u32 s39, s39, 2048\n"
    "ds_read_b128 a[224:227], v224 offset:38912\n"
    "v_mfma_f32_16x16x32_bf16 v[116:119], a[236:239], a[92:95], v[116:119]\n"
    "s_waitcnt lgkmcnt(3)\n"
    "v_cvt_pk_bf16_f32 v81, v82, v83\n"
    "v_mfma_f32_16x16x32_bf16 v[72:75], a[192:195], a[0:3], 0\n"
    "s_add_u32 s38, s38, 6144\n"
    "v_fma_f32 v68, v68, s40, v232\n"
    "v_mfma_f32_16x16x32_bf16 v[72:75], a[196:199], a[4:7], v[72:75]\n"
    "ds_read_b128 a[228:231], v225 offset:38912\n"
    "v_fma_f32 v69, v69, s40, v232\n"
    "v_mfma_f32_16x16x32_bf16 v[72:75], a[200:203], a[8:11], v[72:75]\n"
    "buffer_store_dwordx4 v[244:247], v250, s[84:87], s79 offen offset:256 nt sc1\n"
    "v_mfma_f32_16x16x32_bf16 v[72:75], a[204:207], a[12:15], v[72:75]\n"
    "ds_read_b128 a[232:235], v224 offset:47104\n"
    "v_cvt_pk_bf16_f32 v96, v96, v97\n"
    "v_mfma_f32_16x16x32_bf16 v[72:75], a[208:211], a[16:19], v[72:75]\n"
    "s_add_u32 s78, s78, 3072\n"
    "v_fma_f32 v70, v70, s40, v232\n"
    "v_mfma_f32_16x16x32_bf16 v[72:75], a[212:215], a[20:23], v[72:75]\n"
    "s_add_u32 s79, s79, 3072\n"
    "v_fma_f32 v71, v71, s40, v232\n"
    "v_mfma_f32_16x16x32_bf16 v[88:91], a[192:195], a[24:27], 0\n"
    "ds_read_b128 a[236:239], v225 offset:47104\n"
    "v_cvt_pk_bf16_f32 v97, v98, v99\n"
    "v_mfma_f32_16x16x32_bf16 v[88:91], a[196:199], a[28:31], v[88:91]\n"
    "v_exp_f32 v113, v113\n"
    "v_mfma_f32_16x16x32_bf16 v[88:91], a[200:203], a[32:35], v[88:91]\n"
    "v_exp_f32 v114, v114\n"
    "v_mfma_f32_16x16x32_bf16 v[88:91], a[204:207], a[36:39], v[88:91]\n"
    "v_exp_f32 v115, v115\n"
    "v_mfma_f32_16x16x32_bf16 v[88:91], a[208:211], a[40:43], v[88:91]\n"
    "v_exp_f32 v68, v68\n"
    "v_mfma_f32_16x16x32_bf16 v[88:91], a[212:215], a[44:47], v[88:91]\n"
    "v_exp_f32 v69, v69\n"
    "v_mfma_f32_16x16x32_bf16 v[104:107], a[192:195], a[48:51], 0\n"
    "v_exp_f32 v70, v70\n"
    "v_mfma_f32_16x16x32_bf16 v[104:107], a[196:199], a[52:55], v[104:107]\n"
    "v_exp_f32 v71, v71\n"
    "v_mfma_f32_16x16x32_bf16 v[104:107], a[200:203], a[56:59], v[104:107]\n"
    "v_fma_f32 v84, v84, s40, v233\n"
    "v_fma_f32 v85, v85, s40, v233\n"
    "v_mfma_f32_16x16x32_bf16 v[104:107], a[204:207], a[60:63], v[104:107]\n"
    "v_exp_f32 v84, v84\n"
    "v_mfma_f32_16x16x32_bf16 v[104:107], a[208:211], a[64:67], v[104:107]\n"
    "v_exp_f32 v85, v85\n"
    "v_mfma_f32_16x16x32_bf16 v[104:107], a[212:215], a[68:71], v[104:107]\n"
    "s_waitcnt vmcnt(19) lgkmcnt(0)\n"
    "v_cvt_pk_bf16_f32 v112, v112, v113\n"
    "v_mfma_f32_16x16x32_bf16 v[120:123], a[192:195], a[72:75], 0\n"
    "s_barrier\n"
    "v_fma_f32 v86, v86, s40, v233\n"
    "v_mfma_f32_16x16x32_bf16 v[120:123], a[196:199], a[76:79], v[120:123]\n"
    "ds_read_b64_tr_b16 a[192:193], v226 offset:0\n"
    "v_fma_f32 v87, v87, s40, v233\n"
    "v_mfma_f32_16x16x32_bf16 v[120:123], a[200:203], a[80:83], v[120:123]\n"
    "ds_read_b64_tr_b16 a[194:195], v226 offset:6144\n"
    "v_cvt_pk_bf16_f32 v113, v114, v115\n"
    "v_mfma_f32_16x16x32_bf16 v[120:123], a[204:207], a[84:87], v[120:123]\n"
    "ds_read_b64_tr_b16 a[196:197], v226 offset:12288\n"
    "v_fma_f32 v100, v100, s40, v234\n"
    "v_mfma_f32_16x16x32_bf16 v[120:123], a[208:211], a[88:91], v[120:123]\n"
    "ds_read_b64_tr_b16 a[198:199], v226 offset:18432\n"
    "v_fma_f32 v101, v101, s40, v234\n"
    "v_mfma_f32_16x16x32_bf16 v[120:123], a[212:215], a[92:95], v[120:123]\n"
    "ds_read_b64_tr_b16 a[200:201], v227 offset:0\n"
    "v_cvt_pk_bf16_f32 v66, v68, v69\n"
    "v_mfma_f32_16x16x32_bf16 v[76:79], a[216:219], a[0:3], 0\n"
    "ds_read_b64_tr_b16 a[202:203], v227 offset:6144\n"
    "v_fma_f32 v102, v102, s40, v234\n"
    "v_mfma_f32_16x16x32_bf16 v[76:79], a[220:223], a[4:7], v[76:79]\n"
    "ds_read_b64_tr_b16 a[204:205], v227 offset:12288\n"
    "v_fma_f32 v103, v103, s40, v234\n"
    "v_mfma_f32_16x16x32_bf16 v[76:79], a[224:227], a[8:11], v[76:79]\n"
    "ds_read_b64_tr_b16 a[206:207], v227 offset:18432\n"
    "v_cvt_pk_bf16_f32 v67, v70, v71\n"
    "v_mfma_f32_16x16x32_bf16 v[76:79], a[228:231], a[12:15], v[76:79]\n"
    "ds_read_b64_tr_b16 a[208:209], v226 offset:512\n"
    "v_fma_f32 v116, v116, s40, v235\n"
    "v_mfma_f32_16x16x32_bf16 v[76:79], a[232:235], a[16:19], v[76:79]\n"
    "v_exp_f32 v86, v86\n"
    "v_mfma_f32_16x16x32_bf16 v[76:79], a[236:239], a[20:23], v[76:79]\n"
    "ds_read_b64_tr_b16 a[210:211], v226 offset:6656\n"
    "v_fma_f32 v117, v117, s40, v235\n"
    "v_mfma_f32_16x16x32_bf16 v[92:95], a[216:219], a[24:27], 0\n"
    "v_exp_f32 v87, v87\n"
    "v_mfma_f32_16x16x32_bf16 v[92:95], a[220:223], a[28:31], v[92:95]\n"
    "v_exp_f32 v100, v100\n"
    "v_mfma_f32_16x16x32_bf16 v[92:95], a[224:227], a[32:35], v[92:95]\n"
    "ds_read_b64_tr_b16 a[212:213], v226 offset:12800\n"
    "v_cvt_pk_bf16_f32 v82, v84, v85\n"
    "v_mfma_f32_16x16x32_bf16 v[92:95], a[228:231], a[36:39], v[92:95]\n"
    "v_exp_f32 v101, v101\n"
    "v_mfma_f32_16x16x32_bf16 v[92:95], a[232:235], a[40:43], v[92:95]\n"
    "v_exp_f32 v102, v102\n"
    "v_mfma_f32_16x16x32_bf16 v[92:95], a[236:239], a[44:47], v[92:95]\n"
    "v_exp_f32 v103, v103\n"
    "v_mfma_f32_16x16x32_bf16 v[108:111], a[216:219], a[48:51], 0\n"
    "ds_read_b64_tr_b16 a[214:215], v226 offset:18944\n"
    "v_fma_f32 v118, v118, s40, v235\n"
    "v_mfma_f32_16x16x32_bf16 v[108:111], a[220:223], a[52:55], v[108:111]\n"
    "v_exp_f32 v116, v116\n"
    "v_mfma_f32_16x16x32_bf16 v[108:111], a[224:227], a[56:59], v[108:111]\n"
    "s_sub_u32 s45, s45, 1\n"
    "v_fma_f32 v119, v119, s40, v235\n"
    "v_mfma_f32_16x16x32_bf16 v[108:111], a[228:231], a[60:63], v[108:111]\n"
    "v_exp_f32 v117, v117\n"
    "v_mfma_f32_16x16x32_bf16 v[108:111], a[232:235], a[64:67], v[108:111]\n"
    "v_exp_f32 v118, v118\n"
    "v_mfma_f32_16x16x32_bf16 v[108:111], a[236:239], a[68:71], v[108:111]\n"
    "v_exp_f32 v119, v119\n"
    "v_mfma_f32_16x16x32_bf16 v[124:127], a[216:219], a[72:75], 0\n"
    "v_cvt_pk_bf16_f32 v83, v86, v87\n"
    "v_cvt_pk_bf16_f32 v98, v100, v101\n"
    "v_mfma_f32_16x16x32_bf16 v[124:127], a[220:223], a[76:79], v[124:127]\n"
    "v_cvt_pk_bf16_f32 v99, v102, v103\n"
    "v_cvt_pk_bf16_f32 v114, v116, v117\n"
    "v_mfma_f32_16x16x32_bf16 v[124:127], a[224:227], a[80:83], v[124:127]\n"
    "v_cvt_pk_bf16_f32 v115, v118, v119\n"
    "v_add_u32 v251, 0x400, v251\n"
    "v_mfma_f32_16x16x32_bf16 v[124:127], a[228:231], a[84:87], v[124:127]\n"
    "v_mfma_f32_16x16x32_bf16 v[124:127], a[232:235], a[88:91], v[124:127]\n"
    "v_mfma_f32_16x16x32_bf16 v[124:127], a[236:239], a[92:95], v[124:127]\n"
    "s_waitcnt lgkmcnt(0)\n"
    "s_cbranch_scc1 LB6_%=\n"
    "v_mfma_f32_16x16x32_bf16 v[128:131], a[192:195], v[0:3], v[128:131]\n"
    "ds_read_b64_tr_b16 a[216:217], v227 offset:512\n"
    "v_fma_f32 v72, v72, s40, v232\n"
    "v_mfma_f32_16x16x32_bf16 v[128:131], a[196:199], v[8:11], v[128:131]\n"
    "s_mov_b32 m0, s55\n"
    "v_fma_f32 v73, v73, s40, v232\n"
    "v_mfma_f32_16x16x32_bf16 v[176:179], a[192:195], v[16:19], v[176:179]\n"
    "ds_read_b64_tr_b16 a[218:219], v227 offset:6656\n"
    "v_fma_f32 v74, v74, s40, v232\n"
    "v_mfma_f32_16x16x32_bf16 v[176:179], a[196:199], v[24:27], v[176:179]\n"
    "buffer_load_dwordx4 v228, s[20:23], s36 offen offset:0 nt lds\n"
    "v_mfma_f32_16x16x32_bf16 a[96:99], a[192:195], v[32:35], a[96:99]\n"
    "ds_read_b64_tr_b16 a[220:221], v227 offset:12800\n"
    "v_fma_f32 v75, v75, s40, v232\n"
    "v_mfma_f32_16x16x32_bf16 a[96:99], a[196:199], v[40:43], a[96:99]\n"
    "ds_read_b64_tr_b16 a[222:223], v227 offset:18944\n"
    "v_fma_f32 v88, v88, s40, v233\n"
    "v_mfma_f32_16x16x32_bf16 a[144:147], a[192:195], v[48:51], a[144:147]\n"
    "ds_read_b64_tr_b16 a[224:225], v226 offset:1024\n"
    "v_fma_f32 v89, v89, s40, v233\n"
    "v_mfma_f32_16x16x32_bf16 a[144:147], a[196:199], v[56:59], a[144:147]\n"
    "ds_read_b64_tr_b16 a[226:227], v226 offset:7168\n"
    "v_fma_f32 v90, v90, s40, v233\n"
    "v_mfma_f32_16x16x32_bf16 v[132:135], a[200:203], v[0:3], v[132:135]\n"
    "buffer_load_dwordx4 v229, s[20:23], s36 offen offset:1024 nt lds\n"
    "v_mfma_f32_16x16x32_bf16 v[132:135], a[204:207], v[8:11], v[132:135]\n"
    "ds_read_b64_tr_b16 a[228:229], v226 offset:13312\n"
    "v_fma_f32 v91, v91, s40, v233\n"
    "v_mfma_f32_16x16x32_bf16 v[180:183], a[200:203], v[16:19], v[180:183]\n"
    "ds_read_b64_tr_b16 a[230:231], v226 offset:19456\n"
    "v_fma_f32 v104, v104, s40, v234\n"
    "v_mfma_f32_16x16x32_bf16 v[180:183], a[204:207], v[24:27], v[180:183]\n"
    "s_mov_b32 m0, s56\n"
    "v_fma_f32 v105, v105, s40, v234\n"
    "v_mfma_f32_16x16x32_bf16 a[100:103], a[200:203], v[32:35], a[100:103]\n"
    "ds_read_b64_tr_b16 a[232:233], v227 offset:1024\n"
    "v_fma_f32 v106, v106, s40, v234\n"
    "v_mfma_f32_16x16x32_bf16 a[100:103], a[204:207], v[40:43], a[100:103]\n"
    "buffer_load_dwordx4 v228, s[20:23], s36 offen offset:128 nt lds\n"
    "ds_read_b64_tr_b16 a[234:235], v227 offset:7168\n"
    "v_mfma_f32_16x16x32_bf16 a[148:151], a[200:203], v[48:51], a[148:151]\n"
    "ds_read_b64_tr_b16 a[236:237], v227 offset:13312\n"
    "v_fma_f32 v107, v107, s40, v234\n"
    "v_mfma_f32_16x16x32_bf16 a[148:151], a[204:207], v[56:59], a[148:151]\n"
    "ds_read_b64_tr_b16 a[238:239], v227 offset:19456\n"
    "v_fma_f32 v120, v120, s40, v235\n"
    "v_mfma_f32_16x16x32_bf16 a[240:243], v[240:243], v[0:3], a[240:243]\n"
    "ds_read_b64_tr_b16 a[192:193], v226 offset:1536\n"
    "v_fma_f32 v121, v121, s40, v235\n"
    "v_mfma_f32_16x16x32_bf16 a[240:243], v[240:243], v[8:11], a[240:243]\n"
    "ds_read_b64_tr_b16 a[194:195], v226 offset:7680\n"
    "v_fma_f32 v122, v122, s40, v235\n"
    "v_mfma_f32_16x16x32_bf16 v[136:139], a[208:211], v[0:3], v[136:139]\n"
    "buffer_load_dwordx4 v229, s[20:23], s36 offen offset:1152 nt lds\n"
    "v_mfma_f32_16x16x32_bf16 v[136:139], a[212:215], v[8:11], v[136:139]\n"
    "ds_read_b64_tr_b16 a[196:197], v226 offset:13824\n"
    "v_fma_f32 v123, v123, s40, v235\n"
    "v_mfma_f32_16x16x32_bf16 v[184:187], a[208:211], v[16:19], v[184:187]\n"
    "ds_read_b64_tr_b16 a[198:199], v226 offset:19968\n"
    "v_fma_f32 v76, v76, s40, v232\n"
    "v_mfma_f32_16x16x32_bf16 v[184:187], a[212:215], v[24:27], v[184:187]\n"
    "s_mov_b32 m0, s57\n"
    "v_fma_f32 v77, v77, s40, v232\n"
    "v_mfma_f32_16x16x32_bf16 a[104:107], a[208:211], v[32:35], a[104:107]\n"
    "ds_read_b64_tr_b16 a[200:201], v227 offset:1536\n"
    "v_fma_f32 v78, v78, s40, v232\n"
    "v_mfma_f32_16x16x32_bf16 a[104:107], a[212:215], v[40:43], a[104:107]\n"
    "buffer_load_dwordx4 v228, s[20:23], s36 offen offset:256 nt lds\n"
    "v_mfma_f32_16x16x32_bf16 a[152:155], a[208:211], v[48:51], a[152:155]\n"
    "ds_read_b64_tr_b16 a[202:203], v227 offset:7680\n"
    "v_fma_f32 v79, v79, s40, v232\n"
    "v_mfma_f32_16x16x32_bf16 a[152:155], a[212:215], v[56:59], a[152:155]\n"
    "s_waitcnt lgkmcnt(6)\n"
    "v_fma_f32 v92, v92, s40, v233\n"
    "v_mfma_f32_16x16x32_bf16 v[140:143], a[216:219], v[0:3], v[140:143]\n"
    "ds_read_b64_tr_b16 a[204:205], v227 offset:13824\n"
    "v_fma_f32 v93, v93, s40, v233\n"
    "v_mfma_f32_16x16x32_bf16 v[140:143], a[220:223], v[8:11], v[140:143]\n"
    "ds_read_b64_tr_b16 a[206:207], v227 offset:19968\n"
    "v_fma_f32 v94, v94, s40, v233\n"
    "v_mfma_f32_16x16x32_bf16 v[188:191], a[216:219], v[16:19], v[188:191]\n"
    "buffer_load_dwordx4 v229, s[20:23], s36 offen offset:1280 nt lds\n"
    "v_mfma_f32_16x16x32_bf16 v[188:191], a[220:223], v[24:27], v[188:191]\n"
    "s_add_u32 s36, s36, 24576\n"
    "v_mfma_f32_16x16x32_bf16 a[108:111], a[216:219], v[32:35], a[108:111]\n"
    "ds_read_b64_tr_b16 a[208:209], v226 offset:2048\n"
    "v_mfma_f32_16x16x32_bf16 a[108:111], a[220:223], v[40:43], a[108:111]\n"
    "ds_read_b64_tr_b16 a[210:211], v226 offset:8192\n"
    "v_mfma_f32_16x16x32_bf16 a[156:159], a[216:219], v[48:51], a[156:159]\n"
    "ds_read_b64_tr_b16 a[212:213], v226 offset:14336\n"
    "v_mfma_f32_16x16x32_bf16 a[156:159], a[220:223], v[56:59], a[156:159]\n"
    "ds_read_b64_tr_b16 a[214:215], v226 offset:20480\n"
    "v_mfma_f32_16x16x32_bf16 v[144:147], a[224:227], v[0:3], v[144:147]\n"
    "ds_read_b64_tr_b16 a[216:217], v227 offset:2048\n"
    "v_mfma_f32_16x16x32_bf16 v[144:147], a[228:231], v[8:11], v[144:147]\n"
    "ds_read_b64_tr_b16 a[218:219], v227 offset:8192\n"
    "v_mfma_f32_16x16x32_bf16 v[192:195], a[224:227], v[16:19], v[192:195]\n"
    "ds_read_b64_tr_b16 a[220:221], v227 offset:14336\n"
    "v_mfma_f32_16x16x32_bf16 v[192:195], a[228:231], v[24:27], v[192:195]\n"
    "v_exp_f32 v72, v72\n"
    "v_mfma_f32_16x16x32_bf16 a[112:115], a[224:227], v[32:35], a[112:115]\n"
    "s_mov_b32 m0, s17\n"
    "v_fma_f32 v95, v95, s40, v233\n"
    "v_mfma_f32_16x16x32_bf16 a[112:115], a[228:231], v[40:43], a[112:115]\n"
    "ds_read_b64_tr_b16 a[222:223], v227 offset:20480\n"
    "v_mfma_f32_16x16x32_bf16 a[160:163], a[224:227], v[48:51], a[160:163]\n"
    "buffer_load_dwordx4 v228, s[28:31], s18 offen offset:0 nt lds\n"
    "v_mfma_f32_16x16x32_bf16 a[160:163], a[228:231], v[56:59], a[160:163]\n"
    "v_exp_f32 v73, v73\n"
    "v_mfma_f32_16x16x32_bf16 a[244:247], v[240:243], v[16:19], a[244:247]\n"
    "ds_read_b64_tr_b16 a[224:225], v226 offset:2560\n"
    "v_cvt_pk_bf16_f32 v72, v72, v73\n"
    "v_mfma_f32_16x16x32_bf16 a[244:247], v[240:243], v[24:27], a[244:247]\n"
    "v_exp_f32 v74, v74\n"
    "v_mfma_f32_16x16x32_bf16 v[148:151], a[232:235], v[0:3], v[148:151]\n"
    "ds_read_b64_tr_b16 a[226:227], v226 offset:8704\n"
    "v_fma_f32 v108, v108, s40, v234\n"
    "v_mfma_f32_16x16x32_bf16 v[148:151], a[236:239], v[8:11], v[148:151]\n"
    "buffer_load_dwordx4 v229, s[28:31], s18 offen offset:1024 nt lds\n"
    "v_mfma_f32_16x16x32_bf16 v[196:199], a[232:235], v[16:19], v[196:199]\n"
    "s_add_u32 s17, s17, 8192\n"
    "v_fma_f32 v109, v109, s40, v234\n"
    "v_mfma_f32_16x16x32_bf16 v[196:199], a[236:239], v[24:27], v[196:199]\n"
    "s_add_u32 s18, s18, 128\n"
    "v_mfma_f32_16x16x32_bf16 a[116:119], a[232:235], v[32:35], a[116:119]\n"
    "ds_read_b64_tr_b16 a[228:229], v226 offset:14848\n"
    "v_mfma_f32_16x16x32_bf16 a[116:119], a[236:239], v[40:43], a[116:119]\n"
    "v_exp_f32 v75, v75\n"
    "v_mfma_f32_16x16x32_bf16 a[164:167], a[232:235], v[48:51], a[164:167]\n"
    "v_exp_f32 v88, v88\n"
    "v_mfma_f32_16x16x32_bf16 a[164:167], a[236:239], v[56:59], a[164:167]\n"
    "s_waitcnt lgkmcnt(3)\n"
    "v_cvt_pk_bf16_f32 v73, v74, v75\n"
    "v_mfma_f32_16x16x32_bf16 v[152:155], a[192:195], v[0:3], v[152:155]\n"
    "ds_read_b64_tr_b16 a[230:231], v226 offset:20992\n"
    "v_fma_f32 v110, v110, s40, v234\n"
    "v_mfma_f32_16x16x32_bf16 v[152:155], a[196:199], v[8:11], v[152:155]\n"
    "v_exp_f32 v89, v89\n"
    "v_mfma_f32_16x16x32_bf16 v[200:203], a[192:195], v[16:19], v[200:203]\n"
    "v_exp_f32 v90, v90\n"
    "v_mfma_f32_16x16x32_bf16 v[200:203], a[196:199], v[24:27], v[200:203]\n"
    "ds_read_b64_tr_b16 a[232:233], v227 offset:2560\n"
    "v_cvt_pk_bf16_f32 v88, v88, v89\n"
    "v_mfma_f32_16x16x32_bf16 a[120:123], a[192:195], v[32:35], a[120:123]\n"
    "v_exp_f32 v91, v91\n"
    "v_mfma_f32_16x16x32_bf16 a[120:123], a[196:199], v[40:43], a[120:123]\n"
    "v_exp_f32 v104, v104\n"
    "v_mfma_f32_16x16x32_bf16 a[168:171], a[192:195], v[48:51], a[168:171]\n"
    "ds_read_b64_tr_b16 a[234:235], v227 offset:8704\n"
    "v_cvt_pk_bf16_f32 v89, v90, v91\n"
    "v_mfma_f32_16x16x32_bf16 a[168:171], a[196:199], v[56:59], a[168:171]\n"
    "v_exp_f32 v105, v105\n"
    "v_mfma_f32_16x16x32_bf16 v[156:159], a[200:203], v[0:3], v[156:159]\n"
    "v_exp_f32 v106, v106\n"
    "v_mfma_f32_16x16x32_bf16 v[156:159], a[204:207], v[8:11], v[156:159]\n"
    "v_exp_f32 v107, v107\n"
    "v_mfma_f32_16x16x32_bf16 v[204:207], a[200:203], v[16:19], v[204:207]\n"
    "ds_read_b64_tr_b16 a[236:237], v227 offset:14848\n"
    "v_cvt_pk_bf16_f32 v104, v104, v105\n"
    "v_mfma_f32_16x16x32_bf16 v[204:207], a[204:207], v[24:27], v[204:207]\n"
    "ds_read_b128 v[244:247], v251 offset:8192\n"
    "v_cvt_pk_bf16_f32 v105, v106, v107\n"
    "v_mfma_f32_16x16x32_bf16 a[124:127], a[200:203], v[32:35], a[124:127]\n"
    "v_exp_f32 v120, v120\n"
    "v_mfma_f32_16x16x32_bf16 a[124:127], a[204:207], v[40:43], a[124:127]\n"
    "v_exp_f32 v121, v121\n"
    "v_mfma_f32_16x16x32_bf16 a[172:175], a[200:203], v[48:51], a[172:175]\n"
    "v_exp_f32 v122, v122\n"
    "v_mfma_f32_16x16x32_bf16 a[172:175], a[204:207], v[56:59], a[172:175]\n"
    "v_exp_f32 v123, v123\n"
    "v_mfma_f32_16x16x32_bf16 a[248:251], v[240:243], v[32:35], a[248:251]\n"
    "ds_read_b64_tr_b16 a[238:239], v227 offset:20992\n"
    "v_cvt_pk_bf16_f32 v120, v120, v121\n"
    "v_mfma_f32_16x16x32_bf16 a[248:251], v[240:243], v[40:43], a[248:251]\n"
    "v_exp_f32 v76, v76\n"
    "v_mfma_f32_16x16x32_bf16 v[160:163], a[208:211], v[0:3], v[160:163]\n"
    "v_exp_f32 v77, v77\n"
    "v_mfma_f32_16x16x32_bf16 v[160:163], a[212:215], v[8:11], v[160:163]\n"
    "v_exp_f32 v78, v78\n"
    "v_mfma_f32_16x16x32_bf16 v[208:211], a[208:211], v[16:19], v[208:211]\n"
    "v_exp_f32 v79, v79\n"
    "v_mfma_f32_16x16x32_bf16 v[208:211], a[212:215], v[24:27], v[208:211]\n"
    "v_exp_f32 v92, v92\n"
    "v_mfma_f32_16x16x32_bf16 a[128:131], a[208:211], v[32:35], a[128:131]\n"
    "v_exp_f32 v93, v93\n"
    "v_mfma_f32_16x16x32_bf16 a[128:131], a[212:215], v[40:43], a[128:131]\n"
    "v_exp_f32 v94, v94\n"
    "v_mfma_f32_16x16x32_bf16 a[176:179], a[208:211], v[48:51], a[176:179]\n"
    "v_exp_f32 v95, v95\n"
    "v_mfma_f32_16x16x32_bf16 a[176:179], a[212:215], v[56:59], a[176:179]\n"
    "v_exp_f32 v108, v108\n"
    "v_mfma_f32_16x16x32_bf16 v[164:167], a[216:219], v[0:3], v[164:167]\n"
    "v_exp_f32 v109, v109\n"
    "v_mfma_f32_16x16x32_bf16 v[164:167], a[220:223], v[8:11], v[164:167]\n"
    "s_waitcnt vmcnt(18) lgkmcnt(0)\n"
    "v_cvt_pk_bf16_f32 v121, v122, v123\n"
    "v_mfma_f32_16x16x32_bf16 v[212:215], a[216:219], v[16:19], v[212:215]\n"
    "s_barrier\n"
    "v_cvt_pk_bf16_f32 v74, v76, v77\n"
    "v_mfma_f32_16x16x32_bf16 v[212:215], a[220:223], v[24:27], v[212:215]\n"
    "ds_read_b128 a[192:195], v224 offset:0\n"
    "v_fma_f32 v111, v111, s40, v234\n"
    "v_mfma_f32_16x16x32_bf16 a[132:135], a[216:219], v[32:35], a[132:135]\n"
    "ds_read_b128 a[196:199], v225 offset:0\n"
    "v_cvt_pk_bf16_f32 v75, v78, v79\n"
    "v_mfma_f32_16x16x32_bf16 a[132:135], a[220:223], v[40:43], a[132:135]\n"
    "ds_read_b128 a[200:203], v224 offset:8192\n"
    "v_cvt_pk_bf16_f32 v90, v92, v93\n"
    "v_mfma_f32_16x16x32_bf16 a[180:183], a[216:219], v[48:51], a[180:183]\n"
    "ds_read_b128 a[204:207], v225 offset:8192\n"
    "v_cvt_pk_bf16_f32 v91, v94, v95\n"
    "v_mfma_f32_16x16x32_bf16 a[180:183], a[220:223], v[56:59], a[180:183]\n"
    "buffer_store_dwordx4 v[244:247], v250, s[84:87], s78 offen offset:128 nt sc1\n"
    "v_mfma_f32_16x16x32_bf16 v[168:171], a[224:227], v[0:3], v[168:171]\n"
    "ds_read_b128 a[208:211], v224 offset:16384\n"
    "v_cvt_pk_bf16_f32 v106, v108, v109\n"
    "v_mfma_f32_16x16x32_bf16 v[168:171], a[228:231], v[8:11], v[168:171]\n"
    "v_exp_f32 v110, v110\n"
    "v_mfma_f32_16x16x32_bf16 v[216:219], a[224:227], v[16:19], v[216:219]\n"
    "ds_read_b128 a[212:215], v225 offset:16384\n"
    "v_mfma_f32_16x16x32_bf16 v[216:219], a[228:231], v[24:27], v[216:219]\n"
    "v_exp_f32 v111, v111\n"
    "v_mfma_f32_16x16x32_bf16 a[136:139], a[224:227], v[32:35], a[136:139]\n"
    "ds_read_b128 a[216:219], v224 offset:2048\n"
    "v_cvt_pk_bf16_f32 v107, v110, v111\n"
    "v_mfma_f32_16x16x32_bf16 a[136:139], a[228:231], v[40:43], a[136:139]\n"
    "v_mfma_f32_16x16x32_bf16 a[184:187], a[224:227], v[48:51], a[184:187]\n"
    "v_mfma_f32_16x16x32_bf16 a[184:187], a[228:231], v[56:59], a[184:187]\n"
    "ds_read_b128 a[220:223], v225 offset:2048\n"
    "v_mfma_f32_16x16x32_bf16 a[252:255], v[240:243], v[48:51], a[252:255]\n"
    "v_mfma_f32_16x16x32_bf16 a[252:255], v[240:243], v[56:59], a[252:255]\n"
    "v_mfma_f32_16x16x32_bf16 v[172:175], a[232:235], v[0:3], v[172:175]\n"
    "ds_read_b128 a[224:227], v224 offset:10240\n"
    "v_mfma_f32_16x16x32_bf16 v[172:175], a[236:239], v[8:11], v[172:175]\n"
    "v_mfma_f32_16x16x32_bf16 v[220:223], a[232:235], v[16:19], v[220:223]\n"
    "v_mfma_f32_16x16x32_bf16 v[220:223], a[236:239], v[24:27], v[220:223]\n"
    "v_mfma_f32_16x16x32_bf16 a[140:143], a[232:235], v[32:35], a[140:143]\n"
    "v_mfma_f32_16x16x32_bf16 a[140:143], a[236:239], v[40:43], a[140:143]\n"
    "ds_read_b128 a[228:231], v225 offset:10240\n"
    "v_mfma_f32_16x16x32_bf16 a[188:191], a[232:235], v[48:51], a[188:191]\n"
    "s_waitcnt lgkmcnt(4)\n"
    "v_mfma_f32_16x16x32_bf16 a[188:191], a[236:239], v[56:59], a[188:191]\n"
    "s_branch LLOOP_%=\n"
    "LB6_%=:\n"
    "v_add_u32 v253, s75, v224\n"
    "v_add_u32 v254, s75, v225\n"
    "s_waitcnt vmcnt(16)\n"
    "v_mfma_f32_16x16x32_bf16 v[128:131], a[192:195], v[0:3], v[128:131]\n"
    "ds_read_b64_tr_b16 a[216:217], v227 offset:512\n"
    "v_fma_f32 v72, v72, s40, v232\n"
    "v_mfma_f32_16x16x32_bf16 v[128:131], a[196:199], v[8:11], v[128:131]\n"
    "ds_read_b64_tr_b16 a[218:219], v227 offset:6656\n"
    "ds_read_b128 a[0:3], v253 offset:0\n"
    "v_mfma_f32_16x16x32_bf16 v[176:179], a[192:195], v[16:19], v[176:179]\n"
    "ds_read_b64_tr_b16 a[220:221], v227 offset:12800\n"
    "ds_read_b128 a[4:7], v254 offset:0\n"
    "v_mfma_f32_16x16x32_bf16 v[176:179], a[196:199], v[24:27], v[176:179]\n"
    "ds_read_b64_tr_b16 a[222:223], v227 offset:18944\n"
    "ds_read_b128 a[24:27], v253 offset:2048\n"
    "v_mfma_f32_16x16x32_bf16 a[96:99], a[192:195], v[32:35], a[96:99]\n"
    "ds_read_b64_tr_b16 a[224:225], v226 offset:1024\n"
    "ds_read_b128 a[28:31], v254 offset:2048\n"
    "v_mfma_f32_16x16x32_bf16 a[96:99], a[196:199], v[40:43], a[96:99]\n"
    "ds_read_b64_tr_b16 a[226:227], v226 offset:7168\n"
    "ds_read_b128 a[48:51], v253 offset:4096\n"
    "v_mfma_f32_16x16x32_bf16 a[144:147], a[192:195], v[48:51], a[144:147]\n"
    "ds_read_b64_tr_b16 a[228:229], v226 offset:13312\n"
    "ds_read_b128 a[52:55], v254 offset:4096\n"
    "v_mfma_f32_16x16x32_bf16 a[144:147], a[196:199], v[56:59], a[144:147]\n"
    "ds_read_b64_tr_b16 a[230:231], v226 offset:19456\n"
    "ds_read_b128 a[72:75], v253 offset:6144\n"
    "v_mfma_f32_16x16x32_bf16 v[132:135], a[200:203], v[0:3], v[132:135]\n"
    "ds_read_b64_tr_b16 a[232:233], v227 offset:1024\n"
    "ds_read_b128 a[76:79], v254 offset:6144\n"
    "v_mfma_f32_16x16x32_bf16 v[132:135], a[204:207], v[8:11], v[132:135]\n"
    "ds_read_b64_tr_b16 a[234:235], v227 offset:7168\n"
    "v_fma_f32 v73, v73, s40, v232\n"
    "v_mfma_f32_16x16x32_bf16 v[180:183], a[200:203], v[16:19], v[180:183]\n"
    "ds_read_b64_tr_b16 a[236:237], v227 offset:13312\n"
    "v_fma_f32 v74, v74, s40, v232\n"
    "v_mfma_f32_16x16x32_bf16 v[180:183], a[204:207], v[24:27], v[180:183]\n"
    "v_exp_f32 v72, v72\n"
    "v_mfma_f32_16x16x32_bf16 a[100:103], a[200:203], v[32:35], a[100:103]\n"
    "ds_read_b64_tr_b16 a[238:239], v227 offset:19456\n"
    "v_fma_f32 v75, v75, s40, v232\n"
    "v_mfma_f32_16x16x32_bf16 a[100:103], a[204:207], v[40:43], a[100:103]\n"
    "ds_read_b64_tr_b16 a[192:193], v226 offset:1536\n"
    "v_fma_f32 v88, v88, s40, v233\n"
    "v_mfma_f32_16x16x32_bf16 a[148:151], a[200:203], v[48:51], a[148:151]\n"
    "ds_read_b64_tr_b16 a[194:195], v226 offset:7680\n"
    "v_fma_f32 v89, v89, s40, v233\n"
    "v_mfma_f32_16x16x32_bf16 a[148:151], a[204:207], v[56:59], a[148:151]\n"
    "ds_read_b64_tr_b16 a[196:197], v226 offset:13824\n"
    "v_fma_f32 v90, v90, s40, v233\n"
    "v_mfma_f32_16x16x32_bf16 a[240:243], v[240:243], v[0:3], a[240:243]\n"
    "ds_read_b64_tr_b16 a[198:199], v226 offset:19968\n"
    "v_fma_f32 v91, v91, s40, v233\n"
    "v_mfma_f32_16x16x32_bf16 a[240:243], v[240:243], v[8:11], a[240:243]\n"
    "ds_read_b64_tr_b16 a[200:201], v227 offset:1536\n"
    "v_fma_f32 v104, v104, s40, v234\n"
    "v_mfma_f32_16x16x32_bf16 v[136:139], a[208:211], v[0:3], v[136:139]\n"
    "ds_read_b64_tr_b16 a[202:203], v227 offset:7680\n"
    "v_fma_f32 v105, v105, s40, v234\n"
    "v_mfma_f32_16x16x32_bf16 v[136:139], a[212:215], v[8:11], v[136:139]\n"
    "ds_read_b64_tr_b16 a[204:205], v227 offset:13824\n"
    "v_fma_f32 v106, v106, s40, v234\n"
    "v_mfma_f32_16x16x32_bf16 v[184:187], a[208:211], v[16:19], v[184:187]\n"
    "ds_read_b64_tr_b16 a[206:207], v227 offset:19968\n"
    "s_waitcnt lgkmcnt(11)\n"
    "v_mfma_f32_16x16x32_bf16 v[184:187], a[212:215], v[24:27], v[184:187]\n"
    "s_add_u32 m0, s49, -256\n"
    "v_fma_f32 v107, v107, s40, v234\n"
    "v_mfma_f32_16x16x32_bf16 a[104:107], a[208:211], v[32:35], a[104:107]\n"
    "buffer_load_dwordx4 v228, s[28:31], 0 offen offset:256 nt lds\n"
    "v_mfma_f32_16x16x32_bf16 a[104:107], a[212:215], v[40:43], a[104:107]\n"
    "v_exp_f32 v73, v73\n"
    "v_mfma_f32_16x16x32_bf16 a[152:155], a[208:211], v[48:51], a[152:155]\n"
    "v_exp_f32 v74, v74\n"
    "v_mfma_f32_16x16x32_bf16 a[152:155], a[212:215], v[56:59], a[152:155]\n"
    "s_waitcnt lgkmcnt(8)\n"
    "v_cvt_pk_bf16_f32 v72, v72, v73\n"
    "v_mfma_f32_16x16x32_bf16 v[140:143], a[216:219], v[0:3], v[140:143]\n"
    "ds_read_b64_tr_b16 a[208:209], v226 offset:2048\n"
    "buffer_load_dwordx4 v229, s[28:31], 0 offen offset:1280 nt lds\n"
    "v_mfma_f32_16x16x32_bf16 v[140:143], a[220:223], v[8:11], v[140:143]\n"
    "ds_read_b64_tr_b16 a[210:211], v226 offset:8192\n"
    "v_fma_f32 v120, v120, s40, v235\n"
    "v_mfma_f32_16x16x32_bf16 v[188:191], a[216:219], v[16:19], v[188:191]\n"
    "ds_read_b64_tr_b16 a[212:213], v226 offset:14336\n"
    "v_fma_f32 v121, v121, s40, v235\n"
    "v_mfma_f32_16x16x32_bf16 v[188:191], a[220:223], v[24:27], v[188:191]\n"
    "ds_read_b64_tr_b16 a[214:215], v226 offset:20480\n"
    "v_fma_f32 v122, v122, s40, v235\n"
    "v_mfma_f32_16x16x32_bf16 a[108:111], a[216:219], v[32:35], a[108:111]\n"
    "s_add_u32 m0, s49, 1792\n"
    "v_fma_f32 v123, v123, s40, v235\n"
    "v_mfma_f32_16x16x32_bf16 a[108:111], a[220:223], v[40:43], a[108:111]\n"
    "buffer_load_dwordx4 v228, s[28:31], s42 offen offset:256 nt lds\n"
    "v_mfma_f32_16x16x32_bf16 a[156:159], a[216:219], v[48:51], a[156:159]\n"
    "v_exp_f32 v75, v75\n"
    "v_mfma_f32_16x16x32_bf16 a[156:159], a[220:223], v[56:59], a[156:159]\n"
    "ds_read_b64_tr_b16 a[216:217], v227 offset:2048\n"
    "v_cvt_pk_bf16_f32 v73, v74, v75\n"
    "v_mfma_f32_16x16x32_bf16 v[144:147], a[224:227], v[0:3], v[144:147]\n"
    "ds_read_b64_tr_b16 a[218:219], v227 offset:8192\n"
    "v_fma_f32 v76, v76, s40, v232\n"
    "v_mfma_f32_16x16x32_bf16 v[144:147], a[228:231], v[8:11], v[144:147]\n"
    "ds_read_b64_tr_b16 a[220:221], v227 offset:14336\n"
    "v_fma_f32 v77, v77, s40, v232\n"
    "v_mfma_f32_16x16x32_bf16 v[192:195], a[224:227], v[16:19], v[192:195]\n"
    "buffer_load_dwordx4 v229, s[28:31], s42 offen offset:1280 nt lds\n"
    "v_mfma_f32_16x16x32_bf16 v[192:195], a[228:231], v[24:27], v[192:195]\n"
    "ds_read_b64_tr_b16 a[222:223], v227 offset:20480\n"
    "v_fma_f32 v78, v78, s40, v232\n"
    "v_mfma_f32_16x16x32_bf16 a[112:115], a[224:227], v[32:35], a[112:115]\n"
    "s_add_u32 m0, s49, 3840\n"
    "v_fma_f32 v79, v79, s40, v232\n"
    "v_mfma_f32_16x16x32_bf16 a[112:115], a[228:231], v[40:43], a[112:115]\n"
    "v_exp_f32 v88, v88\n"
    "v_mfma_f32_16x16x32_bf16 a[160:163], a[224:227], v[48:51], a[160:163]\n"
    "buffer_load_dwordx4 v228, s[28:31], s43 offen offset:256 nt lds\n"
    "v_mfma_f32_16x16x32_bf16 a[160:163], a[228:231], v[56:59], a[160:163]\n"
    "v_exp_f32 v89, v89\n"
    "v_mfma_f32_16x16x32_bf16 a[244:247], v[240:243], v[16:19], a[244:247]\n"
    "ds_read_b64_tr_b16 a[224:225], v226 offset:2560\n"
    "v_cvt_pk_bf16_f32 v88, v88, v89\n"
    "v_mfma_f32_16x16x32_bf16 a[244:247], v[240:243], v[24:27], a[244:247]\n"
    "v_exp_f32 v90, v90\n"
    "v_mfma_f32_16x16x32_bf16 v[148:151], a[232:235], v[0:3], v[148:151]\n"
    "ds_read_b64_tr_b16 a[226:227], v226 offset:8704\n"
    "v_fma_f32 v92, v92, s40, v233\n"
    "v_mfma_f32_16x16x32_bf16 v[148:151], a[236:239], v[8:11], v[148:151]\n"
    "buffer_load_dwordx4 v229, s[28:31], s43 offen offset:1280 nt lds\n"
    "v_mfma_f32_16x16x32_bf16 v[196:199], a[232:235], v[16:19], v[196:199]\n"
    "v_exp_f32 v91, v91\n"
    "v_mfma_f32_16x16x32_bf16 v[196:199], a[236:239], v[24:27], v[196:199]\n"
    "ds_read_b64_tr_b16 a[228:229], v226 offset:14848\n"
    "v_cvt_pk_bf16_f32 v89, v90, v91\n"
    "v_mfma_f32_16x16x32_bf16 a[116:119], a[232:235], v[32:35], a[116:119]\n"
    "s_add_u32 m0, s49, 5888\n"
    "v_fma_f32 v93, v93, s40, v233\n"
    "v_mfma_f32_16x16x32_bf16 a[116:119], a[236:239], v[40:43], a[116:119]\n"
    "buffer_load_dwordx4 v228, s[28:31], s44 offen offset:256 nt lds\n"
    "v_mfma_f32_16x16x32_bf16 a[164:167], a[232:235], v[48:51], a[164:167]\n"
    "ds_read_b64_tr_b16 a[230:231], v226 offset:20992\n"
    "v_fma_f32 v94, v94, s40, v233\n"
    "v_mfma_f32_16x16x32_bf16 a[164:167], a[236:239], v[56:59], a[164:167]\n"
    "s_waitcnt lgkmcnt(4)\n"
    "v_fma_f32 v95, v95, s40, v233\n"
    "v_mfma_f32_16x16x32_bf16 v[152:155], a[192:195], v[0:3], v[152:155]\n"
    "v_exp_f32 v104, v104\n"
    "v_mfma_f32_16x16x32_bf16 v[152:155], a[196:199], v[8:11], v[152:155]\n"
    "ds_read_b64_tr_b16 a[232:233], v227 offset:2560\n"
    "v_fma_f32 v108, v108, s40, v234\n"
    "v_mfma_f32_16x16x32_bf16 v[200:203], a[192:195], v[16:19], v[200:203]\n"
    "buffer_load_dwordx4 v229, s[28:31], s44 offen offset:1280 nt lds\n"
    "v_mfma_f32_16x16x32_bf16 v[200:203], a[196:199], v[24:27], v[200:203]\n"
    "v_exp_f32 v105, v105\n"
    "v_mfma_f32_16x16x32_bf16 a[120:123], a[192:195], v[32:35], a[120:123]\n"
    "s_waitcnt vmcnt(9)\n"
    "v_cvt_pk_bf16_f32 v104, v104, v105\n"
    "v_mfma_f32_16x16x32_bf16 a[120:123], a[196:199], v[40:43], a[120:123]\n"
    "ds_read_b64_tr_b16 a[234:235], v227 offset:8704\n"
    "v_fma_f32 v109, v109, s40, v234\n"
    "v_mfma_f32_16x16x32_bf16 a[168:171], a[192:195], v[48:51], a[168:171]\n"
    "ds_read_b128 a[8:11], v253 offset:8192\n"
    "v_fma_f32 v110, v110, s40, v234\n"
    "v_mfma_f32_16x16x32_bf16 a[168:171], a[196:199], v[56:59], a[168:171]\n"
    "v_exp_f32 v106, v106\n"
    "v_mfma_f32_16x16x32_bf16 v[156:159], a[200:203], v[0:3], v[156:159]\n"
    "v_exp_f32 v107, v107\n"
    "v_mfma_f32_16x16x32_bf16 v[156:159], a[204:207], v[8:11], v[156:159]\n"
    "v_exp_f32 v120, v120\n"
    "v_mfma_f32_16x16x32_bf16 v[204:207], a[200:203], v[16:19], v[204:207]\n"
    "ds_read_b64_tr_b16 a[236:237], v227 offset:14848\n"
    "v_cvt_pk_bf16_f32 v105, v106, v107\n"
    "v_mfma_f32_16x16x32_bf16 v[204:207], a[204:207], v[24:27], v[204:207]\n"
    "ds_read_b128 a[12:15], v254 offset:8192\n"
    "v_fma_f32 v111, v111, s40, v234\n"
    "v_mfma_f32_16x16x32_bf16 a[124:127], a[200:203], v[32:35], a[124:127]\n"
    "v_exp_f32 v121, v121\n"
    "v_mfma_f32_16x16x32_bf16 a[124:127], a[204:207], v[40:43], a[124:127]\n"
    "v_exp_f32 v122, v122\n"
    "v_mfma_f32_16x16x32_bf16 a[172:175], a[200:203], v[48:51], a[172:175]\n"
    "v_exp_f32 v123, v123\n"
    "v_mfma_f32_16x16x32_bf16 a[172:175], a[204:207], v[56:59], a[172:175]\n"
    "ds_read_b128 a[32:35], v253 offset:10240\n"
    "v_cvt_pk_bf16_f32 v120, v120, v121\n"
    "v_mfma_f32_16x16x32_bf16 a[248:251], v[240:243], v[32:35], a[248:251]\n"
    "ds_read_b64_tr_b16 a[238:239], v227 offset:20992\n"
    "v_cvt_pk_bf16_f32 v121, v122, v123\n"
    "v_mfma_f32_16x16x32_bf16 a[248:251], v[240:243], v[40:43], a[248:251]\n"
    "v_exp_f32 v76, v76\n"
    "v_mfma_f32_16x16x32_bf16 v[160:163], a[208:211], v[0:3], v[160:163]\n"
    "v_exp_f32 v77, v77\n"
    "v_mfma_f32_16x16x32_bf16 v[160:163], a[212:215], v[8:11], v[160:163]\n"
    "v_exp_f32 v78, v78\n"
    "v_mfma_f32_16x16x32_bf16 v[208:211], a[208:211], v[16:19], v[208:211]\n"
    "ds_read_b128 a[36:39], v254 offset:10240\n"
    "v_cvt_pk_bf16_f32 v74, v76, v77\n"
    "v_mfma_f32_16x16x32_bf16 v[208:211], a[212:215], v[24:27], v[208:211]\n"
    "v_exp_f32 v79, v79\n"
    "v_mfma_f32_16x16x32_bf16 a[128:131], a[208:211], v[32:35], a[128:131]\n"
    "v_exp_f32 v92, v92\n"
    "v_mfma_f32_16x16x32_bf16 a[128:131], a[212:215], v[40:43], a[128:131]\n"
    "v_exp_f32 v93, v93\n"
    "v_mfma_f32_16x16x32_bf16 a[176:179], a[208:211], v[48:51], a[176:179]\n"
    "v_exp_f32 v94, v94\n"
    "v_mfma_f32_16x16x32_bf16 a[176:179], a[212:215], v[56:59], a[176:179]\n"
    "v_exp_f32 v95, v95\n"
    "v_mfma_f32_16x16x32_bf16 v[164:167], a[216:219], v[0:3], v[164:167]\n"
    "ds_read_b128 a[56:59], v253 offset:12288\n"
    "v_cvt_pk_bf16_f32 v75, v78, v79\n"
    "v_mfma_f32_16x16x32_bf16 v[164:167], a[220:223], v[8:11], v[164:167]\n"
    "s_waitcnt vmcnt(11) lgkmcnt(2)\n"
    "v_fma_f32 v124, v124, s40, v235\n"
    "v_mfma_f32_16x16x32_bf16 v[212:215], a[216:219], v[16:19], v[212:215]\n"
    "s_barrier\n"
    "v_fma_f32 v125, v125, s40, v235\n"
    "v_mfma_f32_16x16x32_bf16 v[212:215], a[220:223], v[24:27], v[212:215]\n"
    "ds_read_b64_tr_b16 a[192:193], v226 offset:24576\n"
    "v_cvt_pk_bf16_f32 v90, v92, v93\n"
    "v_mfma_f32_16x16x32_bf16 a[132:135], a[216:219], v[32:35], a[132:135]\n"
    "ds_read_b64_tr_b16 a[194:195], v226 offset:30720\n"
    "v_fma_f32 v126, v126, s40, v235\n"
    "v_mfma_f32_16x16x32_bf16 a[132:135], a[220:223], v[40:43], a[132:135]\n"
    "ds_read_b64_tr_b16 a[196:197], v226 offset:36864\n"
    "ds_read_b128 a[60:63], v254 offset:12288\n"
    "v_mfma_f32_16x16x32_bf16 a[180:183], a[216:219], v[48:51], a[180:183]\n"
    "ds_read_b64_tr_b16 a[198:199], v226 offset:43008\n"
    "v_fma_f32 v127, v127, s40, v235\n"
    "v_mfma_f32_16x16x32_bf16 a[180:183], a[220:223], v[56:59], a[180:183]\n"
    "ds_read_b64_tr_b16 a[200:201], v227 offset:24576\n"
    "v_cvt_pk_bf16_f32 v91, v94, v95\n"
    "v_mfma_f32_16x16x32_bf16 v[168:171], a[224:227], v[0:3], v[168:171]\n"
    "ds_read_b64_tr_b16 a[202:203], v227 offset:30720\n"
    "v_mfma_f32_16x16x32_bf16 v[168:171], a[228:231], v[8:11], v[168:171]\n"
    "ds_read_b64_tr_b16 a[204:205], v227 offset:36864\n"
    "v_mfma_f32_16x16x32_bf16 v[216:219], a[224:227], v[16:19], v[216:219]\n"
    "ds_read_b64_tr_b16 a[206:207], v227 offset:43008\n"
    "ds_read_b128 a[80:83], v253 offset:14336\n"
    "v_mfma_f32_16x16x32_bf16 v[216:219], a[228:231], v[24:27], v[216:219]\n"
    "ds_read_b64_tr_b16 a[208:209], v226 offset:25088\n"
    "v_mfma_f32_16x16x32_bf16 a[136:139], a[224:227], v[32:35], a[136:139]\n"
    "ds_read_b64_tr_b16 a[210:211], v226 offset:31232\n"
    "v_mfma_f32_16x16x32_bf16 a[136:139], a[228:231], v[40:43], a[136:139]\n"
    "ds_read_b64_tr_b16 a[212:213], v226 offset:37376\n"
    "v_mfma_f32_16x16x32_bf16 a[184:187], a[224:227], v[48:51], a[184:187]\n"
    "ds_read_b64_tr_b16 a[214:215], v226 offset:43520\n"
    "v_mfma_f32_16x16x32_bf16 a[184:187], a[228:231], v[56:59], a[184:187]\n"
    "ds_read_b128 a[84:87], v254 offset:14336\n"
    "v_mfma_f32_16x16x32_bf16 a[252:255], v[240:243], v[48:51], a[252:255]\n"
    "v_exp_f32 v108, v108\n"
    "v_mfma_f32_16x16x32_bf16 a[252:255], v[240:243], v[56:59], a[252:255]\n"
    "v_exp_f32 v109, v109\n"
    "v_mfma_f32_16x16x32_bf16 v[172:175], a[232:235], v[0:3], v[172:175]\n"
    "v_exp_f32 v110, v110\n"
    "v_mfma_f32_16x16x32_bf16 v[172:175], a[236:239], v[8:11], v[172:175]\n"
    "v_exp_f32 v111, v111\n"
    "v_cvt_pk_bf16_f32 v106, v108, v109\n"
    "v_mfma_f32_16x16x32_bf16 v[220:223], a[232:235], v[16:19], v[220:223]\n"
    "v_exp_f32 v124, v124\n"
    "v_mfma_f32_16x16x32_bf16 v[220:223], a[236:239], v[24:27], v[220:223]\n"
    "v_exp_f32 v125, v125\n"
    "v_mfma_f32_16x16x32_bf16 a[140:143], a[232:235], v[32:35], a[140:143]\n"
    "v_exp_f32 v126, v126\n"
    "v_mfma_f32_16x16x32_bf16 a[140:143], a[236:239], v[40:43], a[140:143]\n"
    "v_exp_f32 v127, v127\n"
    "v_cvt_pk_bf16_f32 v107, v110, v111\n"
    "v_mfma_f32_16x16x32_bf16 a[188:191], a[232:235], v[48:51], a[188:191]\n"
    "v_cvt_pk_bf16_f32 v122, v124, v125\n"
    "v_cvt_pk_bf16_f32 v123, v126, v127\n"
    "v_mfma_f32_16x16x32_bf16 a[188:191], a[236:239], v[56:59], a[188:191]\n"
    "s_waitcnt lgkmcnt(1)\n"
    "v_mfma_f32_16x16x32_bf16 a[240:243], v[240:243], v[64:67], a[240:243]\n"
    "ds_read_b64_tr_b16 a[216:217], v227 offset:25088\n"
    "s_mov_b32 m0, s55\n"
    "v_xor_b32 v245, 32, v248\n"
    "v_xor_b32 v246, 64, v248\n"
    "v_mfma_f32_16x16x32_bf16 a[240:243], v[240:243], v[72:75], a[240:243]\n"
    "ds_read_b64_tr_b16 a[218:219], v227 offset:31232\n"
    "v_xor_b32 v247, 96, v248\n"
    "v_mfma_f32_16x16x32_bf16 a[244:247], v[240:243], v[80:83], a[244:247]\n"
    "ds_read_b64_tr_b16 a[220:221], v227 offset:37376\n"
    "buffer_load_dwordx4 v228, s[20:23], s36 offen offset:0 nt lds\n"
    "v_mfma_f32_16x16x32_bf16 a[244:247], v[240:243], v[88:91], a[244:247]\n"
    "ds_read_b64_tr_b16 a[222:223], v227 offset:43520\n"
    "v_mfma_f32_16x16x32_bf16 a[248:251], v[240:243], v[96:99], a[248:251]\n"
    "ds_read_b64_tr_b16 a[224:225], v226 offset:25600\n"
    "v_mfma_f32_16x16x32_bf16 a[248:251], v[240:243], v[104:107], a[248:251]\n"
    "ds_read_b64_tr_b16 a[226:227], v226 offset:31744\n"
    "v_mfma_f32_16x16x32_bf16 a[252:255], v[240:243], v[112:115], a[252:255]\n"
    "ds_read_b64_tr_b16 a[228:229], v226 offset:37888\n"
    "v_mfma_f32_16x16x32_bf16 a[252:255], v[240:243], v[120:123], a[252:255]\n"
    "ds_read_b64_tr_b16 a[230:231], v226 offset:44032\n"
    "v_mfma_f32_16x16x32_bf16 v[128:131], a[192:195], v[64:67], v[128:131]\n"
    "buffer_load_dwordx4 v229, s[20:23], s36 offen offset:1024 nt lds\n"
    "v_accvgpr_read_b32 v236, a240\n"
    "v_accvgpr_read_b32 v237, a244\n"
    "v_accvgpr_read_b32 v238, a248\n"
    "v_add_f32 v231, v236, v237\n"
    "v_mfma_f32_16x16x32_bf16 v[128:131], a[196:199], v[72:75], v[128:131]\n"
    "ds_read_b64_tr_b16 a[232:233], v227 offset:25600\n"
    "v_rcp_f32 v236, v236\n"
    "v_rcp_f32 v237, v237\n"
    "v_mfma_f32_16x16x32_bf16 v[176:179], a[192:195], v[80:83], v[176:179]\n"
    "v_accvgpr_read_b32 v239, a252\n"
    "v_add_f32 v255, v238, v239\n"
    "v_rcp_f32 v238, v238\n"
    "v_mfma_f32_16x16x32_bf16 v[176:179], a[196:199], v[88:91], v[176:179]\n"
    "ds_read_b64_tr_b16 a[234:235], v227 offset:31744\n"
    "v_rcp_f32 v239, v239\n"
    "v_add_f32 v231, v231, v255\n"
    "v_mfma_f32_16x16x32_bf16 a[96:99], a[192:195], v[96:99], a[96:99]\n"
    "s_mov_b32 m0, s56\n"
    "v_cmp_ngt_f32 vcc, s41, v231\n"
    "v_mul_f32 v128, v128, v236\n"
    "v_mul_f32 v129, v129, v236\n"
    "v_mfma_f32_16x16x32_bf16 a[96:99], a[196:199], v[104:107], a[96:99]\n"
    "ds_read_b64_tr_b16 a[236:237], v227 offset:37888\n"
    "s_or_b64 s[50:51], s[50:51], vcc\n"
    "v_mul_f32 v176, v176, v237\n"
    "v_mul_f32 v177, v177, v237\n"
    "v_mfma_f32_16x16x32_bf16 a[144:147], a[192:195], v[112:115], a[144:147]\n"
    "buffer_load_dwordx4 v228, s[20:23], s36 offen offset:128 nt lds\n"
    "v_mul_f32 v130, v130, v236\n"
    "v_mul_f32 v178, v178, v237\n"
    "v_mfma_f32_16x16x32_bf16 a[144:147], a[196:199], v[120:123], a[144:147]\n"
    "v_mul_f32 v131, v131, v236\n"
    "v_mul_f32 v179, v179, v237\n"
    "v_cvt_pk_bf16_f32 v128, v128, v129\n"
    "v_cvt_pk_bf16_f32 v176, v176, v177\n"
    "v_mfma_f32_16x16x32_bf16 v[132:135], a[200:203], v[64:67], v[132:135]\n"
    "ds_read_b64_tr_b16 a[238:239], v227 offset:44032\n"
    "v_cvt_pk_bf16_f32 v129, v130, v131\n"
    "v_cvt_pk_bf16_f32 v177, v178, v179\n"
    "v_accvgpr_read_b32 v68, a96\n"
    "v_mfma_f32_16x16x32_bf16 v[132:135], a[204:207], v[72:75], v[132:135]\n"
    "ds_read_b64_tr_b16 a[192:193], v226 offset:26112\n"
    "ds_write_b64 v248, v[128:129] offset:8192\n"
    "v_mfma_f32_16x16x32_bf16 v[180:183], a[200:203], v[80:83], v[180:183]\n"
    "ds_read_b64_tr_b16 a[194:195], v226 offset:32256\n"
    "v_accvgpr_read_b32 v76, a144\n"
    "v_accvgpr_read_b32 v69, a97\n"
    "v_accvgpr_read_b32 v77, a145\n"
    "v_mfma_f32_16x16x32_bf16 v[180:183], a[204:207], v[88:91], v[180:183]\n"
    "ds_read_b64_tr_b16 a[196:197], v226 offset:38400\n"
    "ds_write_b64 v248, v[176:177] offset:10240\n"
    "v_mfma_f32_16x16x32_bf16 a[100:103], a[200:203], v[96:99], a[100:103]\n"
    "ds_read_b64_tr_b16 a[198:199], v226 offset:44544\n"
    "v_accvgpr_read_b32 v70, a98\n"
    "v_accvgpr_read_b32 v78, a146\n"
    "v_accvgpr_read_b32 v71, a99\n"
    "v_mfma_f32_16x16x32_bf16 a[100:103], a[204:207], v[104:107], a[100:103]\n"
    "buffer_load_dwordx4 v229, s[20:23], s36 offen offset:1152 nt lds\n"
    "v_accvgpr_read_b32 v79, a147\n"
    "v_mul_f32 v68, v68, v238\n"
    "v_mfma_f32_16x16x32_bf16 a[148:151], a[200:203], v[112:115], a[148:151]\n"
    "v_mul_f32 v76, v76, v239\n"
    "v_mul_f32 v69, v69, v238\n"
    "v_mul_f32 v77, v77, v239\n"
    "v_mul_f32 v70, v70, v238\n"
    "v_mfma_f32_16x16x32_bf16 a[148:151], a[204:207], v[120:123], a[148:151]\n"
    "ds_read_b64_tr_b16 a[200:201], v227 offset:26112\n"
    "v_mul_f32 v78, v78, v239\n"
    "v_mul_f32 v71, v71, v238\n"
    "v_mul_f32 v79, v79, v239\n"
    "v_mfma_f32_16x16x32_bf16 v[136:139], a[208:211], v[64:67], v[136:139]\n"
    "ds_read_b64_tr_b16 a[202:203], v227 offset:32256\n"
    "v_cvt_pk_bf16_f32 v68, v68, v69\n"
    "v_cvt_pk_bf16_f32 v76, v76, v77\n"
    "v_cvt_pk_bf16_f32 v69, v70, v71\n"
    "v_mfma_f32_16x16x32_bf16 v[136:139], a[212:215], v[72:75], v[136:139]\n"
    "s_mov_b32 m0, s57\n"
    "ds_write_b64 v248, v[68:69] offset:12288\n"
    "v_mfma_f32_16x16x32_bf16 v[184:187], a[208:211], v[80:83], v[184:187]\n"
    "ds_read_b64_tr_b16 a[204:205], v227 offset:38400\n"
    "v_cvt_pk_bf16_f32 v77, v78, v79\n"
    "v_mul_f32 v132, v132, v236\n"
    "v_mul_f32 v180, v180, v237\n"
    "v_mfma_f32_16x16x32_bf16 v[184:187], a[212:215], v[88:91], v[184:187]\n"
    "buffer_load_dwordx4 v228, s[20:23], s36 offen offset:256 nt lds\n"
    "ds_write_b64 v248, v[76:77] offset:14336\n"
    "v_mfma_f32_16x16x32_bf16 a[104:107], a[208:211], v[96:99], a[104:107]\n"
    "ds_read_b64_tr_b16 a[206:207], v227 offset:44544\n"
    "v_mul_f32 v133, v133, v236\n"
    "v_mul_f32 v181, v181, v237\n"
    "v_mfma_f32_16x16x32_bf16 a[104:107], a[212:215], v[104:107], a[104:107]\n"
    "v_mul_f32 v134, v134, v236\n"
    "v_mul_f32 v182, v182, v237\n"
    "v_mul_f32 v135, v135, v236\n"
    "v_mul_f32 v183, v183, v237\n"
    "v_mfma_f32_16x16x32_bf16 a[152:155], a[208:211], v[112:115], a[152:155]\n"
    "v_cvt_pk_bf16_f32 v132, v132, v133\n"
    "v_cvt_pk_bf16_f32 v180, v180, v181\n"
    "v_cvt_pk_bf16_f32 v133, v134, v135\n"
    "v_cvt_pk_bf16_f32 v181, v182, v183\n"
    "v_mfma_f32_16x16x32_bf16 a[152:155], a[212:215], v[120:123], a[152:155]\n"
    "s_waitcnt lgkmcnt(12)\n"
    "ds_write_b64 v245, v[132:133] offset:8192\n"
    "v_mfma_f32_16x16x32_bf16 v[140:143], a[216:219], v[64:67], v[140:143]\n"
    "ds_read_b64_tr_b16 a[208:209], v226 offset:26624\n"
    "v_accvgpr_read_b32 v84, a100\n"
    "v_accvgpr_read_b32 v92, a148\n"
    "v_accvgpr_read_b32 v85, a101\n"
    "v_mfma_f32_16x16x32_bf16 v[140:143], a[220:223], v[72:75], v[140:143]\n"
    "buffer_load_dwordx4 v229, s[20:23], s36 offen offset:1280 nt lds\n"
    "ds_write_b64 v245, v[180:181] offset:10240\n"
    "v_mfma_f32_16x16x32_bf16 v[188:191], a[216:219], v[80:83], v[188:191]\n"
    "ds_read_b64_tr_b16 a[210:211], v226 offset:32768\n"
    "v_accvgpr_read_b32 v93, a149\n"
    "v_accvgpr_read_b32 v86, a102\n"
    "v_mfma_f32_16x16x32_bf16 v[188:191], a[220:223], v[88:91], v[188:191]\n"
    "s_add_u32 s36, s36, 24576\n"
    "v_accvgpr_read_b32 v94, a150\n"
    "v_accvgpr_read_b32 v87, a103\n"
    "v_accvgpr_read_b32 v95, a151\n"
    "v_mfma_f32_16x16x32_bf16 a[108:111], a[216:219], v[96:99], a[108:111]\n"
    "v_mul_f32 v84, v84, v238\n"
    "v_mul_f32 v92, v92, v239\n"
    "v_mul_f32 v85, v85, v238\n"
    "v_mul_f32 v93, v93, v239\n"
    "v_mfma_f32_16x16x32_bf16 a[108:111], a[220:223], v[104:107], a[108:111]\n"
    "ds_read_b64_tr_b16 a[212:213], v226 offset:38912\n"
    "v_mul_f32 v86, v86, v238\n"
    "v_mul_f32 v94, v94, v239\n"
    "v_mul_f32 v87, v87, v238\n"
    "v_mfma_f32_16x16x32_bf16 a[156:159], a[216:219], v[112:115], a[156:159]\n"
    "v_mul_f32 v95, v95, v239\n"
    "v_cvt_pk_bf16_f32 v84, v84, v85\n"
    "v_cvt_pk_bf16_f32 v92, v92, v93\n"
    "v_cvt_pk_bf16_f32 v85, v86, v87\n"
    "v_mfma_f32_16x16x32_bf16 a[156:159], a[220:223], v[120:123], a[156:159]\n"
    "ds_write_b64 v245, v[84:85] offset:12288\n"
    "v_cvt_pk_bf16_f32 v93, v94, v95\n"
    "v_mfma_f32_16x16x32_bf16 v[144:147], a[224:227], v[64:67], v[144:147]\n"
    "ds_read_b64_tr_b16 a[214:215], v226 offset:45056\n"
    "v_mul_f32 v136, v136, v236\n"
    "v_mul_f32 v184, v184, v237\n"
    "v_mul_f32 v137, v137, v236\n"
    "v_mfma_f32_16x16x32_bf16 v[144:147], a[228:231], v[72:75], v[144:147]\n"
    "ds_read_b64_tr_b16 a[216:217], v227 offset:26624\n"
    "ds_write_b64 v245, v[92:93] offset:14336\n"
    "v_mfma_f32_16x16x32_bf16 v[192:195], a[224:227], v[80:83], v[192:195]\n"
    "ds_read_b64_tr_b16 a[218:219], v227 offset:32768\n"
    "v_mul_f32 v185, v185, v237\n"
    "v_mul_f32 v138, v138, v236\n"
    "v_mul_f32 v186, v186, v237\n"
    "v_mfma_f32_16x16x32_bf16 v[192:195], a[228:231], v[88:91], v[192:195]\n"
    "ds_read_b64_tr_b16 a[220:221], v227 offset:38912\n"
    "v_mul_f32 v139, v139, v236\n"
    "v_mul_f32 v187, v187, v237\n"
    "v_cvt_pk_bf16_f32 v136, v136, v137\n"
    "v_mfma_f32_16x16x32_bf16 a[112:115], a[224:227], v[96:99], a[112:115]\n"
    "ds_read_b64_tr_b16 a[222:223], v227 offset:45056\n"
    "v_cvt_pk_bf16_f32 v184, v184, v185\n"
    "v_cvt_pk_bf16_f32 v137, v138, v139\n"
    "v_cvt_pk_bf16_f32 v185, v186, v187\n"
    "v_mfma_f32_16x16x32_bf16 a[112:115], a[228:231], v[104:107], a[112:115]\n"
    "ds_write_b64 v246, v[136:137] offset:8192\n"
    "v_accvgpr_read_b32 v100, a104\n"
    "v_mfma_f32_16x16x32_bf16 a[160:163], a[224:227], v[112:115], a[160:163]\n"
    "v_accvgpr_read_b32 v108, a152\n"
    "v_accvgpr_read_b32 v101, a105\n"
    "v_accvgpr_read_b32 v109, a153\n"
    "v_accvgpr_read_b32 v102, a106\n"
    "v_mfma_f32_16x16x32_bf16 a[160:163], a[228:231], v[120:123], a[160:163]\n"
    "ds_read_b64_tr_b16 a[224:225], v226 offset:27136\n"
    "ds_write_b64 v246, v[184:185] offset:10240\n"
    "v_mfma_f32_16x16x32_bf16 v[148:151], a[232:235], v[64:67], v[148:151]\n"
    "v_accvgpr_read_b32 v110, a154\n"
    "v_accvgpr_read_b32 v103, a107\n"
    "v_accvgpr_read_b32 v111, a155\n"
    "v_mul_f32 v100, v100, v238\n"
    "v_mfma_f32_16x16x32_bf16 v[148:151], a[236:239], v[72:75], v[148:151]\n"
    "ds_read_b64_tr_b16 a[226:227], v226 offset:33280\n"
    "v_mul_f32 v108, v108, v239\n"
    "v_mul_f32 v101, v101, v238\n"
    "v_mul_f32 v109, v109, v239\n"
    "v_mfma_f32_16x16x32_bf16 v[196:199], a[232:235], v[80:83], v[196:199]\n"
    "v_mul_f32 v102, v102, v238\n"
    "v_mul_f32 v110, v110, v239\n"
    "v_mul_f32 v103, v103, v238\n"
    "v_mul_f32 v111, v111, v239\n"
    "v_mfma_f32_16x16x32_bf16 v[196:199], a[236:239], v[88:91], v[196:199]\n"
    "ds_read_b64_tr_b16 a[228:229], v226 offset:39424\n"
    "v_cvt_pk_bf16_f32 v100, v100, v101\n"
    "v_cvt_pk_bf16_f32 v108, v108, v109\n"
    "v_cvt_pk_bf16_f32 v101, v102, v103\n"
    "v_mfma_f32_16x16x32_bf16 a[116:119], a[232:235], v[96:99], a[116:119]\n"
    "ds_write_b64 v246, v[100:101] offset:12288\n"
    "v_cvt_pk_bf16_f32 v109, v110, v111\n"
    "v_mfma_f32_16x16x32_bf16 a[116:119], a[236:239], v[104:107], a[116:119]\n"
    "ds_read_b64_tr_b16 a[230:231], v226 offset:45568\n"
    "v_mul_f32 v140, v140, v236\n"
    "v_mul_f32 v188, v188, v237\n"
    "v_mul_f32 v141, v141, v236\n"
    "v_mfma_f32_16x16x32_bf16 a[164:167], a[232:235], v[112:115], a[164:167]\n"
    "ds_write_b64 v246, v[108:109] offset:14336\n"
    "v_mul_f32 v189, v189, v237\n"
    "v_mfma_f32_16x16x32_bf16 a[164:167], a[236:239], v[120:123], a[164:167]\n"
    "s_waitcnt lgkmcnt(15)\n"
    "v_mul_f32 v142, v142, v236\n"
    "v_mul_f32 v190, v190, v237\n"
    "v_mul_f32 v143, v143, v236\n"
    "v_mfma_f32_16x16x32_bf16 v[152:155], a[192:195], v[64:67], v[152:155]\n"
    "ds_read_b64_tr_b16 a[232:233], v227 offset:27136\n"
    "v_mul_f32 v191, v191, v237\n"
    "v_cvt_pk_bf16_f32 v140, v140, v141\n"
    "v_cvt_pk_bf16_f32 v188, v188, v189\n"
    "v_mfma_f32_16x16x32_bf16 v[152:155], a[196:199], v[72:75], v[152:155]\n"
    "v_cvt_pk_bf16_f32 v141, v142, v143\n"
    "v_cvt_pk_bf16_f32 v189, v190, v191\n"
    "v_accvgpr_read_b32 v116, a108\n"
    "v_accvgpr_read_b32 v124, a156\n"
    "v_mfma_f32_16x16x32_bf16 v[200:203], a[192:195], v[80:83], v[200:203]\n"
    "ds_write_b64 v247, v[140:141] offset:8192\n"
    "v_accvgpr_read_b32 v117, a109\n"
    "v_mfma_f32_16x16x32_bf16 v[200:203], a[196:199], v[88:91], v[200:203]\n"
    "ds_read_b64_tr_b16 a[234:235], v227 offset:33280\n"
    "v_accvgpr_read_b32 v125, a157\n"
    "v_accvgpr_read_b32 v118, a110\n"
    "v_accvgpr_read_b32 v126, a158\n"
    "v_mfma_f32_16x16x32_bf16 a[120:123], a[192:195], v[96:99], a[120:123]\n"
    "s_waitcnt vmcnt(6)\n"
    "ds_write_b64 v247, v[188:189] offset:10240\n"
    "ds_read_b128 a[96:99], v249 offset:8192\n"
    "v_mfma_f32_16x16x32_bf16 a[120:123], a[196:199], v[104:107], a[120:123]\n"
    "ds_read_b128 a[16:19], v253 offset:0\n"
    "ds_read_b128 a[100:103], v249 offset:9216\n"
    "v_accvgpr_read_b32 v119, a111\n"
    "v_mfma_f32_16x16x32_bf16 a[168:171], a[192:195], v[112:115], a[168:171]\n"
    "ds_read_b128 a[104:107], v249 offset:10240\n"
    "ds_read_b128 a[108:111], v249 offset:11264\n"
    "v_accvgpr_read_b32 v127, a159\n"
    "v_mul_f32 v116, v116, v238\n"
    "v_mfma_f32_16x16x32_bf16 a[168:171], a[196:199], v[120:123], a[168:171]\n"
    "ds_read_b64_tr_b16 a[236:237], v227 offset:39424\n"
    "v_mul_f32 v124, v124, v239\n"
    "v_mul_f32 v117, v117, v238\n"
    "v_mul_f32 v125, v125, v239\n"
    "v_mfma_f32_16x16x32_bf16 v[156:159], a[200:203], v[64:67], v[156:159]\n"
    "ds_read_b128 a[20:23], v254 offset:0\n"
    "v_mul_f32 v118, v118, v238\n"
    "v_mul_f32 v126, v126, v239\n"
    "v_mul_f32 v119, v119, v238\n"
    "v_mfma_f32_16x16x32_bf16 v[156:159], a[204:207], v[72:75], v[156:159]\n"
    "ds_read_b128 a[40:43], v253 offset:2048\n"
    "v_mul_f32 v127, v127, v239\n"
    "v_cvt_pk_bf16_f32 v116, v116, v117\n"
    "v_cvt_pk_bf16_f32 v124, v124, v125\n"
    "v_mfma_f32_16x16x32_bf16 v[204:207], a[200:203], v[80:83], v[204:207]\n"
    "v_cvt_pk_bf16_f32 v117, v118, v119\n"
    "v_cvt_pk_bf16_f32 v125, v126, v127\n"
    "v_mul_f32 v144, v144, v236\n"
    "v_mul_f32 v192, v192, v237\n"
    "v_mfma_f32_16x16x32_bf16 v[204:207], a[204:207], v[88:91], v[204:207]\n"
    "ds_read_b128 a[44:47], v254 offset:2048\n"
    "ds_write_b64 v247, v[116:117] offset:12288\n"
    "v_mfma_f32_16x16x32_bf16 a[124:127], a[200:203], v[96:99], a[124:127]\n"
    "ds_read_b64_tr_b16 a[238:239], v227 offset:45568\n"
    "v_mul_f32 v145, v145, v236\n"
    "v_mul_f32 v193, v193, v237\n"
    "v_mul_f32 v146, v146, v236\n"
    "v_mfma_f32_16x16x32_bf16 a[124:127], a[204:207], v[104:107], a[124:127]\n"
    "ds_read_b128 a[64:67], v253 offset:4096\n"
    "ds_write_b64 v247, v[124:125] offset:14336\n"
    "v_mfma_f32_16x16x32_bf16 a[172:175], a[200:203], v[112:115], a[172:175]\n"
    "ds_read_b128 a[144:147], v249 offset:12288\n"
    "ds_read_b128 a[148:151], v249 offset:13312\n"
    "v_mul_f32 v194, v194, v237\n"
    "v_mul_f32 v147, v147, v236\n"
    "v_mfma_f32_16x16x32_bf16 a[172:175], a[204:207], v[120:123], a[172:175]\n"
    "ds_read_b128 a[68:71], v254 offset:4096\n"
    "ds_read_b128 a[152:155], v249 offset:14336\n"
    "v_mul_f32 v195, v195, v237\n"
    "v_cvt_pk_bf16_f32 v144, v144, v145\n"
    "v_mfma_f32_16x16x32_bf16 v[160:163], a[208:211], v[64:67], v[160:163]\n"
    "ds_read_b128 a[156:159], v249 offset:15360\n"
    "s_waitcnt lgkmcnt(15)\n"
    "buffer_store_dwordx4 a[96:99], v250, s[80:83], 0 offen offset:0 nt sc1\n"
    "v_cvt_pk_bf16_f32 v192, v192, v193\n"
    "v_mfma_f32_16x16x32_bf16 v[160:163], a[212:215], v[72:75], v[160:163]\n"
    "ds_read_b128 a[88:91], v253 offset:6144\n"
    "v_cvt_pk_bf16_f32 v145, v146, v147\n"
    "v_cvt_pk_bf16_f32 v193, v194, v195\n"
    "v_accvgpr_read_b32 v68, a112\n"
    "v_mfma_f32_16x16x32_bf16 v[208:211], a[208:211], v[80:83], v[208:211]\n"
    "ds_write_b64 v248, v[144:145] offset:8192\n"
    "v_accvgpr_read_b32 v76, a160\n"
    "v_mfma_f32_16x16x32_bf16 v[208:211], a[212:215], v[88:91], v[208:211]\n"
    "ds_read_b128 a[92:95], v254 offset:6144\n"
    "v_accvgpr_read_b32 v69, a113\n"
    "v_accvgpr_read_b32 v77, a161\n"
    "v_accvgpr_read_b32 v70, a114\n"
    "v_mfma_f32_16x16x32_bf16 a[128:131], a[208:211], v[96:99], a[128:131]\n"
    "ds_write_b64 v248, v[192:193] offset:10240\n"
    "v_accvgpr_read_b32 v78, a162\n"
    "v_mfma_f32_16x16x32_bf16 a[128:131], a[212:215], v[104:107], a[128:131]\n"
    "v_accvgpr_read_b32 v71, a115\n"
    "v_accvgpr_read_b32 v79, a163\n"
    "v_mul_f32 v68, v68, v238\n"
    "v_mul_f32 v76, v76, v239\n"
    "v_mfma_f32_16x16x32_bf16 a[176:179], a[208:211], v[112:115], a[176:179]\n"
    "v_mul_f32 v69, v69, v238\n"
    "v_mul_f32 v77, v77, v239\n"
    "v_mul_f32 v70, v70, v238\n"
    "v_mul_f32 v78, v78, v239\n"
    "v_mfma_f32_16x16x32_bf16 a[176:179], a[212:215], v[120:123], a[176:179]\n"
    "s_waitcnt vmcnt(25) lgkmcnt(11)\n"
    "v_mul_f32 v71, v71, v238\n"
    "v_mul_f32 v79, v79, v239\n"
    "v_cvt_pk_bf16_f32 v68, v68, v69\n"
    "v_mfma_f32_16x16x32_bf16 v[164:167], a[216:219], v[64:67], v[164:167]\n"
    "s_barrier\n"
    "v_cvt_pk_bf16_f32 v76, v76, v77\n"
    "v_cvt_pk_bf16_f32 v69, v70, v71\n"
    "v_cvt_pk_bf16_f32 v77, v78, v79\n"
    "v_mfma_f32_16x16x32_bf16 v[164:167], a[220:223], v[72:75], v[164:167]\n"
    "ds_read_b128 a[192:195], v224 offset:0\n"
    "ds_write_b64 v248, v[68:69] offset:12288\n"
    "v_mfma_f32_16x16x32_bf16 v[212:215], a[216:219], v[80:83], v[212:215]\n"
    "ds_read_b128 a[196:199], v225 offset:0\n"
    "v_mul_f32 v148, v148, v236\n"
    "v_mul_f32 v196, v196, v237\n"
    "v_mul_f32 v149, v149, v236\n"
    "v_mfma_f32_16x16x32_bf16 v[212:215], a[220:223], v[88:91], v[212:215]\n"
    "ds_read_b128 a[200:203], v224 offset:2048\n"
    "ds_write_b64 v248, v[76:77] offset:14336\n"
    "v_mfma_f32_16x16x32_bf16 a[132:135], a[216:219], v[96:99], a[132:135]\n"
    "ds_read_b128 a[204:207], v225 offset:2048\n"
    "buffer_store_dwordx4 a[100:103], v250, s[80:83], 0 offen offset:3072 nt sc1\n"
    "v_mul_f32 v197, v197, v237\n"
    "v_mfma_f32_16x16x32_bf16 a[132:135], a[220:223], v[104:107], a[132:135]\n"
    "v_mul_f32 v150, v150, v236\n"
    "v_mul_f32 v198, v198, v237\n"
    "v_mul_f32 v151, v151, v236\n"
    "v_mul_f32 v199, v199, v237\n"
    "v_mfma_f32_16x16x32_bf16 a[180:183], a[216:219], v[112:115], a[180:183]\n"
    "ds_read_b128 a[208:211], v224 offset:4096\n"
    "v_cvt_pk_bf16_f32 v148, v148, v149\n"
    "v_cvt_pk_bf16_f32 v196, v196, v197\n"
    "v_cvt_pk_bf16_f32 v149, v150, v151\n"
    "v_mfma_f32_16x16x32_bf16 a[180:183], a[220:223], v[120:123], a[180:183]\n"
    "ds_write_b64 v245, v[148:149] offset:8192\n"
    "v_cvt_pk_bf16_f32 v197, v198, v199\n"
    "v_mfma_f32_16x16x32_bf16 v[168:171], a[224:227], v[64:67], v[168:171]\n"
    "ds_read_b128 a[212:215], v225 offset:4096\n"
    "v_accvgpr_read_b32 v84, a116\n"
    "v_accvgpr_read_b32 v92, a164\n"
    "v_accvgpr_read_b32 v85, a117\n"
    "v_mfma_f32_16x16x32_bf16 v[168:171], a[228:231], v[72:75], v[168:171]\n"
    "ds_write_b64 v245, v[196:197] offset:10240\n"
    "v_accvgpr_read_b32 v93, a165\n"
    "v_mfma_f32_16x16x32_bf16 v[216:219], a[224:227], v[80:83], v[216:219]\n"
    "v_accvgpr_read_b32 v86, a118\n"
    "v_accvgpr_read_b32 v94, a166\n"
    "v_accvgpr_read_b32 v87, a119\n"
    "v_accvgpr_read_b32 v95, a167\n"
    "v_mfma_f32_16x16x32_bf16 v[216:219], a[228:231], v[88:91], v[216:219]\n"
    "v_mul_f32 v84, v84, v238\n"
    "v_mul_f32 v92, v92, v239\n"
    "v_mul_f32 v85, v85, v238\n"
    "v_mul_f32 v93, v93, v239\n"
    "v_mfma_f32_16x16x32_bf16 a[136:139], a[224:227], v[96:99], a[136:139]\n"
    "v_mul_f32 v86, v86, v238\n"
    "v_mul_f32 v94, v94, v239\n"
    "v_mul_f32 v87, v87, v238\n"
    "v_mul_f32 v95, v95, v239\n"
    "v_mfma_f32_16x16x32_bf16 a[136:139], a[228:231], v[104:107], a[136:139]\n"
    "v_cvt_pk_bf16_f32 v84, v84, v85\n"
    "v_cvt_pk_bf16_f32 v92, v92, v93\n"
    "v_cvt_pk_bf16_f32 v85, v86, v87\n"
    "v_cvt_pk_bf16_f32 v93, v94, v95\n"
    "v_mfma_f32_16x16x32_bf16 a[184:187], a[224:227], v[112:115], a[184:187]\n"
    "ds_read_b128 a[216:219], v224 offset:6144\n"
    "ds_write_b64 v245, v[84:85] offset:12288\n"
    "v_mfma_f32_16x16x32_bf16 a[184:187], a[228:231], v[120:123], a[184:187]\n"
    "v_mul_f32 v152, v152, v236\n"
    "v_mul_f32 v200, v200, v237\n"
    "v_mul_f32 v153, v153, v236\n"
    "v_mul_f32 v201, v201, v237\n"
    "v_mfma_f32_16x16x32_bf16 v[172:175], a[232:235], v[64:67], v[172:175]\n"
    "buffer_store_dwordx4 a[104:107], v250, s[80:83], s42 offen offset:0 nt sc1\n"
    "ds_write_b64 v245, v[92:93] offset:14336\n"
    "v_mfma_f32_16x16x32_bf16 v[172:175], a[236:239], v[72:75], v[172:175]\n"
    "v_mul_f32 v154, v154, v236\n"
    "v_mul_f32 v202, v202, v237\n"
    "v_mul_f32 v155, v155, v236\n"
    "v_mfma_f32_16x16x32_bf16 v[220:223], a[232:235], v[80:83], v[220:223]\n"
    "v_mul_f32 v203, v203, v237\n"
    "v_cvt_pk_bf16_f32 v152, v152, v153\n"
    "v_cvt_pk_bf16_f32 v200, v200, v201\n"
    "v_cvt_pk_bf16_f32 v153, v154, v155\n"
    "v_mfma_f32_16x16x32_bf16 v[220:223], a[236:239], v[88:91], v[220:223]\n"
    "ds_write_b64 v246, v[152:153] offset:8192\n"
    "v_cvt_pk_bf16_f32 v201, v202, v203\n"
    "v_mfma_f32_16x16x32_bf16 a[140:143], a[232:235], v[96:99], a[140:143]\n"
    "v_accvgpr_read_b32 v100, a120\n"
    "v_accvgpr_read_b32 v108, a168\n"
    "v_accvgpr_read_b32 v101, a121\n"
    "v_accvgpr_read_b32 v109, a169\n"
    "v_mfma_f32_16x16x32_bf16 a[140:143], a[236:239], v[104:107], a[140:143]\n"
    "ds_read_b128 a[220:223], v225 offset:6144\n"
    "ds_write_b64 v246, v[200:201] offset:10240\n"
    "v_mfma_f32_16x16x32_bf16 a[188:191], a[232:235], v[112:115], a[188:191]\n"
    "s_waitcnt lgkmcnt(7)\n"
    "v_accvgpr_read_b32 v102, a122\n"
    "v_accvgpr_read_b32 v110, a170\n"
    "v_accvgpr_read_b32 v103, a123\n"
    "v_mfma_f32_16x16x32_bf16 a[188:191], a[236:239], v[120:123], a[188:191]\n"
    "v_accvgpr_read_b32 v111, a171\n"
    "v_mul_f32 v100, v100, v238\n"
    "v_mul_f32 v108, v108, v239\n"
    "v_mul_f32 v101, v101, v238\n"
    "v_mfma_f32_16x16x32_bf16 v[0:3], a[192:195], a[0:3], 0\n"
    "buffer_store_dwordx4 a[108:111], v250, s[80:83], s42 offen offset:3072 nt sc1\n"
    "v_mul_f32 v109, v109, v239\n"
    "v_mfma_f32_16x16x32_bf16 v[0:3], a[196:199], a[4:7], v[0:3]\n"
    "s_mov_b32 m0, s58\n"
    "v_mul_f32 v102, v102, v238\n"
    "v_mul_f32 v110, v110, v239\n"
    "v_mfma_f32_16x16x32_bf16 v[16:19], a[192:195], a[24:27], 0\n"
    "ds_read_b128 a[224:227], v224 offset:8192\n"
    "v_mul_f32 v103, v103, v238\n"
    "v_mul_f32 v111, v111, v239\n"
    "v_mfma_f32_16x16x32_bf16 v[16:19], a[196:199], a[28:31], v[16:19]\n"
    "buffer_load_dwordx4 v230, s[24:27], s37 offen offset:0 nt lds\n"
    "v_cvt_pk_bf16_f32 v100, v100, v101\n"
    "v_mfma_f32_16x16x32_bf16 v[32:35], a[192:195], a[48:51], 0\n"
    "ds_read_b128 a[228:231], v225 offset:8192\n"
    "v_cvt_pk_bf16_f32 v108, v108, v109\n"
    "v_cvt_pk_bf16_f32 v101, v102, v103\n"
    "v_mfma_f32_16x16x32_bf16 v[32:35], a[196:199], a[52:55], v[32:35]\n"
    "ds_write_b64 v246, v[100:101] offset:12288\n"
    "v_mfma_f32_16x16x32_bf16 v[48:51], a[192:195], a[72:75], 0\n"
    "ds_read_b128 a[232:235], v224 offset:10240\n"
    "v_cvt_pk_bf16_f32 v109, v110, v111\n"
    "v_mul_f32 v156, v156, v236\n"
    "v_mfma_f32_16x16x32_bf16 v[48:51], a[196:199], a[76:79], v[48:51]\n"
    "ds_write_b64 v246, v[108:109] offset:14336\n"
    "v_mfma_f32_16x16x32_bf16 v[4:7], a[200:203], a[0:3], 0\n"
    "buffer_load_dwordx4 v230, s[24:27], s37 offen offset:3072 nt lds\n"
    "v_mul_f32 v204, v204, v237\n"
    "v_mfma_f32_16x16x32_bf16 v[4:7], a[204:207], a[4:7], v[4:7]\n"
    "ds_read_b128 a[236:239], v225 offset:10240\n"
    "v_mul_f32 v157, v157, v236\n"
    "v_mul_f32 v205, v205, v237\n"
    "v_mfma_f32_16x16x32_bf16 v[20:23], a[200:203], a[24:27], 0\n"
    "v_mul_f32 v158, v158, v236\n"
    "v_mul_f32 v206, v206, v237\n"
    "v_mul_f32 v159, v159, v236\n"
    "v_mfma_f32_16x16x32_bf16 v[20:23], a[204:207], a[28:31], v[20:23]\n"
    "s_mov_b32 m0, s59\n"
    "v_mul_f32 v207, v207, v237\n"
    "v_cvt_pk_bf16_f32 v156, v156, v157\n"
    "v_mfma_f32_16x16x32_bf16 v[36:39], a[200:203], a[48:51], 0\n"
    "ds_read_b128 a[192:195], v224 offset:12288\n"
    "v_cvt_pk_bf16_f32 v204, v204, v205\n"
    "v_cvt_pk_bf16_f32 v157, v158, v159\n"
    "v_mfma_f32_16x16x32_bf16 v[36:39], a[204:207], a[52:55], v[36:39]\n"
    "buffer_load_dwordx4 v230, s[24:27], s37 offen offset:128 nt lds\n"
    "v_cvt_pk_bf16_f32 v205, v206, v207\n"
    "v_mfma_f32_16x16x32_bf16 v[52:55], a[200:203], a[72:75], 0\n"
    "ds_write_b64 v247, v[156:157] offset:8192\n"
    "v_mfma_f32_16x16x32_bf16 v[52:55], a[204:207], a[76:79], v[52:55]\n"
    "ds_read_b128 a[196:199], v225 offset:12288\n"
    "v_accvgpr_read_b32 v116, a124\n"
    "v_accvgpr_read_b32 v124, a172\n"
    "v_mfma_f32_16x16x32_bf16 v[8:11], a[208:211], a[0:3], 0\n"
    "ds_write_b64 v247, v[204:205] offset:10240\n"
    "v_mfma_f32_16x16x32_bf16 v[8:11], a[212:215], a[4:7], v[8:11]\n"
    "v_accvgpr_read_b32 v117, a125\n"
    "v_accvgpr_read_b32 v125, a173\n"
    "v_accvgpr_read_b32 v118, a126\n"
    "v_mfma_f32_16x16x32_bf16 v[24:27], a[208:211], a[24:27], 0\n"
    "buffer_load_dwordx4 v230, s[24:27], s37 offen offset:3200 nt lds\n"
    "v_accvgpr_read_b32 v126, a174\n"
    "v_mfma_f32_16x16x32_bf16 v[24:27], a[212:215], a[28:31], v[24:27]\n"
    "ds_read_b128 a[200:203], v224 offset:14336\n"
    "v_accvgpr_read_b32 v119, a127\n"
    "v_accvgpr_read_b32 v127, a175\n"
    "v_mfma_f32_16x16x32_bf16 v[40:43], a[208:211], a[48:51], 0\n"
    "v_mul_f32 v116, v116, v238\n"
    "v_mul_f32 v124, v124, v239\n"
    "v_mul_f32 v117, v117, v238\n"
    "v_mfma_f32_16x16x32_bf16 v[40:43], a[212:215], a[52:55], v[40:43]\n"
    "s_mov_b32 m0, s60\n"
    "v_mul_f32 v125, v125, v239\n"
    "v_mul_f32 v118, v118, v238\n"
    "v_mfma_f32_16x16x32_bf16 v[56:59], a[208:211], a[72:75], 0\n"
    "ds_read_b128 a[204:207], v225 offset:14336\n"
    "v_mul_f32 v126, v126, v239\n"
    "v_mul_f32 v119, v119, v238\n"
    "v_mfma_f32_16x16x32_bf16 v[56:59], a[212:215], a[76:79], v[56:59]\n"
    "s_waitcnt lgkmcnt(6)\n"
    "v_mul_f32 v127, v127, v239\n"
    "v_cvt_pk_bf16_f32 v116, v116, v117\n"
    "v_mfma_f32_16x16x32_bf16 v[12:15], a[216:219], a[0:3], 0\n"
    "buffer_load_dwordx4 v230, s[24:27], s37 offen offset:256 nt lds\n"
    "v_cvt_pk_bf16_f32 v124, v124, v125\n"
    "v_mfma_f32_16x16x32_bf16 v[12:15], a[220:223], a[4:7], v[12:15]\n"
    "v_cvt_pk_bf16_f32 v117, v118, v119\n"
    "v_cvt_pk_bf16_f32 v125, v126, v127\n"
    "v_mul_f32 v160, v160, v236\n"
    "v_mfma_f32_16x16x32_bf16 v[28:31], a[216:219], a[24:27], 0\n"
    "ds_read_b128 a[208:211], v224 offset:16384\n"
    "ds_write_b64 v247, v[116:117] offset:12288\n"
    "v_mfma_f32_16x16x32_bf16 v[28:31], a[220:223], a[28:31], v[28:31]\n"
    "v_mul_f32 v208, v208, v237\n"
    "v_mul_f32 v161, v161, v236\n"
    "v_mfma_f32_16x16x32_bf16 v[44:47], a[216:219], a[48:51], 0\n"
    "ds_write_b64 v247, v[124:125] offset:14336\n"
    "v_mfma_f32_16x16x32_bf16 v[44:47], a[220:223], a[52:55], v[44:47]\n"
    "buffer_load_dwordx4 v230, s[24:27], s37 offen offset:3328 nt lds\n"
    "v_mul_f32 v209, v209, v237\n"
    "v_mfma_f32_16x16x32_bf16 v[60:63], a[216:219], a[72:75], 0\n"
    "s_add_u32 s37, s37, 24576\n"
    "v_mul_f32 v162, v162, v236\n"
    "v_mul_f32 v210, v210, v237\n"
    "v_mfma_f32_16x16x32_bf16 v[60:63], a[220:223], a[76:79], v[60:63]\n"
    "ds_read_b128 a[212:215], v225 offset:16384\n"
    "v_mul_f32 v163, v163, v236\n"
    "v_mul_f32 v211, v211, v237\n"
    "v_mfma_f32_16x16x32_bf16 v[0:3], a[224:227], a[8:11], v[0:3]\n"
    "v_cvt_pk_bf16_f32 v160, v160, v161\n"
    "v_cvt_pk_bf16_f32 v208, v208, v209\n"
    "v_cvt_pk_bf16_f32 v161, v162, v163\n"
    "v_mfma_f32_16x16x32_bf16 v[0:3], a[228:231], a[12:15], v[0:3]\n"
    "ds_write_b64 v248, v[160:161] offset:0\n"
    "v_mfma_f32_16x16x32_bf16 v[16:19], a[224:227], a[32:35], v[16:19]\n"
    "v_cvt_pk_bf16_f32 v209, v210, v211\n"
    "v_accvgpr_read_b32 v68, a128\n"
    "v_accvgpr_read_b32 v76, a176\n"
    "v_mfma_f32_16x16x32_bf16 v[16:19], a[228:231], a[36:39], v[16:19]\n"
    "ds_read_b128 a[216:219], v224 offset:18432\n"
    "ds_write_b64 v248, v[208:209] offset:2048\n"
    "v_mfma_f32_16x16x32_bf16 v[32:35], a[224:227], a[56:59], v[32:35]\n"
    "v_accvgpr_read_b32 v69, a129\n"
    "v_accvgpr_read_b32 v77, a177\n"
    "v_mfma_f32_16x16x32_bf16 v[32:35], a[228:231], a[60:63], v[32:35]\n"
    "v_accvgpr_read_b32 v70, a130\n"
    "v_accvgpr_read_b32 v78, a178\n"
    "v_accvgpr_read_b32 v71, a131\n"
    "v_mfma_f32_16x16x32_bf16 v[48:51], a[224:227], a[80:83], v[48:51]\n"
    "v_accvgpr_read_b32 v79, a179\n"
    "v_mul_f32 v68, v68, v238\n"
    "v_mul_f32 v76, v76, v239\n"
    "v_mul_f32 v69, v69, v238\n"
    "v_mfma_f32_16x16x32_bf16 v[48:51], a[228:231], a[84:87], v[48:51]\n"
    "ds_read_b128 a[220:223], v225 offset:18432\n"
    "v_mul_f32 v77, v77, v239\n"
    "v_mul_f32 v70, v70, v238\n"
    "v_mfma_f32_16x16x32_bf16 v[4:7], a[232:235], a[8:11], v[4:7]\n"
    "v_mul_f32 v78, v78, v239\n"
    "v_mul_f32 v71, v71, v238\n"
    "v_mul_f32 v79, v79, v239\n"
    "v_mfma_f32_16x16x32_bf16 v[4:7], a[236:239], a[12:15], v[4:7]\n"
    "v_cvt_pk_bf16_f32 v68, v68, v69\n"
    "v_cvt_pk_bf16_f32 v76, v76, v77\n"
    "v_cvt_pk_bf16_f32 v69, v70, v71\n"
    "v_mfma_f32_16x16x32_bf16 v[20:23], a[232:235], a[32:35], v[20:23]\n"
    "ds_write_b64 v248, v[68:69] offset:4096\n"
    "v_mfma_f32_16x16x32_bf16 v[20:23], a[236:239], a[36:39], v[20:23]\n"
    "ds_read_b128 a[224:227], v224 offset:20480\n"
    "v_cvt_pk_bf16_f32 v77, v78, v79\n"
    "v_mul_f32 v164, v164, v236\n"
    "v_mfma_f32_16x16x32_bf16 v[36:39], a[232:235], a[56:59], v[36:39]\n"
    "ds_write_b64 v248, v[76:77] offset:6144\n"
    "v_mfma_f32_16x16x32_bf16 v[36:39], a[236:239], a[60:63], v[36:39]\n"
    "v_mul_f32 v212, v212, v237\n"
    "v_mul_f32 v165, v165, v236\n"
    "v_mul_f32 v213, v213, v237\n"
    "v_mfma_f32_16x16x32_bf16 v[52:55], a[232:235], a[80:83], v[52:55]\n"
    "v_mul_f32 v166, v166, v236\n"
    "v_mul_f32 v214, v214, v237\n"
    "v_mul_f32 v167, v167, v236\n"
    "v_mfma_f32_16x16x32_bf16 v[52:55], a[236:239], a[84:87], v[52:55]\n"
    "s_waitcnt lgkmcnt(11)\n"
    "v_mul_f32 v215, v215, v237\n"
    "v_cvt_pk_bf16_f32 v164, v164, v165\n"
    "v_mfma_f32_16x16x32_bf16 v[8:11], a[192:195], a[8:11], v[8:11]\n"
    "ds_read_b128 a[228:231], v225 offset:20480\n"
    "v_cvt_pk_bf16_f32 v212, v212, v213\n"
    "v_cvt_pk_bf16_f32 v165, v166, v167\n"
    "v_mfma_f32_16x16x32_bf16 v[8:11], a[196:199], a[12:15], v[8:11]\n"
    "ds_write_b64 v245, v[164:165] offset:0\n"
    "v_mfma_f32_16x16x32_bf16 v[24:27], a[192:195], a[32:35], v[24:27]\n"
    "v_cvt_pk_bf16_f32 v213, v214, v215\n"
    "v_accvgpr_read_b32 v84, a132\n"
    "v_accvgpr_read_b32 v92, a180\n"
    "v_mfma_f32_16x16x32_bf16 v[24:27], a[196:199], a[36:39], v[24:27]\n"
    "ds_write_b64 v245, v[212:213] offset:2048\n"
    "v_mfma_f32_16x16x32_bf16 v[40:43], a[192:195], a[56:59], v[40:43]\n"
    "v_accvgpr_read_b32 v85, a133\n"
    "v_accvgpr_read_b32 v93, a181\n"
    "v_accvgpr_read_b32 v86, a134\n"
    "v_mfma_f32_16x16x32_bf16 v[40:43], a[196:199], a[60:63], v[40:43]\n"
    "ds_read_b128 a[232:235], v224 offset:22528\n"
    "v_accvgpr_read_b32 v94, a182\n"
    "v_accvgpr_read_b32 v87, a135\n"
    "v_mfma_f32_16x16x32_bf16 v[56:59], a[192:195], a[80:83], v[56:59]\n"
    "v_accvgpr_read_b32 v95, a183\n"
    "v_mul_f32 v84, v84, v238\n"
    "v_mul_f32 v92, v92, v239\n"
    "v_mfma_f32_16x16x32_bf16 v[56:59], a[196:199], a[84:87], v[56:59]\n"
    "v_mul_f32 v85, v85, v238\n"
    "v_mul_f32 v93, v93, v239\n"
    "v_mul_f32 v86, v86, v238\n"
    "v_mfma_f32_16x16x32_bf16 v[12:15], a[200:203], a[8:11], v[12:15]\n"
    "v_mul_f32 v94, v94, v239\n"
    "v_mul_f32 v87, v87, v238\n"
    "v_mul_f32 v95, v95, v239\n"
    "v_mfma_f32_16x16x32_bf16 v[12:15], a[204:207], a[12:15], v[12:15]\n"
    "v_cvt_pk_bf16_f32 v84, v84, v85\n"
    "v_cvt_pk_bf16_f32 v92, v92, v93\n"
    "v_cvt_pk_bf16_f32 v85, v86, v87\n"
    "v_mfma_f32_16x16x32_bf16 v[28:31], a[200:203], a[32:35], v[28:31]\n"
    "ds_write_b64 v245, v[84:85] offset:4096\n"
    "v_mfma_f32_16x16x32_bf16 v[28:31], a[204:207], a[36:39], v[28:31]\n"
    "v_cvt_pk_bf16_f32 v93, v94, v95\n"
    "v_mul_f32 v168, v168, v236\n"
    "v_mul_f32 v216, v216, v237\n"
    "v_mfma_f32_16x16x32_bf16 v[44:47], a[200:203], a[56:59], v[44:47]\n"
    "ds_read_b128 a[236:239], v225 offset:22528\n"
    "ds_write_b64 v245, v[92:93] offset:6144\n"
    "v_mfma_f32_16x16x32_bf16 v[44:47], a[204:207], a[60:63], v[44:47]\n"
    "v_mul_f32 v169, v169, v236\n"
    "v_mul_f32 v217, v217, v237\n"
    "v_mfma_f32_16x16x32_bf16 v[60:63], a[200:203], a[80:83], v[60:63]\n"
    "v_mul_f32 v170, v170, v236\n"
    "v_mul_f32 v218, v218, v237\n"
    "v_mul_f32 v171, v171, v236\n"
    "v_mfma_f32_16x16x32_bf16 v[60:63], a[204:207], a[84:87], v[60:63]\n"
    "s_waitcnt lgkmcnt(14)\n"
    "v_mul_f32 v219, v219, v237\n"
    "v_cvt_pk_bf16_f32 v168, v168, v169\n"
    "v_mfma_f32_16x16x32_bf16 v[0:3], a[208:211], a[16:19], v[0:3]\n"
    "v_cvt_pk_bf16_f32 v216, v216, v217\n"
    "v_cvt_pk_bf16_f32 v169, v170, v171\n"
    "v_cvt_pk_bf16_f32 v217, v218, v219\n"
    "v_mfma_f32_16x16x32_bf16 v[0:3], a[212:215], a[20:23], v[0:3]\n"
    "ds_write_b64 v246, v[168:169] offset:0\n"
    "v_mfma_f32_16x16x32_bf16 v[16:19], a[208:211], a[40:43], v[16:19]\n"
    "v_accvgpr_read_b32 v100, a136\n"
    "v_accvgpr_read_b32 v108, a184\n"
    "v_accvgpr_read_b32 v101, a137\n"
    "v_mfma_f32_16x16x32_bf16 v[16:19], a[212:215], a[44:47], v[16:19]\n"
    "ds_write_b64 v246, v[216:217] offset:2048\n"
    "v_mfma_f32_16x16x32_bf16 v[32:35], a[208:211], a[64:67], v[32:35]\n"
    "v_accvgpr_read_b32 v109, a185\n"
    "v_accvgpr_read_b32 v102, a138\n"
    "v_accvgpr_read_b32 v110, a186\n"
    "v_mfma_f32_16x16x32_bf16 v[32:35], a[212:215], a[68:71], v[32:35]\n"
    "v_accvgpr_read_b32 v103, a139\n"
    "v_accvgpr_read_b32 v111, a187\n"
    "v_mul_f32 v100, v100, v238\n"
    "v_mfma_f32_16x16x32_bf16 v[48:51], a[208:211], a[88:91], v[48:51]\n"
    "v_mul_f32 v108, v108, v239\n"
    "v_mul_f32 v101, v101, v238\n"
    "v_mul_f32 v109, v109, v239\n"
    "v_mfma_f32_16x16x32_bf16 v[48:51], a[212:215], a[92:95], v[48:51]\n"
    "s_waitcnt vmcnt(10) lgkmcnt(3)\n"
    "v_mul_f32 v102, v102, v238\n"
    "v_mul_f32 v110, v110, v239\n"
    "v_mfma_f32_16x16x32_bf16 v[4:7], a[216:219], a[16:19], v[4:7]\n"
    "s_barrier\n"
    "v_mul_f32 v103, v103, v238\n"
    "v_mul_f32 v111, v111, v239\n"
    "v_mfma_f32_16x16x32_bf16 v[4:7], a[220:223], a[20:23], v[4:7]\n"
    "ds_read_b128 a[192:195], v224 offset:24576\n"
    "v_cvt_pk_bf16_f32 v100, v100, v101\n"
    "v_cvt_pk_bf16_f32 v108, v108, v109\n"
    "v_mfma_f32_16x16x32_bf16 v[20:23], a[216:219], a[40:43], v[20:23]\n"
    "ds_read_b128 a[196:199], v225 offset:24576\n"
    "v_cvt_pk_bf16_f32 v101, v102, v103\n"
    "v_cvt_pk_bf16_f32 v109, v110, v111\n"
    "v_mul_f32 v172, v172, v236\n"
    "v_mfma_f32_16x16x32_bf16 v[20:23], a[220:223], a[44:47], v[20:23]\n"
    "ds_read_b128 a[200:203], v224 offset:32768\n"
    "ds_write_b64 v246, v[100:101] offset:4096\n"
    "v_mfma_f32_16x16x32_bf16 v[36:39], a[216:219], a[64:67], v[36:39]\n"
    "ds_read_b128 a[204:207], v225 offset:32768\n"
    "v_mul_f32 v220, v220, v237\n"
    "v_mfma_f32_16x16x32_bf16 v[36:39], a[220:223], a[68:71], v[36:39]\n"
    "ds_write_b64 v246, v[108:109] offset:6144\n"
    "v_mfma_f32_16x16x32_bf16 v[52:55], a[216:219], a[88:91], v[52:55]\n"
    "ds_read_b128 a[208:211], v224 offset:40960\n"
    "v_mul_f32 v173, v173, v236\n"
    "v_mfma_f32_16x16x32_bf16 v[52:55], a[220:223], a[92:95], v[52:55]\n"
    "v_mul_f32 v221, v221, v237\n"
    "v_mul_f32 v174, v174, v236\n"
    "v_mul_f32 v222, v222, v237\n"
    "v_mul_f32 v175, v175, v236\n"
    "v_mfma_f32_16x16x32_bf16 v[8:11], a[224:227], a[16:19], v[8:11]\n"
    "v_mul_f32 v223, v223, v237\n"
    "v_cvt_pk_bf16_f32 v172, v172, v173\n"
    "v_cvt_pk_bf16_f32 v220, v220, v221\n"
    "v_mfma_f32_16x16x32_bf16 v[8:11], a[228:231], a[20:23], v[8:11]\n"
    "ds_read_b128 a[212:215], v225 offset:40960\n"
    "v_cvt_pk_bf16_f32 v173, v174, v175\n"
    "v_cvt_pk_bf16_f32 v221, v222, v223\n"
    "v_mfma_f32_16x16x32_bf16 v[24:27], a[224:227], a[40:43], v[24:27]\n"
    "ds_write_b64 v247, v[172:173] offset:0\n"
    "v_mfma_f32_16x16x32_bf16 v[24:27], a[228:231], a[44:47], v[24:27]\n"
    "ds_read_b128 a[216:219], v224 offset:26624\n"
    "v_accvgpr_read_b32 v116, a140\n"
    "v_accvgpr_read_b32 v124, a188\n"
    "v_mfma_f32_16x16x32_bf16 v[40:43], a[224:227], a[64:67], v[40:43]\n"
    "ds_write_b64 v247, v[220:221] offset:2048\n"
    "v_mfma_f32_16x16x32_bf16 v[40:43], a[228:231], a[68:71], v[40:43]\n"
    "ds_read_b128 a[220:223], v225 offset:26624\n"
    "v_accvgpr_read_b32 v117, a141\n"
    "v_accvgpr_read_b32 v125, a189\n"
    "v_mfma_f32_16x16x32_bf16 v[56:59], a[224:227], a[88:91], v[56:59]\n"
    "v_accvgpr_read_b32 v118, a142\n"
    "v_accvgpr_read_b32 v126, a190\n"
    "v_accvgpr_read_b32 v119, a143\n"
    "v_mfma_f32_16x16x32_bf16 v[56:59], a[228:231], a[92:95], v[56:59]\n"
    "v_accvgpr_read_b32 v127, a191\n"
    "v_mul_f32 v116, v116, v238\n"
    "v_mul_f32 v124, v124, v239\n"
    "v_mfma_f32_16x16x32_bf16 v[12:15], a[232:235], a[16:19], v[12:15]\n"
    "v_mul_f32 v117, v117, v238\n"
    "v_mul_f32 v125, v125, v239\n"
    "v_mul_f32 v118, v118, v238\n"
    "v_mfma_f32_16x16x32_bf16 v[12:15], a[236:239], a[20:23], v[12:15]\n"
    "ds_read_b128 a[224:227], v224 offset:34816\n"
    "v_mul_f32 v126, v126, v239\n"
    "v_mul_f32 v119, v119, v238\n"
    "v_mfma_f32_16x16x32_bf16 v[28:31], a[232:235], a[40:43], v[28:31]\n"
    "v_mul_f32 v127, v127, v239\n"
    "v_cvt_pk_bf16_f32 v116, v116, v117\n"
    "v_cvt_pk_bf16_f32 v124, v124, v125\n"
    "v_mfma_f32_16x16x32_bf16 v[28:31], a[236:239], a[44:47], v[28:31]\n"
    "v_cvt_pk_bf16_f32 v117, v118, v119\n"
    "v_cvt_pk_bf16_f32 v125, v126, v127\n"
    "v_mfma_f32_16x16x32_bf16 v[44:47], a[232:235], a[64:67], v[44:47]\n"
    "ds_write_b64 v247, v[116:117] offset:4096\n"
    "v_mfma_f32_16x16x32_bf16 v[44:47], a[236:239], a[68:71], v[44:47]\n"
    "ds_read_b128 a[228:231], v225 offset:34816\n"
    "v_mfma_f32_16x16x32_bf16 v[60:63], a[232:235], a[88:91], v[60:63]\n"
    "ds_write_b64 v247, v[124:125] offset:6144\n"
    "v_mfma_f32_16x16x32_bf16 v[60:63], a[236:239], a[92:95], v[60:63]\n"
    "s_waitcnt lgkmcnt(8)\n"
    "s_mov_b32 s84, s80\n"
    "s_mov_b32 s85, s81\n"
    "s_mov_b32 s86, s82\n"
    "s_mov_b32 s80, s64\n"
    "s_mov_b32 s81, s65\n"
    "s_mov_b32 s82, s66\n"
    "s_mov_b32 s30, 0\n"
    "s_mov_b32 s46, 1\n"
    "s_mov_b32 s49, 0x30000\n"
    "s_mov_b32 s16, 0\n"
    "v_mfma_f32_16x16x32_bf16 v[64:67], a[192:195], a[0:3], 0\n"
    "s_mov_b32 s39, s49\n"
    "v_max3_f32 v236, v0, v1, v2\n"
    "v_max3_f32 v237, v16, v17, v18\n"
    "v_max3_f32 v238, v32, v33, v34\n"
    "v_mfma_f32_16x16x32_bf16 v[64:67], a[196:199], a[4:7], v[64:67]\n"
    "s_mov_b32 s38, 0\n"
    "v_max3_f32 v239, v48, v49, v50\n"
    "v_max3_f32 v236, v236, v3, v4\n"
    "v_max3_f32 v237, v237, v19, v20\n"
    "v_max3_f32 v238, v238, v35, v36\n"
    "v_mfma_f32_16x16x32_bf16 v[64:67], a[200:203], a[8:11], v[64:67]\n"
    "s_mov_b32 m0, s52\n"
    "v_max3_f32 v239, v239, v51, v52\n"
    "v_max3_f32 v236, v236, v5, v6\n"
    "v_max3_f32 v237, v237, v21, v22\n"
    "v_mfma_f32_16x16x32_bf16 v[64:67], a[204:207], a[12:15], v[64:67]\n"
    "ds_read_b128 a[232:235], v224 offset:43008\n"
    "v_max3_f32 v238, v238, v37, v38\n"
    "v_max3_f32 v239, v239, v53, v54\n"
    "v_max3_f32 v236, v236, v7, v8\n"
    "v_max3_f32 v237, v237, v23, v24\n"
    "v_mfma_f32_16x16x32_bf16 v[64:67], a[208:211], a[16:19], v[64:67]\n"
    "buffer_load_dwordx4 v228, s[20:23], s36 offen offset:0 nt lds\n"
    "v_max3_f32 v238, v238, v39, v40\n"
    "v_max3_f32 v239, v239, v55, v56\n"
    "v_max3_f32 v236, v236, v9, v10\n"
    "v_mfma_f32_16x16x32_bf16 v[64:67], a[212:215], a[20:23], v[64:67]\n"
    "v_max3_f32 v237, v237, v25, v26\n"
    "v_max3_f32 v238, v238, v41, v42\n"
    "v_max3_f32 v239, v239, v57, v58\n"
    "v_max3_f32 v236, v236, v11, v12\n"
    "v_mfma_f32_16x16x32_bf16 v[80:83], a[192:195], a[24:27], 0\n"
    "v_max3_f32 v237, v237, v27, v28\n"
    "v_max3_f32 v238, v238, v43, v44\n"
    "v_max3_f32 v239, v239, v59, v60\n"
    "v_max3_f32 v236, v236, v13, v14\n"
    "v_max3_f32 v237, v237, v29, v30\n"
    "v_mfma_f32_16x16x32_bf16 v[80:83], a[196:199], a[28:31], v[80:83]\n"
    "ds_read_b128 a[236:239], v225 offset:43008\n"
    "v_max3_f32 v238, v238, v45, v46\n"
    "v_max3_f32 v239, v239, v61, v62\n"
    "v_max_f32 v236, v236, v15\n"
    "v_mfma_f32_16x16x32_bf16 v[80:83], a[200:203], a[32:35], v[80:83]\n"
    "v_max_f32 v237, v237, v31\n"
    "v_max_f32 v238, v238, v47\n"
    "v_max_f32 v239, v239, v63\n"
    "v_mov_b32 v231, v236\n"
    "v_mov_b32 v255, v237\n"
    "v_mfma_f32_16x16x32_bf16 v[80:83], a[204:207], a[36:39], v[80:83]\n"
    "buffer_load_dwordx4 v229, s[20:23], s36 offen offset:1024 nt lds\n"
    "v_mov_b32 v253, v238\n"
    "v_mov_b32 v254, v239\n"
    "v_mfma_f32_16x16x32_bf16 v[80:83], a[208:211], a[40:43], v[80:83]\n"
    "v_permlane16_swap_b32 v236, v231\n"
    "v_permlane16_swap_b32 v237, v255\n"
    "v_max_f32 v236, v236, v231\n"
    "v_mfma_f32_16x16x32_bf16 v[80:83], a[212:215], a[44:47], v[80:83]\n"
    "v_permlane16_swap_b32 v238, v253\n"
    "v_permlane16_swap_b32 v239, v254\n"
    "v_max_f32 v237, v237, v255\n"
    "v_mfma_f32_16x16x32_bf16 v[96:99], a[192:195], a[48:51], 0\n"
    "s_mov_b32 m0, s53\n"
    "v_max_f32 v238, v238, v253\n"
    "v_max_f32 v239, v239, v254\n"
    "v_mov_b32 v231, v236\n"
    "v_mov_b32 v255, v237\n"
    "v_mfma_f32_16x16x32_bf16 v[96:99], a[196:199], a[52:55], v[96:99]\n"
    "buffer_store_dwordx4 a[144:147], v250, s[84:87], s43 offen offset:0 nt sc1\n"
    "v_mov_b32 v253, v238\n"
    "v_mov_b32 v254, v239\n"
    "v_mfma_f32_16x16x32_bf16 v[96:99], a[200:203], a[56:59], v[96:99]\n"
    "buffer_load_dwordx4 v228, s[20:23], s36 offen offset:128 nt lds\n"
    "v_permlane32_swap_b32 v236, v231\n"
    "v_max_f32 v236, v236, v231\n"
    "v_mfma_f32_16x16x32_bf16 v[96:99], a[204:207], a[60:63], v[96:99]\n"
    "s_mov_b32 s78, 0\n"
    "v_permlane32_swap_b32 v237, v255\n"
    "v_max_f32 v237, v237, v255\n"
    "v_mfma_f32_16x16x32_bf16 v[96:99], a[208:211], a[64:67], v[96:99]\n"
    "s_mov_b32 s79, s43\n"
    "v_permlane32_swap_b32 v238, v253\n"
    "v_permlane32_swap_b32 v239, v254\n"
    "v_mfma_f32_16x16x32_bf16 v[96:99], a[212:215], a[68:71], v[96:99]\n"
    "s_add_u32 s17, s49, 6144\n"
    "v_max_f32 v238, v238, v253\n"
    "v_max_f32 v239, v239, v254\n"
    "v_mul_f32 v232, s47, v236\n"
    "v_mul_f32 v233, s47, v237\n"
    "v_mfma_f32_16x16x32_bf16 v[112:115], a[192:195], a[72:75], 0\n"
    "s_mov_b32 s18, s44\n"
    "v_mul_f32 v234, s47, v238\n"
    "v_mul_f32 v235, s47, v239\n"
    "v_add_f32 v235, s15, v235\n"
    "v_mfma_f32_16x16x32_bf16 v[112:115], a[196:199], a[76:79], v[112:115]\n"
    "buffer_load_dwordx4 v229, s[20:23], s36 offen offset:1152 nt lds\n"
    "v_fma_f32 v0, v0, s40, v232\n"
    "v_exp_f32 v0, v0\n"
    "v_mfma_f32_16x16x32_bf16 v[112:115], a[200:203], a[80:83], v[112:115]\n"
    "ds_read_b128 a[192:195], v224 offset:28672\n"
    "v_fma_f32 v1, v1, s40, v232\n"
    "v_exp_f32 v1, v1\n"
    "v_mfma_f32_16x16x32_bf16 v[112:115], a[204:207], a[84:87], v[112:115]\n"
    "v_fma_f32 v2, v2, s40, v232\n"
    "v_exp_f32 v2, v2\n"
    "v_fma_f32 v3, v3, s40, v232\n"
    "v_fma_f32 v16, v16, s40, v233\n"
    "v_mfma_f32_16x16x32_bf16 v[112:115], a[208:211], a[88:91], v[112:115]\n"
    "s_mov_b32 m0, s54\n"
    "ds_read_b128 a[196:199], v225 offset:28672\n"
    "v_exp_f32 v3, v3\n"
    "v_fma_f32 v17, v17, s40, v233\n"
    "v_mfma_f32_16x16x32_bf16 v[112:115], a[212:215], a[92:95], v[112:115]\n"
    "s_waitcnt lgkmcnt(2)\n"
    "v_exp_f32 v16, v16\n"
    "v_fma_f32 v18, v18, s40, v233\n"
    "v_mfma_f32_16x16x32_bf16 v[68:71], a[216:219], a[0:3], 0\n"
    "buffer_load_dwordx4 v228, s[20:23], s36 offen offset:256 nt lds\n"
    "v_exp_f32 v17, v17\n"
    "v_fma_f32 v19, v19, s40, v233\n"
    "v_mfma_f32_16x16x32_bf16 v[68:71], a[220:223], a[4:7], v[68:71]\n"
    "ds_read_b128 a[200:203], v224 offset:36864\n"
    "v_exp_f32 v18, v18\n"
    "v_exp_f32 v19, v19\n"
    "v_mfma_f32_16x16x32_bf16 v[68:71], a[224:227], a[8:11], v[68:71]\n"
    "v_fma_f32 v32, v32, s40, v234\n"
    "v_exp_f32 v32, v32\n"
    "v_fma_f32 v33, v33, s40, v234\n"
    "v_mfma_f32_16x16x32_bf16 v[68:71], a[228:231], a[12:15], v[68:71]\n"
    "ds_read_b128 a[204:207], v225 offset:36864\n"
    "v_exp_f32 v33, v33\n"
    "v_cvt_pk_bf16_f32 v0, v0, v1\n"
    "v_fma_f32 v34, v34, s40, v234\n"
    "v_mfma_f32_16x16x32_bf16 v[68:71], a[232:235], a[16:19], v[68:71]\n"
    "v_exp_f32 v34, v34\n"
    "v_fma_f32 v35, v35, s40, v234\n"
    "v_cvt_pk_bf16_f32 v1, v2, v3\n"
    "v_mfma_f32_16x16x32_bf16 v[68:71], a[236:239], a[20:23], v[68:71]\n"
    "buffer_load_dwordx4 v229, s[20:23], s36 offen offset:1280 nt lds\n"
    "v_exp_f32 v35, v35\n"
    "v_fma_f32 v48, v48, s40, v235\n"
    "v_mfma_f32_16x16x32_bf16 v[84:87], a[216:219], a[24:27], 0\n"
    "s_add_u32 s36, s36, 24576\n"
    "v_exp_f32 v48, v48\n"
    "v_fma_f32 v49, v49, s40, v235\n"
    "v_cvt_pk_bf16_f32 v16, v16, v17\n"
    "v_mfma_f32_16x16x32_bf16 v[84:87], a[220:223], a[28:31], v[84:87]\n"
    "ds_read_b128 a[208:211], v224 offset:45056\n"
    "v_exp_f32 v49, v49\n"
    "v_fma_f32 v50, v50, s40, v235\n"
    "v_mfma_f32_16x16x32_bf16 v[84:87], a[224:227], a[32:35], v[84:87]\n"
    "buffer_store_dwordx4 a[148:151], v250, s[84:87], s43 offen offset:3072 nt sc1\n"
    "v_exp_f32 v50, v50\n"
    "v_fma_f32 v51, v51, s40, v235\n"
    "v_mfma_f32_16x16x32_bf16 v[84:87], a[228:231], a[36:39], v[84:87]\n"
    "ds_read_b128 a[212:215], v225 offset:45056\n"
    "v_exp_f32 v51, v51\n"
    "v_cvt_pk_bf16_f32 v17, v18, v19\n"
    "v_mfma_f32_16x16x32_bf16 v[84:87], a[232:235], a[40:43], v[84:87]\n"
    "v_fma_f32 v4, v4, s40, v232\n"
    "v_exp_f32 v4, v4\n"
    "v_fma_f32 v5, v5, s40, v232\n"
    "v_cvt_pk_bf16_f32 v32, v32, v33\n"
    "v_mfma_f32_16x16x32_bf16 v[84:87], a[236:239], a[44:47], v[84:87]\n"
    "v_exp_f32 v5, v5\n"
    "v_fma_f32 v6, v6, s40, v232\n"
    "v_exp_f32 v6, v6\n"
    "v_mfma_f32_16x16x32_bf16 v[100:103], a[216:219], a[48:51], 0\n"
    "v_fma_f32 v7, v7, s40, v232\n"
    "v_exp_f32 v7, v7\n"
    "v_cvt_pk_bf16_f32 v33, v34, v35\n"
    "v_mfma_f32_16x16x32_bf16 v[100:103], a[220:223], a[52:55], v[100:103]\n"
    "v_fma_f32 v20, v20, s40, v233\n"
    "v_exp_f32 v20, v20\n"
    "v_fma_f32 v21, v21, s40, v233\n"
    "v_cvt_pk_bf16_f32 v48, v48, v49\n"
    "v_mfma_f32_16x16x32_bf16 v[100:103], a[224:227], a[56:59], v[100:103]\n"
    "v_exp_f32 v21, v21\n"
    "v_fma_f32 v22, v22, s40, v233\n"
    "v_exp_f32 v22, v22\n"
    "v_mfma_f32_16x16x32_bf16 v[100:103], a[228:231], a[60:63], v[100:103]\n"
    "v_fma_f32 v23, v23, s40, v233\n"
    "v_exp_f32 v23, v23\n"
    "v_cvt_pk_bf16_f32 v49, v50, v51\n"
    "v_mfma_f32_16x16x32_bf16 v[100:103], a[232:235], a[64:67], v[100:103]\n"
    "v_fma_f32 v36, v36, s40, v234\n"
    "v_exp_f32 v36, v36\n"
    "v_fma_f32 v37, v37, s40, v234\n"
    "v_cvt_pk_bf16_f32 v2, v4, v5\n"
    "v_mfma_f32_16x16x32_bf16 v[100:103], a[236:239], a[68:71], v[100:103]\n"
    "v_exp_f32 v37, v37\n"
    "v_fma_f32 v38, v38, s40, v234\n"
    "v_fma_f32 v39, v39, s40, v234\n"
    "v_mfma_f32_16x16x32_bf16 v[116:119], a[216:219], a[72:75], 0\n"
    "v_exp_f32 v38, v38\n"
    "v_exp_f32 v39, v39\n"
    "v_cvt_pk_bf16_f32 v3, v6, v7\n"
    "v_mfma_f32_16x16x32_bf16 v[116:119], a[220:223], a[76:79], v[116:119]\n"
    "ds_read_b128 a[216:219], v224 offset:30720\n"
    "v_fma_f32 v52, v52, s40, v235\n"
    "v_exp_f32 v52, v52\n"
    "v_fma_f32 v53, v53, s40, v235\n"
    "v_mfma_f32_16x16x32_bf16 v[116:119], a[224:227], a[80:83], v[116:119]\n"
    "ds_read_b128 a[220:223], v225 offset:30720\n"
    "v_exp_f32 v53, v53\n"
    "v_cvt_pk_bf16_f32 v18, v20, v21\n"
    "v_mfma_f32_16x16x32_bf16 v[116:119], a[228:231], a[84:87], v[116:119]\n"
    "ds_read_b128 a[224:227], v224 offset:38912\n"
    "v_fma_f32 v54, v54, s40, v235\n"
    "v_exp_f32 v54, v54\n"
    "v_fma_f32 v55, v55, s40, v235\n"
    "v_mfma_f32_16x16x32_bf16 v[116:119], a[232:235], a[88:91], v[116:119]\n"
    "v_exp_f32 v55, v55\n"
    "v_cvt_pk_bf16_f32 v19, v22, v23\n"
    "v_fma_f32 v8, v8, s40, v232\n"
    "v_mfma_f32_16x16x32_bf16 v[116:119], a[236:239], a[92:95], v[116:119]\n"
    "s_waitcnt lgkmcnt(3)\n"
    "v_exp_f32 v8, v8\n"
    "v_fma_f32 v9, v9, s40, v232\n"
    "v_cvt_pk_bf16_f32 v34, v36, v37\n"
    "v_mfma_f32_16x16x32_bf16 v[72:75], a[192:195], a[0:3], 0\n"
    "ds_read_b128 a[228:231], v225 offset:38912\n"
    "v_exp_f32 v9, v9\n"
    "v_fma_f32 v10, v10, s40, v232\n"
    "v_fma_f32 v11, v11, s40, v232\n"
    "v_mfma_f32_16x16x32_bf16 v[72:75], a[196:199], a[4:7], v[72:75]\n"
    "v_exp_f32 v10, v10\n"
    "v_exp_f32 v11, v11\n"
    "v_mfma_f32_16x16x32_bf16 v[72:75], a[200:203], a[8:11], v[72:75]\n"
    "ds_read_b128 a[232:235], v224 offset:47104\n"
    "v_cvt_pk_bf16_f32 v35, v38, v39\n"
    "v_fma_f32 v24, v24, s40, v233\n"
    "v_exp_f32 v24, v24\n"
    "v_mfma_f32_16x16x32_bf16 v[72:75], a[204:207], a[12:15], v[72:75]\n"
    "v_fma_f32 v25, v25, s40, v233\n"
    "v_exp_f32 v25, v25\n"
    "v_cvt_pk_bf16_f32 v50, v52, v53\n"
    "v_fma_f32 v26, v26, s40, v233\n"
    "v_mfma_f32_16x16x32_bf16 v[72:75], a[208:211], a[16:19], v[72:75]\n"
    "buffer_store_dwordx4 a[152:155], v250, s[84:87], s44 offen offset:0 nt sc1\n"
    "v_exp_f32 v26, v26\n"
    "v_mfma_f32_16x16x32_bf16 v[72:75], a[212:215], a[20:23], v[72:75]\n"
    "v_fma_f32 v27, v27, s40, v233\n"
    "v_exp_f32 v27, v27\n"
    "v_cvt_pk_bf16_f32 v51, v54, v55\n"
    "v_fma_f32 v40, v40, s40, v234\n"
    "v_mfma_f32_16x16x32_bf16 v[88:91], a[192:195], a[24:27], 0\n"
    "ds_read_b128 a[236:239], v225 offset:47104\n"
    "v_exp_f32 v40, v40\n"
    "v_fma_f32 v41, v41, s40, v234\n"
    "v_mfma_f32_16x16x32_bf16 v[88:91], a[196:199], a[28:31], v[88:91]\n"
    "v_exp_f32 v41, v41\n"
    "v_cvt_pk_bf16_f32 v8, v8, v9\n"
    "v_fma_f32 v42, v42, s40, v234\n"
    "v_fma_f32 v43, v43, s40, v234\n"
    "v_mfma_f32_16x16x32_bf16 v[88:91], a[200:203], a[32:35], v[88:91]\n"
    "v_exp_f32 v42, v42\n"
    "v_exp_f32 v43, v43\n"
    "v_cvt_pk_bf16_f32 v9, v10, v11\n"
    "v_mfma_f32_16x16x32_bf16 v[88:91], a[204:207], a[36:39], v[88:91]\n"
    "v_fma_f32 v56, v56, s40, v235\n"
    "v_exp_f32 v56, v56\n"
    "v_fma_f32 v57, v57, s40, v235\n"
    "v_mfma_f32_16x16x32_bf16 v[88:91], a[208:211], a[40:43], v[88:91]\n"
    "v_exp_f32 v57, v57\n"
    "v_cvt_pk_bf16_f32 v24, v24, v25\n"
    "v_fma_f32 v58, v58, s40, v235\n"
    "v_fma_f32 v59, v59, s40, v235\n"
    "v_mfma_f32_16x16x32_bf16 v[88:91], a[212:215], a[44:47], v[88:91]\n"
    "v_exp_f32 v58, v58\n"
    "v_exp_f32 v59, v59\n"
    "v_mfma_f32_16x16x32_bf16 v[104:107], a[192:195], a[48:51], 0\n"
    "v_cvt_pk_bf16_f32 v25, v26, v27\n"
    "v_fma_f32 v12, v12, s40, v232\n"
    "v_exp_f32 v12, v12\n"
    "v_fma_f32 v13, v13, s40, v232\n"
    "v_mfma_f32_16x16x32_bf16 v[104:107], a[196:199], a[52:55], v[104:107]\n"
    "v_exp_f32 v13, v13\n"
    "v_cvt_pk_bf16_f32 v40, v40, v41\n"
    "v_fma_f32 v14, v14, s40, v232\n"
    "v_fma_f32 v15, v15, s40, v232\n"
    "v_mfma_f32_16x16x32_bf16 v[104:107], a[200:203], a[56:59], v[104:107]\n"
    "v_exp_f32 v14, v14\n"
    "v_exp_f32 v15, v15\n"
    "v_mfma_f32_16x16x32_bf16 v[104:107], a[204:207], a[60:63], v[104:107]\n"
    "v_cvt_pk_bf16_f32 v41, v42, v43\n"
    "v_fma_f32 v28, v28, s40, v233\n"
    "v_exp_f32 v28, v28\n"
    "v_fma_f32 v29, v29, s40, v233\n"
    "v_mfma_f32_16x16x32_bf16 v[104:107], a[208:211], a[64:67], v[104:107]\n"
    "v_exp_f32 v29, v29\n"
    "v_cvt_pk_bf16_f32 v56, v56, v57\n"
    "v_fma_f32 v30, v30, s40, v233\n"
    "v_fma_f32 v31, v31, s40, v233\n"
    "v_mfma_f32_16x16x32_bf16 v[104:107], a[212:215], a[68:71], v[104:107]\n"
    "s_waitcnt vmcnt(9) lgkmcnt(0)\n"
    "v_exp_f32 v30, v30\n"
    "v_cvt_pk_bf16_f32 v57, v58, v59\n"
    "v_mfma_f32_16x16x32_bf16 v[120:123], a[192:195], a[72:75], 0\n"
    "s_barrier\n"
    "v_exp_f32 v31, v31\n"
    "v_fma_f32 v44, v44, s40, v234\n"
    "v_fma_f32 v45, v45, s40, v234\n"
    "v_mfma_f32_16x16x32_bf16 v[120:123], a[196:199], a[76:79], v[120:123]\n"
    "ds_read_b64_tr_b16 a[192:193], v226 offset:0\n"
    "v_exp_f32 v44, v44\n"
    "v_cvt_pk_bf16_f32 v10, v12, v13\n"
    "v_mfma_f32_16x16x32_bf16 v[120:123], a[200:203], a[80:83], v[120:123]\n"
    "ds_read_b64_tr_b16 a[194:195], v226 offset:6144\n"
    "v_exp_f32 v45, v45\n"
    "v_fma_f32 v46, v46, s40, v234\n"
    "v_fma_f32 v47, v47, s40, v234\n"
    "v_mfma_f32_16x16x32_bf16 v[120:123], a[204:207], a[84:87], v[120:123]\n"
    "ds_read_b64_tr_b16 a[196:197], v226 offset:12288\n"
    "v_exp_f32 v46, v46\n"
    "v_exp_f32 v47, v47\n"
    "v_mfma_f32_16x16x32_bf16 v[120:123], a[208:211], a[88:91], v[120:123]\n"
    "ds_read_b64_tr_b16 a[198:199], v226 offset:18432\n"
    "v_cvt_pk_bf16_f32 v11, v14, v15\n"
    "v_fma_f32 v60, v60, s40, v235\n"
    "v_fma_f32 v61, v61, s40, v235\n"
    "v_mfma_f32_16x16x32_bf16 v[120:123], a[212:215], a[92:95], v[120:123]\n"
    "ds_read_b64_tr_b16 a[200:201], v227 offset:0\n"
    "v_exp_f32 v60, v60\n"
    "v_exp_f32 v61, v61\n"
    "v_mfma_f32_16x16x32_bf16 v[76:79], a[216:219], a[0:3], 0\n"
    "ds_read_b64_tr_b16 a[202:203], v227 offset:6144\n"
    "v_cvt_pk_bf16_f32 v26, v28, v29\n"
    "v_fma_f32 v62, v62, s40, v235\n"
    "v_fma_f32 v63, v63, s40, v235\n"
    "v_mfma_f32_16x16x32_bf16 v[76:79], a[220:223], a[4:7], v[76:79]\n"
    "ds_read_b64_tr_b16 a[204:205], v227 offset:12288\n"
    "v_exp_f32 v62, v62\n"
    "v_exp_f32 v63, v63\n"
    "v_mfma_f32_16x16x32_bf16 v[76:79], a[224:227], a[8:11], v[76:79]\n"
    "ds_read_b64_tr_b16 a[206:207], v227 offset:18432\n"
    "v_cvt_pk_bf16_f32 v27, v30, v31\n"
    "v_cvt_pk_bf16_f32 v42, v44, v45\n"
    "v_cvt_pk_bf16_f32 v43, v46, v47\n"
    "v_cvt_pk_bf16_f32 v58, v60, v61\n"
    "v_mfma_f32_16x16x32_bf16 v[76:79], a[228:231], a[12:15], v[76:79]\n"
    "ds_read_b64_tr_b16 a[208:209], v226 offset:512\n"
    "v_cvt_pk_bf16_f32 v59, v62, v63\n"
    "v_fma_f32 v64, v64, s40, v232\n"
    "v_fma_f32 v65, v65, s40, v232\n"
    "v_mfma_f32_16x16x32_bf16 v[76:79], a[232:235], a[16:19], v[76:79]\n"
    "buffer_store_dwordx4 a[156:159], v250, s[84:87], s44 offen offset:3072 nt sc1\n"
    "v_exp_f32 v64, v64\n"
    "v_fma_f32 v66, v66, s40, v232\n"
    "v_mfma_f32_16x16x32_bf16 v[76:79], a[236:239], a[20:23], v[76:79]\n"
    "ds_read_b64_tr_b16 a[210:211], v226 offset:6656\n"
    "v_exp_f32 v65, v65\n"
    "v_exp_f32 v66, v66\n"
    "v_mfma_f32_16x16x32_bf16 v[92:95], a[216:219], a[24:27], 0\n"
    "v_fma_f32 v67, v67, s40, v232\n"
    "v_exp_f32 v67, v67\n"
    "v_fma_f32 v80, v80, s40, v233\n"
    "v_mfma_f32_16x16x32_bf16 v[92:95], a[220:223], a[28:31], v[92:95]\n"
    "v_exp_f32 v80, v80\n"
    "v_fma_f32 v81, v81, s40, v233\n"
    "v_exp_f32 v81, v81\n"
    "v_mfma_f32_16x16x32_bf16 v[92:95], a[224:227], a[32:35], v[92:95]\n"
    "ds_read_b64_tr_b16 a[212:213], v226 offset:12800\n"
    "v_fma_f32 v82, v82, s40, v233\n"
    "v_exp_f32 v82, v82\n"
    "v_mfma_f32_16x16x32_bf16 v[92:95], a[228:231], a[36:39], v[92:95]\n"
    "v_fma_f32 v83, v83, s40, v233\n"
    "v_exp_f32 v83, v83\n"
    "v_fma_f32 v96, v96, s40, v234\n"
    "v_fma_f32 v97, v97, s40, v234\n"
    "v_mfma_f32_16x16x32_bf16 v[92:95], a[232:235], a[40:43], v[92:95]\n"
    "v_exp_f32 v96, v96\n"
    "v_exp_f32 v97, v97\n"
    "v_cvt_pk_bf16_f32 v64, v64, v65\n"
    "v_mfma_f32_16x16x32_bf16 v[92:95], a[236:239], a[44:47], v[92:95]\n"
    "v_fma_f32 v98, v98, s40, v234\n"
    "v_exp_f32 v98, v98\n"
    "v_fma_f32 v99, v99, s40, v234\n"
    "v_mfma_f32_16x16x32_bf16 v[108:111], a[216:219], a[48:51], 0\n"
    "ds_read_b64_tr_b16 a[214:215], v226 offset:18944\n"
    "v_exp_f32 v99, v99\n"
    "v_cvt_pk_bf16_f32 v65, v66, v67\n"
    "v_fma_f32 v112, v112, s40, v235\n"
    "v_mfma_f32_16x16x32_bf16 v[108:111], a[220:223], a[52:55], v[108:111]\n"
    "v_exp_f32 v112, v112\n"
    "v_fma_f32 v113, v113, s40, v235\n"
    "v_cvt_pk_bf16_f32 v80, v80, v81\n"
    "v_mfma_f32_16x16x32_bf16 v[108:111], a[224:227], a[56:59], v[108:111]\n"
    "v_exp_f32 v113, v113\n"
    "v_fma_f32 v114, v114, s40, v235\n"
    "v_exp_f32 v114, v114\n"
    "v_mfma_f32_16x16x32_bf16 v[108:111], a[228:231], a[60:63], v[108:111]\n"
    "v_fma_f32 v115, v115, s40, v235\n"
    "v_exp_f32 v115, v115\n"
    "v_cvt_pk_bf16_f32 v81, v82, v83\n"
    "v_fma_f32 v68, v68, s40, v232\n"
    "v_mfma_f32_16x16x32_bf16 v[108:111], a[232:235], a[64:67], v[108:111]\n"
    "v_exp_f32 v68, v68\n"
    "v_fma_f32 v69, v69, s40, v232\n"
    "v_cvt_pk_bf16_f32 v96, v96, v97\n"
    "v_mfma_f32_16x16x32_bf16 v[108:111], a[236:239], a[68:71], v[108:111]\n"
    "v_exp_f32 v69, v69\n"
    "v_fma_f32 v70, v70, s40, v232\n"
    "v_exp_f32 v70, v70\n"
    "v_mfma_f32_16x16x32_bf16 v[124:127], a[216:219], a[72:75], 0\n"
    "v_fma_f32 v71, v71, s40, v232\n"
    "v_exp_f32 v71, v71\n"
    "v_cvt_pk_bf16_f32 v97, v98, v99\n"
    "v_fma_f32 v84, v84, s40, v233\n"
    "v_mfma_f32_16x16x32_bf16 v[124:127], a[220:223], a[76:79], v[124:127]\n"
    "v_exp_f32 v84, v84\n"
    "v_fma_f32 v85, v85, s40, v233\n"
    "v_cvt_pk_bf16_f32 v112, v112, v113\n"
    "v_mfma_f32_16x16x32_bf16 v[124:127], a[224:227], a[80:83], v[124:127]\n"
    "v_exp_f32 v85, v85\n"
    "v_fma_f32 v86, v86, s40, v233\n"
    "v_exp_f32 v86, v86\n"
    "v_mfma_f32_16x16x32_bf16 v[124:127], a[228:231], a[84:87], v[124:127]\n"
    "v_fma_f32 v87, v87, s40, v233\n"
    "v_exp_f32 v87, v87\n"
    "v_cvt_pk_bf16_f32 v113, v114, v115\n"
    "v_mfma_f32_16x16x32_bf16 v[124:127], a[232:235], a[88:91], v[124:127]\n"
    "v_cvt_pk_bf16_f32 v66, v68, v69\n"
    "v_cvt_pk_bf16_f32 v67, v70, v71\n"
    "v_cvt_pk_bf16_f32 v82, v84, v85\n"
    "v_cvt_pk_bf16_f32 v83, v86, v87\n"
    "v_mov_b32 v251, v249\n"
    "v_mfma_f32_16x16x32_bf16 v[124:127], a[236:239], a[92:95], v[124:127]\n"
    "s_waitcnt lgkmcnt(0)\n"
    "v_mfma_f32_16x16x32_bf16 v[128:131], a[192:195], v[0:3], 0\n"
    "ds_read_b64_tr_b16 a[216:217], v227 offset:512\n"
    "v_fma_f32 v100, v100, s40, v234\n"
    "v_mfma_f32_16x16x32_bf16 v[128:131], a[196:199], v[8:11], v[128:131]\n"
    "s_mov_b32 m0, s61\n"
    "v_fma_f32 v101, v101, s40, v234\n"
    "v_mfma_f32_16x16x32_bf16 v[176:179], a[192:195], v[16:19], 0\n"
    "ds_read_b64_tr_b16 a[218:219], v227 offset:6656\n"
    "v_fma_f32 v102, v102, s40, v234\n"
    "v_mfma_f32_16x16x32_bf16 v[176:179], a[196:199], v[24:27], v[176:179]\n"
    "buffer_load_dwordx4 v230, s[24:27], s37 offen offset:0 nt lds\n"
    "v_mfma_f32_16x16x32_bf16 a[96:99], a[192:195], v[32:35], 0\n"
    "ds_read_b64_tr_b16 a[220:221], v227 offset:12800\n"
    "v_fma_f32 v103, v103, s40, v234\n"
    "v_mfma_f32_16x16x32_bf16 a[96:99], a[196:199], v[40:43], a[96:99]\n"
    "ds_read_b64_tr_b16 a[222:223], v227 offset:18944\n"
    "v_fma_f32 v116, v116, s40, v235\n"
    "v_mfma_f32_16x16x32_bf16 a[144:147], a[192:195], v[48:51], 0\n"
    "ds_read_b64_tr_b16 a[224:225], v226 offset:1024\n"
    "v_fma_f32 v117, v117, s40, v235\n"
    "v_mfma_f32_16x16x32_bf16 a[144:147], a[196:199], v[56:59], a[144:147]\n"
    "ds_read_b64_tr_b16 a[226:227], v226 offset:7168\n"
    "v_fma_f32 v118, v118, s40, v235\n"
    "v_mfma_f32_16x16x32_bf16 v[132:135], a[200:203], v[0:3], 0\n"
    "buffer_load_dwordx4 v230, s[24:27], s37 offen offset:3072 nt lds\n"
    "v_mfma_f32_16x16x32_bf16 v[132:135], a[204:207], v[8:11], v[132:135]\n"
    "ds_read_b64_tr_b16 a[228:229], v226 offset:13312\n"
    "v_fma_f32 v119, v119, s40, v235\n"
    "v_mfma_f32_16x16x32_bf16 v[180:183], a[200:203], v[16:19], 0\n"
    "ds_read_b64_tr_b16 a[230:231], v226 offset:19456\n"
    "v_fma_f32 v72, v72, s40, v232\n"
    "v_mfma_f32_16x16x32_bf16 v[180:183], a[204:207], v[24:27], v[180:183]\n"
    "s_mov_b32 m0, s62\n"
    "v_fma_f32 v73, v73, s40, v232\n"
    "v_mfma_f32_16x16x32_bf16 a[100:103], a[200:203], v[32:35], 0\n"
    "ds_read_b64_tr_b16 a[232:233], v227 offset:1024\n"
    "v_fma_f32 v74, v74, s40, v232\n"
    "v_mfma_f32_16x16x32_bf16 a[100:103], a[204:207], v[40:43], a[100:103]\n"
    "buffer_load_dwordx4 v230, s[24:27], s37 offen offset:128 nt lds\n"
    "ds_read_b64_tr_b16 a[234:235], v227 offset:7168\n"
    "v_mfma_f32_16x16x32_bf16 a[148:151], a[200:203], v[48:51], 0\n"
    "ds_read_b64_tr_b16 a[236:237], v227 offset:13312\n"
    "v_fma_f32 v75, v75, s40, v232\n"
    "v_mfma_f32_16x16x32_bf16 a[148:151], a[204:207], v[56:59], a[148:151]\n"
    "ds_read_b64_tr_b16 a[238:239], v227 offset:19456\n"
    "v_fma_f32 v88, v88, s40, v233\n"
    "v_mfma_f32_16x16x32_bf16 a[240:243], v[240:243], v[0:3], 0\n"
    "ds_read_b64_tr_b16 a[192:193], v226 offset:1536\n"
    "v_fma_f32 v89, v89, s40, v233\n"
    "v_mfma_f32_16x16x32_bf16 a[240:243], v[240:243], v[8:11], a[240:243]\n"
    "ds_read_b64_tr_b16 a[194:195], v226 offset:7680\n"
    "v_fma_f32 v90, v90, s40, v233\n"
    "v_mfma_f32_16x16x32_bf16 v[136:139], a[208:211], v[0:3], 0\n"
    "buffer_load_dwordx4 v230, s[24:27], s37 offen offset:3200 nt lds\n"
    "v_mfma_f32_16x16x32_bf16 v[136:139], a[212:215], v[8:11], v[136:139]\n"
    "ds_read_b64_tr_b16 a[196:197], v226 offset:13824\n"
    "v_fma_f32 v91, v91, s40, v233\n"
    "v_mfma_f32_16x16x32_bf16 v[184:187], a[208:211], v[16:19], 0\n"
    "ds_read_b64_tr_b16 a[198:199], v226 offset:19968\n"
    "v_fma_f32 v104, v104, s40, v234\n"
    "v_mfma_f32_16x16x32_bf16 v[184:187], a[212:215], v[24:27], v[184:187]\n"
    "s_mov_b32 m0, s63\n"
    "v_fma_f32 v105, v105, s40, v234\n"
    "v_mfma_f32_16x16x32_bf16 a[104:107], a[208:211], v[32:35], 0\n"
    "ds_read_b64_tr_b16 a[200:201], v227 offset:1536\n"
    "v_fma_f32 v106, v106, s40, v234\n"
    "v_mfma_f32_16x16x32_bf16 a[104:107], a[212:215], v[40:43], a[104:107]\n"
    "buffer_load_dwordx4 v230, s[24:27], s37 offen offset:256 nt lds\n"
    "v_mfma_f32_16x16x32_bf16 a[152:155], a[208:211], v[48:51], 0\n"
    "ds_read_b64_tr_b16 a[202:203], v227 offset:7680\n"
    "v_fma_f32 v107, v107, s40, v234\n"
    "v_mfma_f32_16x16x32_bf16 a[152:155], a[212:215], v[56:59], a[152:155]\n"
    "s_waitcnt lgkmcnt(6)\n"
    "v_fma_f32 v120, v120, s40, v235\n"
    "v_mfma_f32_16x16x32_bf16 v[140:143], a[216:219], v[0:3], 0\n"
    "ds_read_b64_tr_b16 a[204:205], v227 offset:13824\n"
    "v_fma_f32 v121, v121, s40, v235\n"
    "v_mfma_f32_16x16x32_bf16 v[140:143], a[220:223], v[8:11], v[140:143]\n"
    "ds_read_b64_tr_b16 a[206:207], v227 offset:19968\n"
    "v_fma_f32 v122, v122, s40, v235\n"
    "v_mfma_f32_16x16x32_bf16 v[188:191], a[216:219], v[16:19], 0\n"
    "buffer_load_dwordx4 v230, s[24:27], s37 offen offset:3328 nt lds\n"
    "v_mfma_f32_16x16x32_bf16 v[188:191], a[220:223], v[24:27], v[188:191]\n"
    "s_add_u32 s37, s37, 24576\n"
    "v_exp_f32 v100, v100\n"
    "v_mfma_f32_16x16x32_bf16 a[108:111], a[216:219], v[32:35], 0\n"
    "ds_read_b64_tr_b16 a[208:209], v226 offset:2048\n"
    "v_fma_f32 v123, v123, s40, v235\n"
    "v_mfma_f32_16x16x32_bf16 a[108:111], a[220:223], v[40:43], a[108:111]\n"
    "ds_read_b64_tr_b16 a[210:211], v226 offset:8192\n"
    "v_mfma_f32_16x16x32_bf16 a[156:159], a[216:219], v[48:51], 0\n"
    "ds_read_b64_tr_b16 a[212:213], v226 offset:14336\n"
    "v_mfma_f32_16x16x32_bf16 a[156:159], a[220:223], v[56:59], a[156:159]\n"
    "ds_read_b64_tr_b16 a[214:215], v226 offset:20480\n"
    "v_mfma_f32_16x16x32_bf16 v[144:147], a[224:227], v[0:3], 0\n"
    "ds_read_b64_tr_b16 a[216:217], v227 offset:2048\n"
    "v_mfma_f32_16x16x32_bf16 v[144:147], a[228:231], v[8:11], v[144:147]\n"
    "ds_read_b64_tr_b16 a[218:219], v227 offset:8192\n"
    "v_mfma_f32_16x16x32_bf16 v[192:195], a[224:227], v[16:19], 0\n"
    "ds_read_b64_tr_b16 a[220:221], v227 offset:14336\n"
    "v_mfma_f32_16x16x32_bf16 v[192:195], a[228:231], v[24:27], v[192:195]\n"
    "v_exp_f32 v101, v101\n"
    "v_mfma_f32_16x16x32_bf16 a[112:115], a[224:227], v[32:35], 0\n"
    "ds_read_b64_tr_b16 a[222:223], v227 offset:20480\n"
    "v_cvt_pk_bf16_f32 v98, v100, v101\n"
    "v_mfma_f32_16x16x32_bf16 a[112:115], a[228:231], v[40:43], a[112:115]\n"
    "v_exp_f32 v102, v102\n"
    "v_mfma_f32_16x16x32_bf16 a[160:163], a[224:227], v[48:51], 0\n"
    "v_exp_f32 v103, v103\n"
    "v_mfma_f32_16x16x32_bf16 a[160:163], a[228:231], v[56:59], a[160:163]\n"
    "v_exp_f32 v116, v116\n"
    "v_cvt_pk_bf16_f32 v99, v102, v103\n"
    "v_mfma_f32_16x16x32_bf16 a[244:247], v[240:243], v[16:19], 0\n"
    "ds_read_b64_tr_b16 a[224:225], v226 offset:2560\n"
    "v_fma_f32 v76, v76, s40, v232\n"
    "v_mfma_f32_16x16x32_bf16 a[244:247], v[240:243], v[24:27], a[244:247]\n"
    "v_exp_f32 v117, v117\n"
    "v_mfma_f32_16x16x32_bf16 v[148:151], a[232:235], v[0:3], 0\n"
    "ds_read_b64_tr_b16 a[226:227], v226 offset:8704\n"
    "v_cvt_pk_bf16_f32 v114, v116, v117\n"
    "v_mfma_f32_16x16x32_bf16 v[148:151], a[236:239], v[8:11], v[148:151]\n"
    "v_exp_f32 v118, v118\n"
    "v_mfma_f32_16x16x32_bf16 v[196:199], a[232:235], v[16:19], 0\n"
    "v_exp_f32 v119, v119\n"
    "v_mfma_f32_16x16x32_bf16 v[196:199], a[236:239], v[24:27], v[196:199]\n"
    "ds_read_b64_tr_b16 a[228:229], v226 offset:14848\n"
    "v_cvt_pk_bf16_f32 v115, v118, v119\n"
    "v_mfma_f32_16x16x32_bf16 a[116:119], a[232:235], v[32:35], 0\n"
    "v_exp_f32 v72, v72\n"
    "v_mfma_f32_16x16x32_bf16 a[116:119], a[236:239], v[40:43], a[116:119]\n"
    "v_exp_f32 v73, v73\n"
    "v_fma_f32 v77, v77, s40, v232\n"
    "v_mfma_f32_16x16x32_bf16 a[164:167], a[232:235], v[48:51], 0\n"
    "ds_read_b64_tr_b16 a[230:231], v226 offset:20992\n"
    "v_cvt_pk_bf16_f32 v72, v72, v73\n"
    "v_mfma_f32_16x16x32_bf16 a[164:167], a[236:239], v[56:59], a[164:167]\n"
    "s_waitcnt lgkmcnt(4)\n"
    "v_fma_f32 v78, v78, s40, v232\n"
    "v_mfma_f32_16x16x32_bf16 v[152:155], a[192:195], v[0:3], 0\n"
    "v_exp_f32 v74, v74\n"
    "v_mfma_f32_16x16x32_bf16 v[152:155], a[196:199], v[8:11], v[152:155]\n"
    "ds_read_b64_tr_b16 a[232:233], v227 offset:2560\n"
    "v_fma_f32 v79, v79, s40, v232\n"
    "v_mfma_f32_16x16x32_bf16 v[200:203], a[192:195], v[16:19], 0\n"
    "v_exp_f32 v75, v75\n"
    "v_mfma_f32_16x16x32_bf16 v[200:203], a[196:199], v[24:27], v[200:203]\n"
    "v_exp_f32 v88, v88\n"
    "v_mfma_f32_16x16x32_bf16 a[120:123], a[192:195], v[32:35], 0\n"
    "v_exp_f32 v89, v89\n"
    "v_mfma_f32_16x16x32_bf16 a[120:123], a[196:199], v[40:43], a[120:123]\n"
    "ds_read_b64_tr_b16 a[234:235], v227 offset:8704\n"
    "v_cvt_pk_bf16_f32 v73, v74, v75\n"
    "v_mfma_f32_16x16x32_bf16 a[168:171], a[192:195], v[48:51], 0\n"
    "s_mov_b32 m0, s55\n"
    "v_exp_f32 v90, v90\n"
    "v_mfma_f32_16x16x32_bf16 a[168:171], a[196:199], v[56:59], a[168:171]\n"
    "v_exp_f32 v91, v91\n"
    "v_mfma_f32_16x16x32_bf16 v[156:159], a[200:203], v[0:3], 0\n"
    "buffer_load_dwordx4 v228, s[20:23], s36 offen offset:0 nt lds\n"
    "v_mfma_f32_16x16x32_bf16 v[156:159], a[204:207], v[8:11], v[156:159]\n"
    "v_exp_f32 v104, v104\n"
    "v_mfma_f32_16x16x32_bf16 v[204:207], a[200:203], v[16:19], 0\n"
    "ds_read_b64_tr_b16 a[236:237], v227 offset:14848\n"
    "v_cvt_pk_bf16_f32 v88, v88, v89\n"
    "v_mfma_f32_16x16x32_bf16 v[204:207], a[204:207], v[24:27], v[204:207]\n"
    "ds_read_b128 v[244:247], v251 offset:8192\n"
    "v_cvt_pk_bf16_f32 v89, v90, v91\n"
    "v_mfma_f32_16x16x32_bf16 a[124:127], a[200:203], v[32:35], 0\n"
    "v_exp_f32 v105, v105\n"
    "v_mfma_f32_16x16x32_bf16 a[124:127], a[204:207], v[40:43], a[124:127]\n"
    "buffer_load_dwordx4 v229, s[20:23], s36 offen offset:1024 nt lds\n"
    "v_mfma_f32_16x16x32_bf16 a[172:175], a[200:203], v[48:51], 0\n"
    "v_exp_f32 v106, v106\n"
    "v_cvt_pk_bf16_f32 v104, v104, v105\n"
    "v_mfma_f32_16x16x32_bf16 a[172:175], a[204:207], v[56:59], a[172:175]\n"
    "v_exp_f32 v107, v107\n"
    "v_mfma_f32_16x16x32_bf16 a[248:251], v[240:243], v[32:35], 0\n"
    "s_mov_b32 m0, s56\n"
    "v_cvt_pk_bf16_f32 v105, v106, v107\n"
    "v_mfma_f32_16x16x32_bf16 a[248:251], v[240:243], v[40:43], a[248:251]\n"
    "ds_read_b64_tr_b16 a[238:239], v227 offset:20992\n"
    "v_fma_f32 v92, v92, s40, v233\n"
    "v_mfma_f32_16x16x32_bf16 v[160:163], a[208:211], v[0:3], 0\n"
    "buffer_load_dwordx4 v228, s[20:23], s36 offen offset:128 nt lds\n"
    "v_mfma_f32_16x16x32_bf16 v[160:163], a[212:215], v[8:11], v[160:163]\n"
    "v_exp_f32 v120, v120\n"
    "v_mfma_f32_16x16x32_bf16 v[208:211], a[208:211], v[16:19], 0\n"
    "v_exp_f32 v121, v121\n"
    "v_mfma_f32_16x16x32_bf16 v[208:211], a[212:215], v[24:27], v[208:211]\n"
    "v_exp_f32 v122, v122\n"
    "v_mfma_f32_16x16x32_bf16 a[128:131], a[208:211], v[32:35], 0\n"
    "v_exp_f32 v123, v123\n"
    "v_fma_f32 v93, v93, s40, v233\n"
    "v_mfma_f32_16x16x32_bf16 a[128:131], a[212:215], v[40:43], a[128:131]\n"
    "buffer_load_dwordx4 v229, s[20:23], s36 offen offset:1152 nt lds\n"
    "v_mfma_f32_16x16x32_bf16 a[176:179], a[208:211], v[48:51], 0\n"
    "v_exp_f32 v76, v76\n"
    "v_mfma_f32_16x16x32_bf16 a[176:179], a[212:215], v[56:59], a[176:179]\n"
    "v_exp_f32 v77, v77\n"
    "v_mfma_f32_16x16x32_bf16 v[164:167], a[216:219], v[0:3], 0\n"
    "s_mov_b32 m0, s57\n"
    "v_cvt_pk_bf16_f32 v120, v120, v121\n"
    "v_mfma_f32_16x16x32_bf16 v[164:167], a[220:223], v[8:11], v[164:167]\n"
    "s_waitcnt vmcnt(13) lgkmcnt(0)\n"
    "v_fma_f32 v94, v94, s40, v233\n"
    "v_mfma_f32_16x16x32_bf16 v[212:215], a[216:219], v[16:19], 0\n"
    "s_barrier\n"
    "buffer_store_dwordx4 v[244:247], v250, s[84:87], s78 offen offset:128 nt sc1\n"
    "v_mfma_f32_16x16x32_bf16 v[212:215], a[220:223], v[24:27], v[212:215]\n"
    "buffer_load_dwordx4 v228, s[20:23], s36 offen offset:256 nt lds\n"
    "v_mfma_f32_16x16x32_bf16 a[132:135], a[216:219], v[32:35], 0\n"
    "ds_read_b128 a[192:195], v224 offset:0\n"
    "v_fma_f32 v95, v95, s40, v233\n"
    "v_mfma_f32_16x16x32_bf16 a[132:135], a[220:223], v[40:43], a[132:135]\n"
    "ds_read_b128 a[196:199], v225 offset:0\n"
    "v_exp_f32 v78, v78\n"
    "v_mfma_f32_16x16x32_bf16 a[180:183], a[216:219], v[48:51], 0\n"
    "ds_read_b128 a[200:203], v224 offset:8192\n"
    "v_cvt_pk_bf16_f32 v121, v122, v123\n"
    "v_mfma_f32_16x16x32_bf16 a[180:183], a[220:223], v[56:59], a[180:183]\n"
    "ds_read_b128 a[204:207], v225 offset:8192\n"
    "v_fma_f32 v108, v108, s40, v234\n"
    "v_mfma_f32_16x16x32_bf16 v[168:171], a[224:227], v[0:3], 0\n"
    "buffer_load_dwordx4 v229, s[20:23], s36 offen offset:1280 nt lds\n"
    "v_mfma_f32_16x16x32_bf16 v[168:171], a[228:231], v[8:11], v[168:171]\n"
    "s_add_u32 s36, s36, 24576\n"
    "v_fma_f32 v109, v109, s40, v234\n"
    "v_mfma_f32_16x16x32_bf16 v[216:219], a[224:227], v[16:19], 0\n"
    "ds_read_b128 a[208:211], v224 offset:16384\n"
    "v_cvt_pk_bf16_f32 v74, v76, v77\n"
    "v_mfma_f32_16x16x32_bf16 v[216:219], a[228:231], v[24:27], v[216:219]\n"
    "s_mov_b32 s45, 2\n"
    "v_fma_f32 v110, v110, s40, v234\n"
    "v_mfma_f32_16x16x32_bf16 a[136:139], a[224:227], v[32:35], 0\n"
    "ds_read_b128 a[212:215], v225 offset:16384\n"
    "v_fma_f32 v111, v111, s40, v234\n"
    "v_mfma_f32_16x16x32_bf16 a[136:139], a[228:231], v[40:43], a[136:139]\n"
    "v_exp_f32 v79, v79\n"
    "v_mfma_f32_16x16x32_bf16 a[184:187], a[224:227], v[48:51], 0\n"
    "ds_read_b128 a[216:219], v224 offset:2048\n"
    "v_exp_f32 v92, v92\n"
    "v_mfma_f32_16x16x32_bf16 a[184:187], a[228:231], v[56:59], a[184:187]\n"
    "v_exp_f32 v93, v93\n"
    "v_mfma_f32_16x16x32_bf16 a[252:255], v[240:243], v[48:51], 0\n"
    "ds_read_b128 a[220:223], v225 offset:2048\n"
    "v_cvt_pk_bf16_f32 v75, v78, v79\n"
    "v_mfma_f32_16x16x32_bf16 a[252:255], v[240:243], v[56:59], a[252:255]\n"
    "v_exp_f32 v94, v94\n"
    "v_mfma_f32_16x16x32_bf16 v[172:175], a[232:235], v[0:3], 0\n"
    "v_exp_f32 v95, v95\n"
    "v_mfma_f32_16x16x32_bf16 v[172:175], a[236:239], v[8:11], v[172:175]\n"
    "ds_read_b128 a[224:227], v224 offset:10240\n"
    "v_cvt_pk_bf16_f32 v90, v92, v93\n"
    "v_mfma_f32_16x16x32_bf16 v[220:223], a[232:235], v[16:19], 0\n"
    "v_exp_f32 v108, v108\n"
    "v_mfma_f32_16x16x32_bf16 v[220:223], a[236:239], v[24:27], v[220:223]\n"
    "v_exp_f32 v109, v109\n"
    "v_cvt_pk_bf16_f32 v91, v94, v95\n"
    "v_mfma_f32_16x16x32_bf16 a[140:143], a[232:235], v[32:35], 0\n"
    "v_exp_f32 v110, v110\n"
    "v_mfma_f32_16x16x32_bf16 a[140:143], a[236:239], v[40:43], a[140:143]\n"
    "ds_read_b128 a[228:231], v225 offset:10240\n"
    "v_cvt_pk_bf16_f32 v106, v108, v109\n"
    "v_mfma_f32_16x16x32_bf16 a[188:191], a[232:235], v[48:51], 0\n"
    "v_exp_f32 v111, v111\n"
    "v_mfma_f32_16x16x32_bf16 a[188:191], a[236:239], v[56:59], a[188:191]\n"
    "s_waitcnt lgkmcnt(4)\n"
    "v_cvt_pk_bf16_f32 v107, v110, v111\n"
    "s_branch LLOOP_%=\n"
    "LBD_%=:\n"
    "v_mfma_f32_16x16x32_bf16 v[128:131], a[192:195], v[64:67], v[128:131]\n"
    "s_sub_u32 m0, s58, 0xc000\n"
    "v_fma_f32 v8, v8, s40, v232\n"
    "v_mfma_f32_16x16x32_bf16 v[128:131], a[196:199], v[72:75], v[128:131]\n"
    "v_exp_f32 v8, v8\n"
    "v_fma_f32 v9, v9, s40, v232\n"
    "v_mfma_f32_16x16x32_bf16 v[176:179], a[192:195], v[80:83], v[176:179]\n"
    "buffer_load_dwordx4 v230, s[24:27], s37 offen offset:0 nt lds\n"
    "v_mfma_f32_16x16x32_bf16 v[176:179], a[196:199], v[88:91], v[176:179]\n"
    "ds_read_b64_tr_b16 a[216:217], v227 offset:25088\n"
    "v_exp_f32 v9, v9\n"
    "v_mfma_f32_16x16x32_bf16 a[96:99], a[192:195], v[96:99], a[96:99]\n"
    "v_fma_f32 v10, v10, s40, v232\n"
    "v_fma_f32 v11, v11, s40, v232\n"
    "v_mfma_f32_16x16x32_bf16 a[96:99], a[196:199], v[104:107], a[96:99]\n"
    "v_exp_f32 v10, v10\n"
    "v_fma_f32 v24, v24, s40, v233\n"
    "v_mfma_f32_16x16x32_bf16 a[144:147], a[192:195], v[112:115], a[144:147]\n"
    "ds_read_b64_tr_b16 a[218:219], v227 offset:31232\n"
    "v_fma_f32 v25, v25, s40, v233\n"
    "v_mfma_f32_16x16x32_bf16 a[144:147], a[196:199], v[120:123], a[144:147]\n"
    "s_waitcnt vmcnt(13)\n"
    "v_exp_f32 v11, v11\n"
    "v_mfma_f32_16x16x32_bf16 a[240:243], v[240:243], v[64:67], a[240:243]\n"
    "s_barrier\n"
    "v_fma_f32 v26, v26, s40, v233\n"
    "v_mfma_f32_16x16x32_bf16 a[240:243], v[240:243], v[72:75], a[240:243]\n"
    "buffer_load_dwordx4 v230, s[24:27], s37 offen offset:3072 nt lds\n"
    "v_fma_f32 v27, v27, s40, v233\n"
    "v_mfma_f32_16x16x32_bf16 v[132:135], a[200:203], v[64:67], v[132:135]\n"
    "ds_read_b64_tr_b16 a[220:221], v227 offset:37376\n"
    "v_fma_f32 v40, v40, s40, v234\n"
    "v_mfma_f32_16x16x32_bf16 v[132:135], a[204:207], v[72:75], v[132:135]\n"
    "v_exp_f32 v24, v24\n"
    "v_fma_f32 v41, v41, s40, v234\n"
    "v_mfma_f32_16x16x32_bf16 v[180:183], a[200:203], v[80:83], v[180:183]\n"
    "s_sub_u32 m0, s59, 0xc000\n"
    "v_cvt_pk_bf16_f32 v8, v8, v9\n"
    "v_mfma_f32_16x16x32_bf16 v[180:183], a[204:207], v[88:91], v[180:183]\n"
    "ds_read_b128 v[244:247], v249 offset:2048\n"
    "v_exp_f32 v25, v25\n"
    "v_mfma_f32_16x16x32_bf16 a[100:103], a[200:203], v[96:99], a[100:103]\n"
    "buffer_load_dwordx4 v230, s[24:27], s37 offen offset:128 nt lds\n"
    "v_mfma_f32_16x16x32_bf16 a[100:103], a[204:207], v[104:107], a[100:103]\n"
    "ds_read_b64_tr_b16 a[222:223], v227 offset:43520\n"
    "v_exp_f32 v26, v26\n"
    "v_mfma_f32_16x16x32_bf16 a[148:151], a[200:203], v[112:115], a[148:151]\n"
    "ds_read_b128 a[224:227], v224 offset:24576\n"
    "v_fma_f32 v42, v42, s40, v234\n"
    "v_mfma_f32_16x16x32_bf16 a[148:151], a[204:207], v[120:123], a[148:151]\n"
    "ds_read_b128 a[228:231], v225 offset:24576\n"
    "v_exp_f32 v27, v27\n"
    "v_mfma_f32_16x16x32_bf16 a[244:247], v[240:243], v[80:83], a[244:247]\n"
    "ds_read_b128 a[232:235], v224 offset:32768\n"
    "v_fma_f32 v43, v43, s40, v234\n"
    "v_mfma_f32_16x16x32_bf16 a[244:247], v[240:243], v[88:91], a[244:247]\n"
    "buffer_load_dwordx4 v230, s[24:27], s37 offen offset:3200 nt lds\n"
    "v_cvt_pk_bf16_f32 v9, v10, v11\n"
    "v_mfma_f32_16x16x32_bf16 v[136:139], a[208:211], v[64:67], v[136:139]\n"
    "ds_read_b128 a[236:239], v225 offset:32768\n"
    "v_fma_f32 v56, v56, s40, v235\n"
    "v_mfma_f32_16x16x32_bf16 v[136:139], a[212:215], v[72:75], v[136:139]\n"
    "v_exp_f32 v40, v40\n"
    "v_fma_f32 v57, v57, s40, v235\n"
    "v_mfma_f32_16x16x32_bf16 v[184:187], a[208:211], v[80:83], v[184:187]\n"
    "s_sub_u32 m0, s60, 0xc000\n"
    "v_cvt_pk_bf16_f32 v24, v24, v25\n"
    "v_mfma_f32_16x16x32_bf16 v[184:187], a[212:215], v[88:91], v[184:187]\n"
    "ds_read_b128 a[192:195], v224 offset:40960\n"
    "v_exp_f32 v41, v41\n"
    "v_mfma_f32_16x16x32_bf16 a[104:107], a[208:211], v[96:99], a[104:107]\n"
    "buffer_load_dwordx4 v230, s[24:27], s37 offen offset:256 nt lds\n"
    "v_mfma_f32_16x16x32_bf16 a[104:107], a[212:215], v[104:107], a[104:107]\n"
    "ds_read_b128 a[196:199], v225 offset:40960\n"
    "v_exp_f32 v42, v42\n"
    "v_mfma_f32_16x16x32_bf16 a[152:155], a[208:211], v[112:115], a[152:155]\n"
    "v_exp_f32 v43, v43\n"
    "v_mfma_f32_16x16x32_bf16 a[152:155], a[212:215], v[120:123], a[152:155]\n"
    "v_exp_f32 v56, v56\n"
    "v_fma_f32 v58, v58, s40, v235\n"
    "v_mfma_f32_16x16x32_bf16 a[248:251], v[240:243], v[96:99], a[248:251]\n"
    "ds_read_b128 a[200:203], v224 offset:26624\n"
    "v_fma_f32 v59, v59, s40, v235\n"
    "v_mfma_f32_16x16x32_bf16 a[248:251], v[240:243], v[104:107], a[248:251]\n"
    "s_waitcnt lgkmcnt(7)\n"
    "v_exp_f32 v57, v57\n"
    "v_mfma_f32_16x16x32_bf16 v[140:143], a[216:219], v[64:67], v[140:143]\n"
    "buffer_load_dwordx4 v230, s[24:27], s37 offen offset:3328 nt lds\n"
    "v_mfma_f32_16x16x32_bf16 v[140:143], a[220:223], v[72:75], v[140:143]\n"
    "ds_read_b128 a[204:207], v225 offset:26624\n"
    "v_exp_f32 v58, v58\n"
    "v_mfma_f32_16x16x32_bf16 v[188:191], a[216:219], v[80:83], v[188:191]\n"
    "buffer_store_dwordx4 v[244:247], v250, s[84:87], s42 offen offset:256 nt sc1\n"
    "v_mfma_f32_16x16x32_bf16 v[188:191], a[220:223], v[88:91], v[188:191]\n"
    "v_exp_f32 v59, v59\n"
    "v_cvt_pk_bf16_f32 v25, v26, v27\n"
    "v_mfma_f32_16x16x32_bf16 a[108:111], a[216:219], v[96:99], a[108:111]\n"
    "ds_read_b128 a[208:211], v224 offset:34816\n"
    "v_fma_f32 v12, v12, s40, v232\n"
    "v_mfma_f32_16x16x32_bf16 a[108:111], a[220:223], v[104:107], a[108:111]\n"
    "v_exp_f32 v12, v12\n"
    "v_fma_f32 v13, v13, s40, v232\n"
    "v_mfma_f32_16x16x32_bf16 a[156:159], a[216:219], v[112:115], a[156:159]\n"
    "v_exp_f32 v13, v13\n"
    "v_mfma_f32_16x16x32_bf16 a[156:159], a[220:223], v[120:123], a[156:159]\n"
    "v_cvt_pk_bf16_f32 v40, v40, v41\n"
    "v_fma_f32 v14, v14, s40, v232\n"
    "v_fma_f32 v15, v15, s40, v232\n"
    "v_mfma_f32_16x16x32_bf16 a[252:255], v[240:243], v[112:115], a[252:255]\n"
    "ds_read_b128 a[212:215], v225 offset:34816\n"
    "v_cvt_pk_bf16_f32 v41, v42, v43\n"
    "v_mfma_f32_16x16x32_bf16 a[252:255], v[240:243], v[120:123], a[252:255]\n"
    "s_waitcnt lgkmcnt(4)\n"
    "v_exp_f32 v14, v14\n"
    "v_mfma_f32_16x16x32_bf16 v[100:103], a[224:227], a[48:51], 0\n"
    "v_subrev_u32 v253, 0xc000, v226\n"
    "v_fma_f32 v28, v28, s40, v233\n"
    "v_mfma_f32_16x16x32_bf16 v[100:103], a[228:231], a[52:55], v[100:103]\n"
    "v_exp_f32 v15, v15\n"
    "v_fma_f32 v29, v29, s40, v233\n"
    "v_mfma_f32_16x16x32_bf16 v[100:103], a[232:235], a[56:59], v[100:103]\n"
    "ds_read_b128 a[216:219], v224 offset:43008\n"
    "v_cvt_pk_bf16_f32 v56, v56, v57\n"
    "v_mfma_f32_16x16x32_bf16 v[100:103], a[236:239], a[60:63], v[100:103]\n"
    "v_exp_f32 v28, v28\n"
    "v_fma_f32 v30, v30, s40, v233\n"
    "v_mfma_f32_16x16x32_bf16 v[100:103], a[192:195], a[64:67], v[100:103]\n"
    "v_subrev_u32 v254, 0xc000, v227\n"
    "v_fma_f32 v31, v31, s40, v233\n"
    "v_mfma_f32_16x16x32_bf16 v[100:103], a[196:199], a[68:71], v[100:103]\n"
    "v_exp_f32 v29, v29\n"
    "v_cvt_pk_bf16_f32 v57, v58, v59\n"
    "v_mfma_f32_16x16x32_bf16 v[116:119], a[224:227], a[72:75], 0\n"
    "v_exp_f32 v30, v30\n"
    "v_mfma_f32_16x16x32_bf16 v[116:119], a[228:231], a[76:79], v[116:119]\n"
    "ds_read_b128 a[220:223], v225 offset:43008\n"
    "v_exp_f32 v31, v31\n"
    "v_mfma_f32_16x16x32_bf16 v[116:119], a[232:235], a[80:83], v[116:119]\n"
    "ds_read_b128 v[244:247], v249 offset:6144\n"
    "v_fma_f32 v44, v44, s40, v234\n"
    "v_mfma_f32_16x16x32_bf16 v[116:119], a[236:239], a[84:87], v[116:119]\n"
    "v_exp_f32 v44, v44\n"
    "v_fma_f32 v45, v45, s40, v234\n"
    "v_mfma_f32_16x16x32_bf16 v[116:119], a[192:195], a[88:91], v[116:119]\n"
    "v_exp_f32 v45, v45\n"
    "v_mfma_f32_16x16x32_bf16 v[116:119], a[196:199], a[92:95], v[116:119]\n"
    "v_cvt_pk_bf16_f32 v10, v12, v13\n"
    "v_fma_f32 v46, v46, s40, v234\n"
    "v_fma_f32 v47, v47, s40, v234\n"
    "v_mfma_f32_16x16x32_bf16 v[68:71], a[224:227], a[0:3], 0\n"
    "v_exp_f32 v46, v46\n"
    "v_mfma_f32_16x16x32_bf16 v[68:71], a[228:231], a[4:7], v[68:71]\n"
    "v_exp_f32 v47, v47\n"
    "v_cvt_pk_bf16_f32 v11, v14, v15\n"
    "v_mfma_f32_16x16x32_bf16 v[68:71], a[232:235], a[8:11], v[68:71]\n"
    "v_fma_f32 v60, v60, s40, v235\n"
    "v_fma_f32 v61, v61, s40, v235\n"
    "v_mfma_f32_16x16x32_bf16 v[68:71], a[236:239], a[12:15], v[68:71]\n"
    "v_exp_f32 v60, v60\n"
    "v_cvt_pk_bf16_f32 v26, v28, v29\n"
    "v_mfma_f32_16x16x32_bf16 v[68:71], a[192:195], a[16:19], v[68:71]\n"
    "v_exp_f32 v61, v61\n"
    "v_mfma_f32_16x16x32_bf16 v[68:71], a[196:199], a[20:23], v[68:71]\n"
    "v_fma_f32 v62, v62, s40, v235\n"
    "v_exp_f32 v62, v62\n"
    "v_mfma_f32_16x16x32_bf16 v[84:87], a[224:227], a[24:27], 0\n"
    "v_fma_f32 v63, v63, s40, v235\n"
    "v_cvt_pk_bf16_f32 v27, v30, v31\n"
    "v_mfma_f32_16x16x32_bf16 v[84:87], a[228:231], a[28:31], v[84:87]\n"
    "ds_read_b128 a[224:227], v224 offset:28672\n"
    "v_exp_f32 v63, v63\n"
    "v_mfma_f32_16x16x32_bf16 v[84:87], a[232:235], a[32:35], v[84:87]\n"
    "v_cvt_pk_bf16_f32 v42, v44, v45\n"
    "v_cvt_pk_bf16_f32 v43, v46, v47\n"
    "v_mfma_f32_16x16x32_bf16 v[84:87], a[236:239], a[36:39], v[84:87]\n"
    "ds_read_b128 a[228:231], v225 offset:28672\n"
    "v_cvt_pk_bf16_f32 v58, v60, v61\n"
    "v_cvt_pk_bf16_f32 v59, v62, v63\n"
    "v_mfma_f32_16x16x32_bf16 v[84:87], a[192:195], a[40:43], v[84:87]\n"
    "v_fma_f32 v100, v100, s40, v234\n"
    "v_fma_f32 v101, v101, s40, v234\n"
    "v_mfma_f32_16x16x32_bf16 v[84:87], a[196:199], a[44:47], v[84:87]\n"
    "s_waitcnt lgkmcnt(2)\n"
    "v_exp_f32 v100, v100\n"
    "v_mfma_f32_16x16x32_bf16 v[108:111], a[200:203], a[48:51], 0\n"
    "ds_read_b128 a[232:235], v224 offset:36864\n"
    "v_fma_f32 v102, v102, s40, v234\n"
    "v_mfma_f32_16x16x32_bf16 v[108:111], a[204:207], a[52:55], v[108:111]\n"
    "buffer_store_dwordx4 v[244:247], v250, s[84:87], s44 offen offset:256 nt sc1\n"
    "v_fma_f32 v103, v103, s40, v234\n"
    "v_mfma_f32_16x16x32_bf16 v[108:111], a[208:211], a[56:59], v[108:111]\n"
    "ds_read_b128 a[236:239], v225 offset:36864\n"
    "v_fma_f32 v116, v116, s40, v235\n"
    "v_mfma_f32_16x16x32_bf16 v[108:111], a[212:215], a[60:63], v[108:111]\n"
    "v_exp_f32 v101, v101\n"
    "v_fma_f32 v117, v117, s40, v235\n"
    "v_mfma_f32_16x16x32_bf16 v[108:111], a[216:219], a[64:67], v[108:111]\n"
    "v_exp_f32 v102, v102\n"
    "v_mfma_f32_16x16x32_bf16 v[108:111], a[220:223], a[68:71], v[108:111]\n"
    "ds_read_b128 a[192:195], v224 offset:45056\n"
    "v_exp_f32 v103, v103\n"
    "v_mfma_f32_16x16x32_bf16 v[124:127], a[200:203], a[72:75], 0\n"
    "v_exp_f32 v116, v116\n"
    "v_mfma_f32_16x16x32_bf16 v[124:127], a[204:207], a[76:79], v[124:127]\n"
    "ds_read_b128 v[244:247], v249 offset:11264\n"
    "v_exp_f32 v117, v117\n"
    "v_mfma_f32_16x16x32_bf16 v[124:127], a[208:211], a[80:83], v[124:127]\n"
    "ds_read_b128 a[196:199], v225 offset:45056\n"
    "v_fma_f32 v118, v118, s40, v235\n"
    "v_mfma_f32_16x16x32_bf16 v[124:127], a[212:215], a[84:87], v[124:127]\n"
    "v_exp_f32 v118, v118\n"
    "v_fma_f32 v119, v119, s40, v235\n"
    "v_mfma_f32_16x16x32_bf16 v[124:127], a[216:219], a[88:91], v[124:127]\n"
    "v_exp_f32 v119, v119\n"
    "v_mfma_f32_16x16x32_bf16 v[124:127], a[220:223], a[92:95], v[124:127]\n"
    "v_fma_f32 v68, v68, s40, v232\n"
    "v_exp_f32 v68, v68\n"
    "v_mfma_f32_16x16x32_bf16 v[76:79], a[200:203], a[0:3], 0\n"
    "v_fma_f32 v69, v69, s40, v232\n"
    "v_cvt_pk_bf16_f32 v100, v100, v101\n"
    "v_mfma_f32_16x16x32_bf16 v[76:79], a[204:207], a[4:7], v[76:79]\n"
    "v_exp_f32 v69, v69\n"
    "v_fma_f32 v70, v70, s40, v232\n"
    "v_mfma_f32_16x16x32_bf16 v[76:79], a[208:211], a[8:11], v[76:79]\n"
    "v_exp_f32 v70, v70\n"
    "v_mfma_f32_16x16x32_bf16 v[76:79], a[212:215], a[12:15], v[76:79]\n"
    "v_fma_f32 v71, v71, s40, v232\n"
    "v_exp_f32 v71, v71\n"
    "v_mfma_f32_16x16x32_bf16 v[76:79], a[216:219], a[16:19], v[76:79]\n"
    "v_cvt_pk_bf16_f32 v101, v102, v103\n"
    "v_fma_f32 v84, v84, s40, v233\n"
    "v_mfma_f32_16x16x32_bf16 v[76:79], a[220:223], a[20:23], v[76:79]\n"
    "v_exp_f32 v84, v84\n"
    "v_fma_f32 v85, v85, s40, v233\n"
    "v_mfma_f32_16x16x32_bf16 v[92:95], a[200:203], a[24:27], 0\n"
    "v_exp_f32 v85, v85\n"
    "v_mfma_f32_16x16x32_bf16 v[92:95], a[204:207], a[28:31], v[92:95]\n"
    "ds_read_b128 a[200:203], v224 offset:30720\n"
    "v_cvt_pk_bf16_f32 v116, v116, v117\n"
    "v_fma_f32 v86, v86, s40, v233\n"
    "v_mfma_f32_16x16x32_bf16 v[92:95], a[208:211], a[32:35], v[92:95]\n"
    "v_exp_f32 v86, v86\n"
    "v_mfma_f32_16x16x32_bf16 v[92:95], a[212:215], a[36:39], v[92:95]\n"
    "ds_read_b128 a[204:207], v225 offset:30720\n"
    "v_fma_f32 v87, v87, s40, v233\n"
    "v_cvt_pk_bf16_f32 v117, v118, v119\n"
    "v_mfma_f32_16x16x32_bf16 v[92:95], a[216:219], a[40:43], v[92:95]\n"
    "v_exp_f32 v87, v87\n"
    "v_mfma_f32_16x16x32_bf16 v[92:95], a[220:223], a[44:47], v[92:95]\n"
    "s_waitcnt lgkmcnt(2)\n"
    "v_fma_f32 v108, v108, s40, v234\n"
    "v_fma_f32 v109, v109, s40, v234\n"
    "v_mfma_f32_16x16x32_bf16 v[36:39], a[224:227], a[48:51], 0\n"
    "ds_read_b128 a[208:211], v224 offset:38912\n"
    "v_cvt_pk_bf16_f32 v68, v68, v69\n"
    "v_mfma_f32_16x16x32_bf16 v[36:39], a[228:231], a[52:55], v[36:39]\n"
    "buffer_store_dwordx4 v[244:247], v250, s[84:87], s42 offen offset:3200 nt sc1\n"
    "v_fma_f32 v110, v110, s40, v234\n"
    "v_mfma_f32_16x16x32_bf16 v[36:39], a[232:235], a[56:59], v[36:39]\n"
    "ds_read_b128 a[212:215], v225 offset:38912\n"
    "v_fma_f32 v111, v111, s40, v234\n"
    "v_mfma_f32_16x16x32_bf16 v[36:39], a[236:239], a[60:63], v[36:39]\n"
    "v_exp_f32 v108, v108\n"
    "v_cvt_pk_bf16_f32 v69, v70, v71\n"
    "v_mfma_f32_16x16x32_bf16 v[36:39], a[192:195], a[64:67], v[36:39]\n"
    "v_exp_f32 v109, v109\n"
    "v_mfma_f32_16x16x32_bf16 v[36:39], a[196:199], a[68:71], v[36:39]\n"
    "ds_read_b128 a[216:219], v224 offset:47104\n"
    "v_exp_f32 v110, v110\n"
    "v_mfma_f32_16x16x32_bf16 v[52:55], a[224:227], a[72:75], 0\n"
    "v_exp_f32 v111, v111\n"
    "v_mfma_f32_16x16x32_bf16 v[52:55], a[228:231], a[76:79], v[52:55]\n"
    "ds_read_b128 v[244:247], v249 offset:15360\n"
    "v_fma_f32 v124, v124, s40, v235\n"
    "v_fma_f32 v125, v125, s40, v235\n"
    "v_mfma_f32_16x16x32_bf16 v[52:55], a[232:235], a[80:83], v[52:55]\n"
    "ds_read_b128 a[220:223], v225 offset:47104\n"
    "v_cvt_pk_bf16_f32 v84, v84, v85\n"
    "v_mfma_f32_16x16x32_bf16 v[52:55], a[236:239], a[84:87], v[52:55]\n"
    "v_exp_f32 v124, v124\n"
    "v_fma_f32 v126, v126, s40, v235\n"
    "v_mfma_f32_16x16x32_bf16 v[52:55], a[192:195], a[88:91], v[52:55]\n"
    "v_exp_f32 v125, v125\n"
    "v_mfma_f32_16x16x32_bf16 v[52:55], a[196:199], a[92:95], v[52:55]\n"
    "v_exp_f32 v126, v126\n"
    "v_fma_f32 v127, v127, s40, v235\n"
    "v_mfma_f32_16x16x32_bf16 v[4:7], a[224:227], a[0:3], 0\n"
    "v_exp_f32 v127, v127\n"
    "v_mfma_f32_16x16x32_bf16 v[4:7], a[228:231], a[4:7], v[4:7]\n"
    "v_cvt_pk_bf16_f32 v85, v86, v87\n"
    "v_fma_f32 v76, v76, s40, v232\n"
    "v_fma_f32 v77, v77, s40, v232\n"
    "v_mfma_f32_16x16x32_bf16 v[4:7], a[232:235], a[8:11], v[4:7]\n"
    "v_exp_f32 v76, v76\n"
    "v_mfma_f32_16x16x32_bf16 v[4:7], a[236:239], a[12:15], v[4:7]\n"
    "v_exp_f32 v77, v77\n"
    "v_cvt_pk_bf16_f32 v102, v108, v109\n"
    "v_mfma_f32_16x16x32_bf16 v[4:7], a[192:195], a[16:19], v[4:7]\n"
    "v_fma_f32 v78, v78, s40, v232\n"
    "v_fma_f32 v79, v79, s40, v232\n"
    "v_mfma_f32_16x16x32_bf16 v[4:7], a[196:199], a[20:23], v[4:7]\n"
    "v_exp_f32 v78, v78\n"
    "v_cvt_pk_bf16_f32 v103, v110, v111\n"
    "v_mfma_f32_16x16x32_bf16 v[20:23], a[224:227], a[24:27], 0\n"
    "v_exp_f32 v79, v79\n"
    "v_mfma_f32_16x16x32_bf16 v[20:23], a[228:231], a[28:31], v[20:23]\n"
    "s_waitcnt vmcnt(12)\n"
    "v_fma_f32 v92, v92, s40, v233\n"
    "v_fma_f32 v93, v93, s40, v233\n"
    "v_mfma_f32_16x16x32_bf16 v[20:23], a[232:235], a[32:35], v[20:23]\n"
    "s_barrier\n"
    "v_cvt_pk_bf16_f32 v118, v124, v125\n"
    "v_mfma_f32_16x16x32_bf16 v[20:23], a[236:239], a[36:39], v[20:23]\n"
    "ds_read_b64_tr_b16 a[224:225], v226 offset:0\n"
    "v_exp_f32 v92, v92\n"
    "v_mfma_f32_16x16x32_bf16 v[20:23], a[192:195], a[40:43], v[20:23]\n"
    "ds_read_b64_tr_b16 a[226:227], v226 offset:6144\n"
    "v_fma_f32 v94, v94, s40, v233\n"
    "v_mfma_f32_16x16x32_bf16 v[20:23], a[196:199], a[44:47], v[20:23]\n"
    "s_waitcnt lgkmcnt(2)\n"
    "v_exp_f32 v93, v93\n"
    "v_mfma_f32_16x16x32_bf16 v[44:47], a[200:203], a[48:51], 0\n"
    "ds_read_b64_tr_b16 a[228:229], v226 offset:12288\n"
    "buffer_store_dwordx4 v[244:247], v250, s[84:87], s44 offen offset:3200 nt sc1\n"
    "v_mfma_f32_16x16x32_bf16 v[44:47], a[204:207], a[52:55], v[44:47]\n"
    "ds_read_b64_tr_b16 a[230:231], v226 offset:18432\n"
    "v_exp_f32 v94, v94\n"
    "v_mfma_f32_16x16x32_bf16 v[44:47], a[208:211], a[56:59], v[44:47]\n"
    "ds_read_b64_tr_b16 a[232:233], v227 offset:0\n"
    "v_fma_f32 v95, v95, s40, v233\n"
    "v_mfma_f32_16x16x32_bf16 v[44:47], a[212:215], a[60:63], v[44:47]\n"
    "v_exp_f32 v95, v95\n"
    "v_cvt_pk_bf16_f32 v119, v126, v127\n"
    "v_mfma_f32_16x16x32_bf16 v[44:47], a[216:219], a[64:67], v[44:47]\n"
    "ds_read_b64_tr_b16 a[234:235], v227 offset:6144\n"
    "v_fma_f32 v36, v36, s40, v234\n"
    "v_mfma_f32_16x16x32_bf16 v[44:47], a[220:223], a[68:71], v[44:47]\n"
    "v_exp_f32 v36, v36\n"
    "v_fma_f32 v37, v37, s40, v234\n"
    "v_mfma_f32_16x16x32_bf16 v[60:63], a[200:203], a[72:75], 0\n"
    "ds_read_b64_tr_b16 a[236:237], v227 offset:12288\n"
    "v_cvt_pk_bf16_f32 v70, v76, v77\n"
    "v_mfma_f32_16x16x32_bf16 v[60:63], a[204:207], a[76:79], v[60:63]\n"
    "ds_read_b128 v[244:247], v249 offset:3072\n"
    "v_exp_f32 v37, v37\n"
    "v_mfma_f32_16x16x32_bf16 v[60:63], a[208:211], a[80:83], v[60:63]\n"
    "v_fma_f32 v38, v38, s40, v234\n"
    "v_fma_f32 v39, v39, s40, v234\n"
    "v_mfma_f32_16x16x32_bf16 v[60:63], a[212:215], a[84:87], v[60:63]\n"
    "ds_read_b64_tr_b16 a[238:239], v227 offset:18432\n"
    "v_exp_f32 v38, v38\n"
    "v_mfma_f32_16x16x32_bf16 v[60:63], a[216:219], a[88:91], v[60:63]\n"
    "ds_read_b64_tr_b16 a[192:193], v226 offset:512\n"
    "v_cvt_pk_bf16_f32 v71, v78, v79\n"
    "v_mfma_f32_16x16x32_bf16 v[60:63], a[220:223], a[92:95], v[60:63]\n"
    "v_exp_f32 v39, v39\n"
    "v_fma_f32 v52, v52, s40, v235\n"
    "v_mfma_f32_16x16x32_bf16 v[12:15], a[200:203], a[0:3], 0\n"
    "ds_read_b64_tr_b16 a[194:195], v226 offset:6656\n"
    "v_fma_f32 v53, v53, s40, v235\n"
    "v_mfma_f32_16x16x32_bf16 v[12:15], a[204:207], a[4:7], v[12:15]\n"
    "v_exp_f32 v52, v52\n"
    "v_cvt_pk_bf16_f32 v86, v92, v93\n"
    "v_mfma_f32_16x16x32_bf16 v[12:15], a[208:211], a[8:11], v[12:15]\n"
    "ds_read_b64_tr_b16 a[196:197], v226 offset:12800\n"
    "v_fma_f32 v54, v54, s40, v235\n"
    "v_mfma_f32_16x16x32_bf16 v[12:15], a[212:215], a[12:15], v[12:15]\n"
    "v_exp_f32 v53, v53\n"
    "v_fma_f32 v55, v55, s40, v235\n"
    "v_mfma_f32_16x16x32_bf16 v[12:15], a[216:219], a[16:19], v[12:15]\n"
    "ds_read_b64_tr_b16 a[198:199], v226 offset:18944\n"
    "v_cvt_pk_bf16_f32 v87, v94, v95\n"
    "v_mfma_f32_16x16x32_bf16 v[12:15], a[220:223], a[20:23], v[12:15]\n"
    "v_exp_f32 v54, v54\n"
    "v_fma_f32 v4, v4, s40, v232\n"
    "v_mfma_f32_16x16x32_bf16 v[28:31], a[200:203], a[24:27], 0\n"
    "ds_read_b64_tr_b16 a[48:49], v227 offset:512\n"
    "v_fma_f32 v5, v5, s40, v232\n"
    "v_mfma_f32_16x16x32_bf16 v[28:31], a[204:207], a[28:31], v[28:31]\n"
    "v_exp_f32 v55, v55\n"
    "v_cvt_pk_bf16_f32 v36, v36, v37\n"
    "v_mfma_f32_16x16x32_bf16 v[28:31], a[208:211], a[32:35], v[28:31]\n"
    "v_exp_f32 v4, v4\n"
    "v_mfma_f32_16x16x32_bf16 v[28:31], a[212:215], a[36:39], v[28:31]\n"
    "ds_read_b64_tr_b16 a[50:51], v227 offset:6656\n"
    "v_exp_f32 v5, v5\n"
    "v_mfma_f32_16x16x32_bf16 v[28:31], a[216:219], a[40:43], v[28:31]\n"
    "v_fma_f32 v6, v6, s40, v232\n"
    "v_fma_f32 v7, v7, s40, v232\n"
    "v_mfma_f32_16x16x32_bf16 v[28:31], a[220:223], a[44:47], v[28:31]\n"
    "s_waitcnt lgkmcnt(6)\n"
    "v_exp_f32 v6, v6\n"
    "v_mfma_f32_16x16x32_bf16 v[128:131], a[224:227], v[0:3], v[128:131]\n"
    "ds_read_b64_tr_b16 a[52:53], v227 offset:12800\n"
    "v_cvt_pk_bf16_f32 v37, v38, v39\n"
    "v_mfma_f32_16x16x32_bf16 v[128:131], a[228:231], v[8:11], v[128:131]\n"
    "buffer_store_dwordx4 v[244:247], v250, s[84:87], s42 offen offset:3328 nt sc1\n"
    "v_fma_f32 v44, v44, s40, v234\n"
    "v_mfma_f32_16x16x32_bf16 v[176:179], a[224:227], v[16:19], v[176:179]\n"
    "v_exp_f32 v7, v7\n"
    "v_mfma_f32_16x16x32_bf16 v[176:179], a[228:231], v[24:27], v[176:179]\n"
    "ds_read_b128 v[244:247], v249 offset:7168\n"
    "v_exp_f32 v44, v44\n"
    "v_mfma_f32_16x16x32_bf16 a[96:99], a[224:227], v[32:35], a[96:99]\n"
    "ds_read_b64_tr_b16 a[54:55], v227 offset:18944\n"
    "v_fma_f32 v45, v45, s40, v234\n"
    "v_mfma_f32_16x16x32_bf16 a[96:99], a[228:231], v[40:43], a[96:99]\n"
    "v_exp_f32 v45, v45\n"
    "v_cvt_pk_bf16_f32 v52, v52, v53\n"
    "v_mfma_f32_16x16x32_bf16 a[144:147], a[224:227], v[48:51], a[144:147]\n"
    "v_fma_f32 v46, v46, s40, v234\n"
    "v_fma_f32 v47, v47, s40, v234\n"
    "v_mfma_f32_16x16x32_bf16 a[144:147], a[228:231], v[56:59], a[144:147]\n"
    "v_exp_f32 v46, v46\n"
    "v_cvt_pk_bf16_f32 v53, v54, v55\n"
    "v_mfma_f32_16x16x32_bf16 a[240:243], v[240:243], v[0:3], a[240:243]\n"
    "v_exp_f32 v47, v47\n"
    "v_mfma_f32_16x16x32_bf16 a[240:243], v[240:243], v[8:11], a[240:243]\n"
    "v_fma_f32 v20, v20, s40, v233\n"
    "v_exp_f32 v20, v20\n"
    "v_mfma_f32_16x16x32_bf16 v[132:135], a[232:235], v[0:3], v[132:135]\n"
    "v_fma_f32 v21, v21, s40, v233\n"
    "v_cvt_pk_bf16_f32 v4, v4, v5\n"
    "v_mfma_f32_16x16x32_bf16 v[132:135], a[236:239], v[8:11], v[132:135]\n"
    "v_exp_f32 v21, v21\n"
    "v_fma_f32 v22, v22, s40, v233\n"
    "v_mfma_f32_16x16x32_bf16 v[180:183], a[232:235], v[16:19], v[180:183]\n"
    "v_exp_f32 v22, v22\n"
    "v_mfma_f32_16x16x32_bf16 v[180:183], a[236:239], v[24:27], v[180:183]\n"
    "s_waitcnt vmcnt(5)\n"
    "v_fma_f32 v23, v23, s40, v233\n"
    "v_cvt_pk_bf16_f32 v5, v6, v7\n"
    "v_mfma_f32_16x16x32_bf16 a[100:103], a[232:235], v[32:35], a[100:103]\n"
    "s_barrier\n"
    "v_fma_f32 v60, v60, s40, v235\n"
    "v_mfma_f32_16x16x32_bf16 a[100:103], a[236:239], v[40:43], a[100:103]\n"
    "ds_read_b64_tr_b16 a[56:57], v253 offset:0\n"
    "v_exp_f32 v23, v23\n"
    "v_mfma_f32_16x16x32_bf16 a[148:151], a[232:235], v[48:51], a[148:151]\n"
    "ds_read_b64_tr_b16 a[58:59], v253 offset:6144\n"
    "v_fma_f32 v61, v61, s40, v235\n"
    "v_mfma_f32_16x16x32_bf16 a[148:151], a[236:239], v[56:59], a[148:151]\n"
    "ds_read_b64_tr_b16 a[60:61], v253 offset:12288\n"
    "v_exp_f32 v60, v60\n"
    "v_mfma_f32_16x16x32_bf16 a[244:247], v[240:243], v[16:19], a[244:247]\n"
    "ds_read_b64_tr_b16 a[62:63], v253 offset:18432\n"
    "v_cvt_pk_bf16_f32 v38, v44, v45\n"
    "v_mfma_f32_16x16x32_bf16 a[244:247], v[240:243], v[24:27], a[244:247]\n"
    "s_waitcnt lgkmcnt(4)\n"
    "v_exp_f32 v61, v61\n"
    "v_mfma_f32_16x16x32_bf16 v[136:139], a[192:195], v[0:3], v[136:139]\n"
    "ds_read_b64_tr_b16 a[64:65], v254 offset:0\n"
    "buffer_store_dwordx4 v[244:247], v250, s[84:87], s44 offen offset:3328 nt sc1\n"
    "v_mfma_f32_16x16x32_bf16 v[136:139], a[196:199], v[8:11], v[136:139]\n"
    "ds_read_b64_tr_b16 a[66:67], v254 offset:6144\n"
    "v_fma_f32 v62, v62, s40, v235\n"
    "v_fma_f32 v63, v63, s40, v235\n"
    "v_mfma_f32_16x16x32_bf16 v[184:187], a[192:195], v[16:19], v[184:187]\n"
    "ds_read_b64_tr_b16 a[68:69], v254 offset:12288\n"
    "v_cvt_pk_bf16_f32 v39, v46, v47\n"
    "v_mfma_f32_16x16x32_bf16 v[184:187], a[196:199], v[24:27], v[184:187]\n"
    "ds_read_b64_tr_b16 a[70:71], v254 offset:18432\n"
    "v_exp_f32 v62, v62\n"
    "v_mfma_f32_16x16x32_bf16 a[104:107], a[192:195], v[32:35], a[104:107]\n"
    "ds_read_b64_tr_b16 a[72:73], v253 offset:512\n"
    "v_fma_f32 v12, v12, s40, v232\n"
    "v_mfma_f32_16x16x32_bf16 a[104:107], a[196:199], v[40:43], a[104:107]\n"
    "ds_read_b64_tr_b16 a[74:75], v253 offset:6656\n"
    "v_exp_f32 v63, v63\n"
    "v_mfma_f32_16x16x32_bf16 a[152:155], a[192:195], v[48:51], a[152:155]\n"
    "ds_read_b64_tr_b16 a[76:77], v253 offset:12800\n"
    "v_fma_f32 v13, v13, s40, v232\n"
    "v_mfma_f32_16x16x32_bf16 a[152:155], a[196:199], v[56:59], a[152:155]\n"
    "ds_read_b64_tr_b16 a[78:79], v253 offset:18944\n"
    "v_exp_f32 v12, v12\n"
    "v_mfma_f32_16x16x32_bf16 a[248:251], v[240:243], v[32:35], a[248:251]\n"
    "ds_read_b64_tr_b16 a[80:81], v254 offset:512\n"
    "v_cvt_pk_bf16_f32 v20, v20, v21\n"
    "v_mfma_f32_16x16x32_bf16 a[248:251], v[240:243], v[40:43], a[248:251]\n"
    "ds_read_b64_tr_b16 a[82:83], v254 offset:6656\n"
    "v_exp_f32 v13, v13\n"
    "v_mfma_f32_16x16x32_bf16 v[140:143], a[48:51], v[0:3], v[140:143]\n"
    "ds_read_b64_tr_b16 a[84:85], v254 offset:12800\n"
    "v_fma_f32 v14, v14, s40, v232\n"
    "v_mfma_f32_16x16x32_bf16 v[140:143], a[52:55], v[8:11], v[140:143]\n"
    "ds_read_b64_tr_b16 a[86:87], v254 offset:18944\n"
    "v_exp_f32 v14, v14\n"
    "v_mfma_f32_16x16x32_bf16 v[188:191], a[48:51], v[16:19], v[188:191]\n"
    "ds_read_b64_tr_b16 a[88:89], v226 offset:25600\n"
    "v_fma_f32 v15, v15, s40, v232\n"
    "v_mfma_f32_16x16x32_bf16 v[188:191], a[52:55], v[24:27], v[188:191]\n"
    "ds_read_b64_tr_b16 a[90:91], v226 offset:31744\n"
    "v_exp_f32 v15, v15\n"
    "v_mfma_f32_16x16x32_bf16 a[108:111], a[48:51], v[32:35], a[108:111]\n"
    "ds_read_b64_tr_b16 a[92:93], v226 offset:37888\n"
    "v_cvt_pk_bf16_f32 v21, v22, v23\n"
    "v_mfma_f32_16x16x32_bf16 a[108:111], a[52:55], v[40:43], a[108:111]\n"
    "ds_read_b64_tr_b16 a[94:95], v226 offset:44032\n"
    "v_fma_f32 v28, v28, s40, v233\n"
    "v_fma_f32 v29, v29, s40, v233\n"
    "v_mfma_f32_16x16x32_bf16 a[156:159], a[48:51], v[48:51], a[156:159]\n"
    "ds_read_b64_tr_b16 a[0:1], v226 offset:1024\n"
    "v_cvt_pk_bf16_f32 v54, v60, v61\n"
    "v_mfma_f32_16x16x32_bf16 a[156:159], a[52:55], v[56:59], a[156:159]\n"
    "ds_read_b64_tr_b16 a[2:3], v226 offset:7168\n"
    "v_exp_f32 v28, v28\n"
    "v_mfma_f32_16x16x32_bf16 a[252:255], v[240:243], v[48:51], a[252:255]\n"
    "ds_read_b64_tr_b16 a[4:5], v226 offset:13312\n"
    "v_fma_f32 v30, v30, s40, v233\n"
    "v_mfma_f32_16x16x32_bf16 a[252:255], v[240:243], v[56:59], a[252:255]\n"
    "ds_read_b64_tr_b16 a[6:7], v226 offset:19456\n"
    "v_exp_f32 v29, v29\n"
    "v_mfma_f32_16x16x32_bf16 a[248:251], v[240:243], v[100:103], a[248:251]\n"
    "ds_read_b64_tr_b16 a[16:17], v227 offset:25600\n"
    "v_fma_f32 v31, v31, s40, v233\n"
    "v_xor_b32 v245, 32, v248\n"
    "v_xor_b32 v246, 64, v248\n"
    "v_mfma_f32_16x16x32_bf16 a[248:251], v[240:243], v[36:39], a[248:251]\n"
    "s_waitcnt lgkmcnt(9)\n"
    "v_exp_f32 v30, v30\n"
    "v_xor_b32 v247, 96, v248\n"
    "v_mfma_f32_16x16x32_bf16 a[96:99], a[56:59], v[100:103], a[96:99]\n"
    "ds_read_b64_tr_b16 a[18:19], v227 offset:31744\n"
    "v_cvt_pk_bf16_f32 v55, v62, v63\n"
    "v_mfma_f32_16x16x32_bf16 a[96:99], a[60:63], v[36:39], a[96:99]\n"
    "ds_read_b64_tr_b16 a[20:21], v227 offset:37888\n"
    "v_exp_f32 v31, v31\n"
    "v_accvgpr_read_b32 v238, a248\n"
    "v_mov_b32 v231, v238\n"
    "v_rcp_f32 v238, v238\n"
    "v_mfma_f32_16x16x32_bf16 a[100:103], a[64:67], v[100:103], a[100:103]\n"
    "ds_read_b64_tr_b16 a[22:23], v227 offset:44032\n"
    "v_cvt_pk_bf16_f32 v6, v12, v13\n"
    "v_accvgpr_read_b32 v108, a96\n"
    "v_accvgpr_read_b32 v109, a97\n"
    "v_accvgpr_read_b32 v110, a98\n"
    "v_accvgpr_read_b32 v111, a99\n"
    "v_mfma_f32_16x16x32_bf16 a[100:103], a[68:71], v[36:39], a[100:103]\n"
    "ds_read_b64_tr_b16 a[8:9], v253 offset:1024\n"
    "v_cvt_pk_bf16_f32 v7, v14, v15\n"
    "v_cvt_pk_bf16_f32 v22, v28, v29\n"
    "v_mul_f32 v108, v108, v238\n"
    "v_mul_f32 v109, v109, v238\n"
    "v_mfma_f32_16x16x32_bf16 a[104:107], a[72:75], v[100:103], a[104:107]\n"
    "ds_read_b64_tr_b16 a[10:11], v253 offset:7168\n"
    "v_cvt_pk_bf16_f32 v23, v30, v31\n"
    "v_mul_f32 v110, v110, v238\n"
    "v_mul_f32 v111, v111, v238\n"
    "v_cvt_pk_bf16_f32 v108, v108, v109\n"
    "v_mfma_f32_16x16x32_bf16 a[104:107], a[76:79], v[36:39], a[104:107]\n"
    "ds_read_b64_tr_b16 a[12:13], v253 offset:13312\n"
    "v_cvt_pk_bf16_f32 v109, v110, v111\n"
    "v_accvgpr_read_b32 v44, a100\n"
    "v_accvgpr_read_b32 v45, a101\n"
    "v_mfma_f32_16x16x32_bf16 a[108:111], a[80:83], v[100:103], a[108:111]\n"
    "ds_write_b64 v248, v[108:109] offset:12288\n"
    "v_accvgpr_read_b32 v46, a102\n"
    "v_accvgpr_read_b32 v47, a103\n"
    "v_mfma_f32_16x16x32_bf16 a[108:111], a[84:87], v[36:39], a[108:111]\n"
    "ds_read_b64_tr_b16 a[14:15], v253 offset:19456\n"
    "v_mul_f32 v44, v44, v238\n"
    "v_mul_f32 v45, v45, v238\n"
    "v_mul_f32 v46, v46, v238\n"
    "v_mfma_f32_16x16x32_bf16 a[252:255], v[240:243], v[116:119], a[252:255]\n"
    "ds_read_b64_tr_b16 a[24:25], v227 offset:1024\n"
    "v_mul_f32 v47, v47, v238\n"
    "v_cvt_pk_bf16_f32 v44, v44, v45\n"
    "v_cvt_pk_bf16_f32 v45, v46, v47\n"
    "v_accvgpr_read_b32 v124, a104\n"
    "v_mfma_f32_16x16x32_bf16 a[252:255], v[240:243], v[52:55], a[252:255]\n"
    "ds_read_b64_tr_b16 a[26:27], v227 offset:7168\n"
    "ds_write_b64 v245, v[44:45] offset:12288\n"
    "v_mfma_f32_16x16x32_bf16 a[144:147], a[56:59], v[116:119], a[144:147]\n"
    "ds_read_b64_tr_b16 a[200:201], v227 offset:13312\n"
    "v_accvgpr_read_b32 v125, a105\n"
    "v_accvgpr_read_b32 v126, a106\n"
    "v_accvgpr_read_b32 v127, a107\n"
    "v_mul_f32 v124, v124, v238\n"
    "v_mfma_f32_16x16x32_bf16 a[144:147], a[60:63], v[52:55], a[144:147]\n"
    "ds_read_b64_tr_b16 a[202:203], v227 offset:19456\n"
    "v_mul_f32 v125, v125, v238\n"
    "v_mul_f32 v126, v126, v238\n"
    "v_mul_f32 v127, v127, v238\n"
    "v_mfma_f32_16x16x32_bf16 a[148:151], a[64:67], v[116:119], a[148:151]\n"
    "ds_read_b64_tr_b16 a[32:33], v226 offset:26112\n"
    "v_cvt_pk_bf16_f32 v124, v124, v125\n"
    "v_cvt_pk_bf16_f32 v125, v126, v127\n"
    "v_accvgpr_read_b32 v60, a108\n"
    "v_accvgpr_read_b32 v61, a109\n"
    "v_mfma_f32_16x16x32_bf16 a[148:151], a[68:71], v[52:55], a[148:151]\n"
    "ds_read_b64_tr_b16 a[34:35], v226 offset:32256\n"
    "ds_write_b64 v246, v[124:125] offset:12288\n"
    "v_mfma_f32_16x16x32_bf16 a[152:155], a[72:75], v[116:119], a[152:155]\n"
    "ds_read_b64_tr_b16 a[208:209], v226 offset:38400\n"
    "v_accvgpr_read_b32 v62, a110\n"
    "v_accvgpr_read_b32 v63, a111\n"
    "v_mul_f32 v60, v60, v238\n"
    "v_mul_f32 v61, v61, v238\n"
    "v_mfma_f32_16x16x32_bf16 a[152:155], a[76:79], v[52:55], a[152:155]\n"
    "ds_read_b64_tr_b16 a[210:211], v226 offset:44544\n"
    "v_mul_f32 v62, v62, v238\n"
    "v_mul_f32 v63, v63, v238\n"
    "v_cvt_pk_bf16_f32 v60, v60, v61\n"
    "v_mfma_f32_16x16x32_bf16 a[156:159], a[80:83], v[116:119], a[156:159]\n"
    "ds_read_b64_tr_b16 a[36:37], v226 offset:1536\n"
    "v_cvt_pk_bf16_f32 v61, v62, v63\n"
    "v_accvgpr_read_b32 v239, a252\n"
    "v_add_f32 v231, v231, v239\n"
    "v_accvgpr_read_b32 v76, a144\n"
    "v_mfma_f32_16x16x32_bf16 a[156:159], a[84:87], v[52:55], a[156:159]\n"
    "ds_read_b64_tr_b16 a[38:39], v226 offset:7680\n"
    "ds_write_b64 v247, v[60:61] offset:12288\n"
    "v_mfma_f32_16x16x32_bf16 a[240:243], v[240:243], v[68:71], a[240:243]\n"
    "ds_read_b64_tr_b16 a[212:213], v226 offset:13824\n"
    "ds_read_b128 a[96:99], v249 offset:12288\n"
    "v_rcp_f32 v239, v239\n"
    "v_accvgpr_read_b32 v77, a145\n"
    "v_mfma_f32_16x16x32_bf16 a[240:243], v[240:243], v[4:7], a[240:243]\n"
    "ds_read_b64_tr_b16 a[214:215], v226 offset:19968\n"
    "ds_read_b128 a[100:103], v249 offset:13312\n"
    "v_accvgpr_read_b32 v78, a146\n"
    "v_accvgpr_read_b32 v79, a147\n"
    "v_mfma_f32_16x16x32_bf16 v[128:131], a[56:59], v[68:71], v[128:131]\n"
    "ds_read_b64_tr_b16 a[28:29], v254 offset:1024\n"
    "v_mul_f32 v76, v76, v239\n"
    "v_mul_f32 v77, v77, v239\n"
    "v_mul_f32 v78, v78, v239\n"
    "v_mul_f32 v79, v79, v239\n"
    "v_mfma_f32_16x16x32_bf16 v[128:131], a[60:63], v[4:7], v[128:131]\n"
    "ds_read_b64_tr_b16 a[30:31], v254 offset:7168\n"
    "v_cvt_pk_bf16_f32 v76, v76, v77\n"
    "v_cvt_pk_bf16_f32 v77, v78, v79\n"
    "v_accvgpr_read_b32 v12, a148\n"
    "v_mfma_f32_16x16x32_bf16 v[132:135], a[64:67], v[68:71], v[132:135]\n"
    "ds_read_b64_tr_b16 a[204:205], v254 offset:13312\n"
    "ds_write_b64 v248, v[76:77] offset:14336\n"
    "v_accvgpr_read_b32 v13, a149\n"
    "v_mfma_f32_16x16x32_bf16 v[132:135], a[68:71], v[4:7], v[132:135]\n"
    "v_accvgpr_read_b32 v14, a150\n"
    "v_accvgpr_read_b32 v15, a151\n"
    "v_mul_f32 v12, v12, v239\n"
    "v_mul_f32 v13, v13, v239\n"
    "v_mfma_f32_16x16x32_bf16 v[136:139], a[72:75], v[68:71], v[136:139]\n"
    "ds_read_b64_tr_b16 a[206:207], v254 offset:19456\n"
    "v_mul_f32 v14, v14, v239\n"
    "v_mul_f32 v15, v15, v239\n"
    "v_cvt_pk_bf16_f32 v12, v12, v13\n"
    "v_cvt_pk_bf16_f32 v13, v14, v15\n"
    "v_mfma_f32_16x16x32_bf16 v[136:139], a[76:79], v[4:7], v[136:139]\n"
    "ds_read_b64_tr_b16 a[44:45], v227 offset:26112\n"
    "ds_write_b64 v245, v[12:13] offset:14336\n"
    "v_mfma_f32_16x16x32_bf16 v[140:143], a[80:83], v[68:71], v[140:143]\n"
    "ds_read_b64_tr_b16 a[46:47], v227 offset:32256\n"
    "v_accvgpr_read_b32 v92, a152\n"
    "v_accvgpr_read_b32 v93, a153\n"
    "v_accvgpr_read_b32 v94, a154\n"
    "v_accvgpr_read_b32 v95, a155\n"
    "v_mfma_f32_16x16x32_bf16 v[140:143], a[84:87], v[4:7], v[140:143]\n"
    "ds_read_b64_tr_b16 a[220:221], v227 offset:38400\n"
    "v_mul_f32 v92, v92, v239\n"
    "v_mul_f32 v93, v93, v239\n"
    "v_mul_f32 v94, v94, v239\n"
    "v_mfma_f32_16x16x32_bf16 a[244:247], v[240:243], v[84:87], a[244:247]\n"
    "ds_read_b64_tr_b16 a[222:223], v227 offset:44544\n"
    "v_mul_f32 v95, v95, v239\n"
    "v_cvt_pk_bf16_f32 v92, v92, v93\n"
    "v_cvt_pk_bf16_f32 v93, v94, v95\n"
    "v_accvgpr_read_b32 v28, a156\n"
    "v_mfma_f32_16x16x32_bf16 a[244:247], v[240:243], v[20:23], a[244:247]\n"
    "ds_read_b64_tr_b16 a[224:225], v227 offset:1536\n"
    "s_waitcnt lgkmcnt(11)\n"
    "buffer_store_dwordx4 a[96:99], v250, s[80:83], s43 offen offset:0 nt sc1\n"
    "v_accvgpr_read_b32 v29, a157\n"
    "v_mfma_f32_16x16x32_bf16 v[176:179], a[56:59], v[84:87], v[176:179]\n"
    "ds_read_b64_tr_b16 a[226:227], v227 offset:7680\n"
    "ds_write_b64 v246, v[92:93] offset:14336\n"
    "v_accvgpr_read_b32 v30, a158\n"
    "v_mfma_f32_16x16x32_bf16 v[176:179], a[60:63], v[20:23], v[176:179]\n"
    "ds_read_b64_tr_b16 a[228:229], v227 offset:13824\n"
    "v_accvgpr_read_b32 v31, a159\n"
    "v_mul_f32 v28, v28, v239\n"
    "v_mul_f32 v29, v29, v239\n"
    "v_mfma_f32_16x16x32_bf16 v[180:183], a[64:67], v[84:87], v[180:183]\n"
    "ds_read_b64_tr_b16 a[230:231], v227 offset:19968\n"
    "buffer_store_dwordx4 a[100:103], v250, s[80:83], s43 offen offset:3072 nt sc1\n"
    "v_mul_f32 v30, v30, v239\n"
    "v_mul_f32 v31, v31, v239\n"
    "v_mfma_f32_16x16x32_bf16 v[180:183], a[68:71], v[20:23], v[180:183]\n"
    "ds_read_b64_tr_b16 a[40:41], v253 offset:1536\n"
    "v_cvt_pk_bf16_f32 v28, v28, v29\n"
    "v_cvt_pk_bf16_f32 v29, v30, v31\n"
    "v_accvgpr_read_b32 v236, a240\n"
    "v_mfma_f32_16x16x32_bf16 v[184:187], a[72:75], v[84:87], v[184:187]\n"
    "ds_read_b64_tr_b16 a[42:43], v253 offset:7680\n"
    "ds_write_b64 v247, v[28:29] offset:14336\n"
    "v_add_f32 v231, v231, v236\n"
    "v_mfma_f32_16x16x32_bf16 v[184:187], a[76:79], v[20:23], v[184:187]\n"
    "ds_read_b64_tr_b16 a[216:217], v253 offset:13824\n"
    "ds_read_b128 a[144:147], v249 offset:14336\n"
    "v_rcp_f32 v236, v236\n"
    "v_mfma_f32_16x16x32_bf16 v[188:191], a[80:83], v[84:87], v[188:191]\n"
    "ds_read_b64_tr_b16 a[218:219], v253 offset:19968\n"
    "ds_read_b128 a[148:151], v249 offset:15360\n"
    "v_mul_f32 v128, v128, v236\n"
    "v_mul_f32 v129, v129, v236\n"
    "v_mul_f32 v130, v130, v236\n"
    "v_mfma_f32_16x16x32_bf16 v[188:191], a[84:87], v[20:23], v[188:191]\n"
    "v_mul_f32 v131, v131, v236\n"
    "v_cvt_pk_bf16_f32 v128, v128, v129\n"
    "v_cvt_pk_bf16_f32 v129, v130, v131\n"
    "v_mfma_f32_16x16x32_bf16 a[112:115], a[88:91], v[96:99], a[112:115]\n"
    "ds_read_b64_tr_b16 a[232:233], v254 offset:1536\n"
    "ds_write_b64 v248, v[128:129] offset:8192\n"
    "v_mul_f32 v132, v132, v236\n"
    "v_mfma_f32_16x16x32_bf16 a[112:115], a[92:95], v[104:107], a[112:115]\n"
    "ds_read_b64_tr_b16 a[234:235], v254 offset:7680\n"
    "v_mul_f32 v133, v133, v236\n"
    "v_mul_f32 v134, v134, v236\n"
    "v_mul_f32 v135, v135, v236\n"
    "v_mfma_f32_16x16x32_bf16 a[112:115], a[0:3], v[32:35], a[112:115]\n"
    "ds_read_b64_tr_b16 a[236:237], v254 offset:13824\n"
    "v_cvt_pk_bf16_f32 v132, v132, v133\n"
    "v_cvt_pk_bf16_f32 v133, v134, v135\n"
    "v_mul_f32 v136, v136, v236\n"
    "v_mul_f32 v137, v137, v236\n"
    "v_mfma_f32_16x16x32_bf16 a[112:115], a[4:7], v[40:43], a[112:115]\n"
    "ds_read_b64_tr_b16 a[238:239], v254 offset:19968\n"
    "ds_write_b64 v245, v[132:133] offset:8192\n"
    "v_mfma_f32_16x16x32_bf16 a[112:115], a[8:11], v[100:103], a[112:115]\n"
    "v_mul_f32 v138, v138, v236\n"
    "v_mul_f32 v139, v139, v236\n"
    "v_cvt_pk_bf16_f32 v136, v136, v137\n"
    "v_cvt_pk_bf16_f32 v137, v138, v139\n"
    "v_mul_f32 v140, v140, v236\n"
    "v_mfma_f32_16x16x32_bf16 a[112:115], a[12:15], v[36:39], a[112:115]\n"
    "ds_read_b64_tr_b16 a[192:193], v226 offset:26624\n"
    "ds_write_b64 v246, v[136:137] offset:8192\n"
    "v_mfma_f32_16x16x32_bf16 a[116:119], a[16:19], v[96:99], a[116:119]\n"
    "v_mul_f32 v141, v141, v236\n"
    "v_mul_f32 v142, v142, v236\n"
    "v_mul_f32 v143, v143, v236\n"
    "v_cvt_pk_bf16_f32 v140, v140, v141\n"
    "v_cvt_pk_bf16_f32 v141, v142, v143\n"
    "v_mfma_f32_16x16x32_bf16 a[116:119], a[20:23], v[104:107], a[116:119]\n"
    "ds_write_b64 v247, v[140:141] offset:8192\n"
    "ds_read_b128 v[128:131], v249 offset:8192\n"
    "v_mfma_f32_16x16x32_bf16 a[116:119], a[24:27], v[32:35], a[116:119]\n"
    "ds_read_b64_tr_b16 a[194:195], v226 offset:32768\n"
    "s_waitcnt lgkmcnt(11)\n"
    "buffer_store_dwordx4 a[144:147], v250, s[80:83], s44 offen offset:0 nt sc1\n"
    "v_accvgpr_read_b32 v237, a244\n"
    "v_mfma_f32_16x16x32_bf16 a[116:119], a[200:203], v[40:43], a[116:119]\n"
    "ds_read_b128 v[132:135], v249 offset:9216\n"
    "v_add_f32 v231, v231, v237\n"
    "v_rcp_f32 v237, v237\n"
    "v_mfma_f32_16x16x32_bf16 a[116:119], a[28:31], v[100:103], a[116:119]\n"
    "v_cmp_ngt_f32 vcc, s41, v231\n"
    "v_mul_f32 v176, v176, v237\n"
    "v_mul_f32 v177, v177, v237\n"
    "v_mfma_f32_16x16x32_bf16 a[116:119], a[204:207], v[36:39], a[116:119]\n"
    "ds_read_b64_tr_b16 a[196:197], v226 offset:38912\n"
    "buffer_store_dwordx4 a[148:151], v250, s[80:83], s44 offen offset:3072 nt sc1\n"
    "s_or_b64 s[50:51], s[50:51], vcc\n"
    "v_mfma_f32_16x16x32_bf16 a[120:123], a[32:35], v[96:99], a[120:123]\n"
    "v_mul_f32 v178, v178, v237\n"
    "v_mul_f32 v179, v179, v237\n"
    "v_mfma_f32_16x16x32_bf16 a[120:123], a[208:211], v[104:107], a[120:123]\n"
    "v_cvt_pk_bf16_f32 v176, v176, v177\n"
    "v_cvt_pk_bf16_f32 v177, v178, v179\n"
    "v_mfma_f32_16x16x32_bf16 a[120:123], a[36:39], v[32:35], a[120:123]\n"
    "ds_read_b64_tr_b16 a[198:199], v226 offset:45056\n"
    "ds_write_b64 v248, v[176:177] offset:10240\n"
    "v_mfma_f32_16x16x32_bf16 a[120:123], a[212:215], v[40:43], a[120:123]\n"
    "v_mul_f32 v180, v180, v237\n"
    "v_mfma_f32_16x16x32_bf16 a[120:123], a[40:43], v[100:103], a[120:123]\n"
    "v_mul_f32 v181, v181, v237\n"
    "v_mul_f32 v182, v182, v237\n"
    "v_mfma_f32_16x16x32_bf16 a[120:123], a[216:219], v[36:39], a[120:123]\n"
    "ds_read_b64_tr_b16 a[48:49], v226 offset:2048\n"
    "v_mul_f32 v183, v183, v237\n"
    "v_cvt_pk_bf16_f32 v180, v180, v181\n"
    "v_mfma_f32_16x16x32_bf16 a[124:127], a[44:47], v[96:99], a[124:127]\n"
    "v_cvt_pk_bf16_f32 v181, v182, v183\n"
    "v_mul_f32 v184, v184, v237\n"
    "v_mfma_f32_16x16x32_bf16 a[124:127], a[220:223], v[104:107], a[124:127]\n"
    "ds_write_b64 v245, v[180:181] offset:10240\n"
    "s_waitcnt lgkmcnt(5)\n"
    "v_mfma_f32_16x16x32_bf16 a[124:127], a[224:227], v[32:35], a[124:127]\n"
    "ds_read_b64_tr_b16 a[50:51], v226 offset:8192\n"
    "buffer_store_dwordx4 v[128:131], v250, s[80:83], 0 offen offset:0 nt sc1\n"
    "v_mfma_f32_16x16x32_bf16 a[124:127], a[228:231], v[40:43], a[124:127]\n"
    "v_mul_f32 v185, v185, v237\n"
    "v_mul_f32 v186, v186, v237\n"
    "v_mfma_f32_16x16x32_bf16 a[124:127], a[232:235], v[100:103], a[124:127]\n"
    "v_mul_f32 v187, v187, v237\n"
    "v_cvt_pk_bf16_f32 v184, v184, v185\n"
    "v_cvt_pk_bf16_f32 v185, v186, v187\n"
    "v_mfma_f32_16x16x32_bf16 a[124:127], a[236:239], v[36:39], a[124:127]\n"
    "ds_read_b64_tr_b16 a[52:53], v226 offset:14336\n"
    "buffer_store_dwordx4 v[132:135], v250, s[80:83], 0 offen offset:3072 nt sc1\n"
    "v_mfma_f32_16x16x32_bf16 a[160:163], a[88:91], v[112:115], a[160:163]\n"
    "ds_write_b64 v246, v[184:185] offset:10240\n"
    "v_mfma_f32_16x16x32_bf16 a[160:163], a[92:95], v[120:123], a[160:163]\n"
    "v_mul_f32 v188, v188, v237\n"
    "v_mul_f32 v189, v189, v237\n"
    "v_mfma_f32_16x16x32_bf16 a[160:163], a[0:3], v[48:51], a[160:163]\n"
    "ds_read_b64_tr_b16 a[54:55], v226 offset:20480\n"
    "v_mul_f32 v190, v190, v237\n"
    "v_mul_f32 v191, v191, v237\n"
    "v_mfma_f32_16x16x32_bf16 a[160:163], a[4:7], v[56:59], a[160:163]\n"
    "v_cvt_pk_bf16_f32 v188, v188, v189\n"
    "v_cvt_pk_bf16_f32 v189, v190, v191\n"
    "v_accvgpr_read_b32 v108, a112\n"
    "v_mfma_f32_16x16x32_bf16 a[160:163], a[8:11], v[116:119], a[160:163]\n"
    "ds_write_b64 v247, v[188:189] offset:10240\n"
    "v_mfma_f32_16x16x32_bf16 a[160:163], a[12:15], v[52:55], a[160:163]\n"
    "ds_read_b64_tr_b16 a[56:57], v253 offset:2048\n"
    "ds_read_b128 v[176:179], v249 offset:10240\n"
    "v_mfma_f32_16x16x32_bf16 a[164:167], a[16:19], v[112:115], a[164:167]\n"
    "ds_read_b128 v[180:183], v249 offset:11264\n"
    "v_accvgpr_read_b32 v109, a113\n"
    "v_accvgpr_read_b32 v110, a114\n"
    "v_mfma_f32_16x16x32_bf16 a[164:167], a[20:23], v[120:123], a[164:167]\n"
    "v_accvgpr_read_b32 v111, a115\n"
    "v_mul_f32 v108, v108, v238\n"
    "v_mfma_f32_16x16x32_bf16 a[164:167], a[24:27], v[48:51], a[164:167]\n"
    "v_mul_f32 v109, v109, v238\n"
    "v_mul_f32 v110, v110, v238\n"
    "v_mul_f32 v111, v111, v238\n"
    "v_mfma_f32_16x16x32_bf16 a[164:167], a[200:203], v[56:59], a[164:167]\n"
    "ds_read_b64_tr_b16 a[58:59], v253 offset:8192\n"
    "v_cvt_pk_bf16_f32 v108, v108, v109\n"
    "v_cvt_pk_bf16_f32 v109, v110, v111\n"
    "v_mfma_f32_16x16x32_bf16 a[164:167], a[28:31], v[116:119], a[164:167]\n"
    "ds_write_b64 v248, v[108:109] offset:12288\n"
    "v_mfma_f32_16x16x32_bf16 a[164:167], a[204:207], v[52:55], a[164:167]\n"
    "v_accvgpr_read_b32 v44, a116\n"
    "v_accvgpr_read_b32 v45, a117\n"
    "v_mfma_f32_16x16x32_bf16 a[168:171], a[32:35], v[112:115], a[168:171]\n"
    "v_accvgpr_read_b32 v46, a118\n"
    "v_accvgpr_read_b32 v47, a119\n"
    "v_mul_f32 v44, v44, v238\n"
    "v_mfma_f32_16x16x32_bf16 a[168:171], a[208:211], v[120:123], a[168:171]\n"
    "ds_read_b64_tr_b16 a[60:61], v253 offset:14336\n"
    "v_mul_f32 v45, v45, v238\n"
    "v_mul_f32 v46, v46, v238\n"
    "v_mfma_f32_16x16x32_bf16 a[168:171], a[36:39], v[48:51], a[168:171]\n"
    "v_mul_f32 v47, v47, v238\n"
    "v_cvt_pk_bf16_f32 v44, v44, v45\n"
    "v_mfma_f32_16x16x32_bf16 a[168:171], a[212:215], v[56:59], a[168:171]\n"
    "v_cvt_pk_bf16_f32 v45, v46, v47\n"
    "v_accvgpr_read_b32 v124, a120\n"
    "v_accvgpr_read_b32 v125, a121\n"
    "v_mfma_f32_16x16x32_bf16 a[168:171], a[40:43], v[116:119], a[168:171]\n"
    "ds_write_b64 v245, v[44:45] offset:12288\n"
    "s_waitcnt lgkmcnt(4)\n"
    "v_mfma_f32_16x16x32_bf16 a[168:171], a[216:219], v[52:55], a[168:171]\n"
    "ds_read_b64_tr_b16 a[62:63], v253 offset:20480\n"
    "buffer_store_dwordx4 v[176:179], v250, s[80:83], s42 offen offset:0 nt sc1\n"
    "v_mfma_f32_16x16x32_bf16 a[172:175], a[44:47], v[112:115], a[172:175]\n"
    "v_accvgpr_read_b32 v126, a122\n"
    "v_accvgpr_read_b32 v127, a123\n"
    "v_mfma_f32_16x16x32_bf16 a[172:175], a[220:223], v[120:123], a[172:175]\n"
    "v_mul_f32 v124, v124, v238\n"
    "v_mul_f32 v125, v125, v238\n"
    "v_mul_f32 v126, v126, v238\n"
    "v_mfma_f32_16x16x32_bf16 a[172:175], a[224:227], v[48:51], a[172:175]\n"
    "buffer_store_dwordx4 v[180:183], v250, s[80:83], s42 offen offset:3072 nt sc1\n"
    "v_mul_f32 v127, v127, v238\n"
    "v_mfma_f32_16x16x32_bf16 a[172:175], a[228:231], v[56:59], a[172:175]\n"
    "ds_read_b64_tr_b16 a[64:65], v227 offset:26624\n"
    "v_cvt_pk_bf16_f32 v124, v124, v125\n"
    "v_mfma_f32_16x16x32_bf16 a[172:175], a[232:235], v[116:119], a[172:175]\n"
    "v_cvt_pk_bf16_f32 v125, v126, v127\n"
    "v_accvgpr_read_b32 v60, a124\n"
    "v_accvgpr_read_b32 v61, a125\n"
    "v_mfma_f32_16x16x32_bf16 a[172:175], a[236:239], v[52:55], a[172:175]\n"
    "ds_write_b64 v246, v[124:125] offset:12288\n"
    "v_mfma_f32_16x16x32_bf16 v[144:147], a[88:91], v[64:67], v[144:147]\n"
    "v_accvgpr_read_b32 v62, a126\n"
    "v_accvgpr_read_b32 v63, a127\n"
    "v_mfma_f32_16x16x32_bf16 v[144:147], a[92:95], v[72:75], v[144:147]\n"
    "v_mul_f32 v60, v60, v238\n"
    "v_mul_f32 v61, v61, v238\n"
    "v_mul_f32 v62, v62, v238\n"
    "v_mfma_f32_16x16x32_bf16 v[144:147], a[0:3], v[0:3], v[144:147]\n"
    "ds_read_b64_tr_b16 a[66:67], v227 offset:32768\n"
    "v_mul_f32 v63, v63, v238\n"
    "v_cvt_pk_bf16_f32 v60, v60, v61\n"
    "v_mfma_f32_16x16x32_bf16 v[144:147], a[4:7], v[8:11], v[144:147]\n"
    "v_cvt_pk_bf16_f32 v61, v62, v63\n"
    "v_accvgpr_read_b32 v76, a160\n"
    "v_mfma_f32_16x16x32_bf16 v[144:147], a[8:11], v[68:71], v[144:147]\n"
    "ds_write_b64 v247, v[60:61] offset:12288\n"
    "ds_read_b128 a[112:115], v249 offset:12288\n"
    "v_mfma_f32_16x16x32_bf16 v[144:147], a[12:15], v[4:7], v[144:147]\n"
    "ds_read_b128 a[116:119], v249 offset:13312\n"
    "v_accvgpr_read_b32 v77, a161\n"
    "v_mfma_f32_16x16x32_bf16 v[148:151], a[16:19], v[64:67], v[148:151]\n"
    "v_accvgpr_read_b32 v78, a162\n"
    "v_accvgpr_read_b32 v79, a163\n"
    "v_mul_f32 v76, v76, v239\n"
    "v_mfma_f32_16x16x32_bf16 v[148:151], a[20:23], v[72:75], v[148:151]\n"
    "ds_read_b64_tr_b16 a[68:69], v227 offset:38912\n"
    "v_mul_f32 v77, v77, v239\n"
    "v_mfma_f32_16x16x32_bf16 v[148:151], a[24:27], v[0:3], v[148:151]\n"
    "v_mul_f32 v78, v78, v239\n"
    "v_mul_f32 v79, v79, v239\n"
    "v_cvt_pk_bf16_f32 v76, v76, v77\n"
    "v_mfma_f32_16x16x32_bf16 v[148:151], a[200:203], v[8:11], v[148:151]\n"
    "v_cvt_pk_bf16_f32 v77, v78, v79\n"
    "v_accvgpr_read_b32 v12, a164\n"
    "v_accvgpr_read_b32 v13, a165\n"
    "v_mfma_f32_16x16x32_bf16 v[148:151], a[28:31], v[68:71], v[148:151]\n"
    "ds_write_b64 v248, v[76:77] offset:14336\n"
    "v_mfma_f32_16x16x32_bf16 v[148:151], a[204:207], v[4:7], v[148:151]\n"
    "v_accvgpr_read_b32 v14, a166\n"
    "v_accvgpr_read_b32 v15, a167\n"
    "v_mfma_f32_16x16x32_bf16 v[152:155], a[32:35], v[64:67], v[152:155]\n"
    "v_mul_f32 v12, v12, v239\n"
    "v_mul_f32 v13, v13, v239\n"
    "v_mul_f32 v14, v14, v239\n"
    "v_mfma_f32_16x16x32_bf16 v[152:155], a[208:211], v[72:75], v[152:155]\n"
    "ds_read_b64_tr_b16 a[70:71], v227 offset:45056\n"
    "v_mul_f32 v15, v15, v239\n"
    "v_mfma_f32_16x16x32_bf16 v[152:155], a[36:39], v[0:3], v[152:155]\n"
    "ds_read_b64_tr_b16 a[72:73], v227 offset:2048\n"
    "v_cvt_pk_bf16_f32 v12, v12, v13\n"
    "v_cvt_pk_bf16_f32 v13, v14, v15\n"
    "v_mfma_f32_16x16x32_bf16 v[152:155], a[212:215], v[8:11], v[152:155]\n"
    "ds_read_b64_tr_b16 a[74:75], v227 offset:8192\n"
    "ds_write_b64 v245, v[12:13] offset:14336\n"
    "s_waitcnt lgkmcnt(6)\n"
    "v_mfma_f32_16x16x32_bf16 v[152:155], a[40:43], v[68:71], v[152:155]\n"
    "ds_read_b64_tr_b16 a[76:77], v227 offset:14336\n"
    "buffer_store_dwordx4 a[112:115], v250, s[80:83], s43 offen offset:128 nt sc1\n"
    "v_mfma_f32_16x16x32_bf16 v[152:155], a[216:219], v[4:7], v[152:155]\n"
    "ds_read_b64_tr_b16 a[78:79], v227 offset:20480\n"
    "v_accvgpr_read_b32 v92, a168\n"
    "v_mfma_f32_16x16x32_bf16 v[156:159], a[44:47], v[64:67], v[156:159]\n"
    "ds_read_b64_tr_b16 a[80:81], v254 offset:2048\n"
    "v_accvgpr_read_b32 v93, a169\n"
    "v_mfma_f32_16x16x32_bf16 v[156:159], a[220:223], v[72:75], v[156:159]\n"
    "ds_read_b64_tr_b16 a[82:83], v254 offset:8192\n"
    "buffer_store_dwordx4 a[116:119], v250, s[80:83], s43 offen offset:3200 nt sc1\n"
    "v_mfma_f32_16x16x32_bf16 v[156:159], a[224:227], v[0:3], v[156:159]\n"
    "ds_read_b64_tr_b16 a[84:85], v254 offset:14336\n"
    "v_accvgpr_read_b32 v94, a170\n"
    "v_mfma_f32_16x16x32_bf16 v[156:159], a[228:231], v[8:11], v[156:159]\n"
    "ds_read_b64_tr_b16 a[86:87], v254 offset:20480\n"
    "v_accvgpr_read_b32 v95, a171\n"
    "v_mul_f32 v92, v92, v239\n"
    "v_mfma_f32_16x16x32_bf16 v[156:159], a[232:235], v[68:71], v[156:159]\n"
    "v_mul_f32 v93, v93, v239\n"
    "v_mul_f32 v94, v94, v239\n"
    "v_mul_f32 v95, v95, v239\n"
    "v_mfma_f32_16x16x32_bf16 v[156:159], a[236:239], v[4:7], v[156:159]\n"
    "v_cvt_pk_bf16_f32 v92, v92, v93\n"
    "v_cvt_pk_bf16_f32 v93, v94, v95\n"
    "v_mfma_f32_16x16x32_bf16 v[192:195], a[88:91], v[80:83], v[192:195]\n"
    "ds_write_b64 v246, v[92:93] offset:14336\n"
    "v_mfma_f32_16x16x32_bf16 v[192:195], a[92:95], v[88:91], v[192:195]\n"
    "ds_read_b64_tr_b16 a[88:89], v226 offset:27136\n"
    "v_accvgpr_read_b32 v28, a172\n"
    "v_accvgpr_read_b32 v29, a173\n"
    "v_mfma_f32_16x16x32_bf16 v[192:195], a[0:3], v[16:19], v[192:195]\n"
    "ds_read_b64_tr_b16 a[90:91], v226 offset:33280\n"
    "v_accvgpr_read_b32 v30, a174\n"
    "v_accvgpr_read_b32 v31, a175\n"
    "v_mfma_f32_16x16x32_bf16 v[192:195], a[4:7], v[24:27], v[192:195]\n"
    "ds_read_b64_tr_b16 a[92:93], v226 offset:39424\n"
    "v_mul_f32 v28, v28, v239\n"
    "v_mfma_f32_16x16x32_bf16 v[192:195], a[8:11], v[84:87], v[192:195]\n"
    "ds_read_b64_tr_b16 a[94:95], v226 offset:45568\n"
    "v_mul_f32 v29, v29, v239\n"
    "v_mul_f32 v30, v30, v239\n"
    "v_mfma_f32_16x16x32_bf16 v[192:195], a[12:15], v[20:23], v[192:195]\n"
    "ds_read_b64_tr_b16 a[0:1], v226 offset:2560\n"
    "v_mul_f32 v31, v31, v239\n"
    "v_cvt_pk_bf16_f32 v28, v28, v29\n"
    "v_mfma_f32_16x16x32_bf16 v[196:199], a[16:19], v[80:83], v[196:199]\n"
    "ds_read_b64_tr_b16 a[2:3], v226 offset:8704\n"
    "v_cvt_pk_bf16_f32 v29, v30, v31\n"
    "v_mfma_f32_16x16x32_bf16 v[196:199], a[20:23], v[88:91], v[196:199]\n"
    "ds_read_b64_tr_b16 a[4:5], v226 offset:14848\n"
    "ds_write_b64 v247, v[28:29] offset:14336\n"
    "v_mfma_f32_16x16x32_bf16 v[196:199], a[24:27], v[16:19], v[196:199]\n"
    "ds_read_b128 a[160:163], v249 offset:14336\n"
    "ds_read_b128 a[164:167], v249 offset:15360\n"
    "v_mfma_f32_16x16x32_bf16 v[196:199], a[200:203], v[24:27], v[196:199]\n"
    "ds_read_b64_tr_b16 a[6:7], v226 offset:20992\n"
    "v_mul_f32 v144, v144, v236\n"
    "v_mfma_f32_16x16x32_bf16 v[196:199], a[28:31], v[84:87], v[196:199]\n"
    "ds_read_b64_tr_b16 a[8:9], v253 offset:2560\n"
    "v_mul_f32 v145, v145, v236\n"
    "v_mul_f32 v146, v146, v236\n"
    "v_mfma_f32_16x16x32_bf16 v[196:199], a[204:207], v[20:23], v[196:199]\n"
    "ds_read_b64_tr_b16 a[10:11], v253 offset:8704\n"
    "v_mul_f32 v147, v147, v236\n"
    "v_cvt_pk_bf16_f32 v144, v144, v145\n"
    "v_mfma_f32_16x16x32_bf16 v[200:203], a[32:35], v[80:83], v[200:203]\n"
    "ds_read_b64_tr_b16 a[12:13], v253 offset:14848\n"
    "v_cvt_pk_bf16_f32 v145, v146, v147\n"
    "v_mul_f32 v148, v148, v236\n"
    "v_mfma_f32_16x16x32_bf16 v[200:203], a[208:211], v[88:91], v[200:203]\n"
    "ds_read_b64_tr_b16 a[14:15], v253 offset:20992\n"
    "v_mul_f32 v149, v149, v236\n"
    "v_mfma_f32_16x16x32_bf16 v[200:203], a[36:39], v[16:19], v[200:203]\n"
    "ds_read_b64_tr_b16 a[16:17], v227 offset:27136\n"
    "ds_write_b64 v248, v[144:145] offset:8192\n"
    "v_mfma_f32_16x16x32_bf16 v[200:203], a[212:215], v[24:27], v[200:203]\n"
    "ds_read_b64_tr_b16 a[18:19], v227 offset:33280\n"
    "v_mul_f32 v150, v150, v236\n"
    "v_mfma_f32_16x16x32_bf16 v[200:203], a[40:43], v[84:87], v[200:203]\n"
    "ds_read_b64_tr_b16 a[20:21], v227 offset:39424\n"
    "v_mul_f32 v151, v151, v236\n"
    "v_mfma_f32_16x16x32_bf16 v[200:203], a[216:219], v[20:23], v[200:203]\n"
    "ds_read_b64_tr_b16 a[22:23], v227 offset:45568\n"
    "v_cvt_pk_bf16_f32 v148, v148, v149\n"
    "v_cvt_pk_bf16_f32 v149, v150, v151\n"
    "v_mfma_f32_16x16x32_bf16 v[204:207], a[44:47], v[80:83], v[204:207]\n"
    "ds_read_b64_tr_b16 a[24:25], v227 offset:2560\n"
    "ds_write_b64 v245, v[148:149] offset:8192\n"
    "s_waitcnt lgkmcnt(12)\n"
    "v_mfma_f32_16x16x32_bf16 v[204:207], a[220:223], v[88:91], v[204:207]\n"
    "ds_read_b64_tr_b16 a[26:27], v227 offset:8704\n"
    "buffer_store_dwordx4 a[160:163], v250, s[80:83], s44 offen offset:128 nt sc1\n"
    "v_mfma_f32_16x16x32_bf16 v[204:207], a[224:227], v[16:19], v[204:207]\n"
    "ds_read_b64_tr_b16 a[200:201], v227 offset:14848\n"
    "v_mul_f32 v152, v152, v236\n"
    "v_mfma_f32_16x16x32_bf16 v[204:207], a[228:231], v[24:27], v[204:207]\n"
    "ds_read_b64_tr_b16 a[202:203], v227 offset:20992\n"
    "v_mul_f32 v153, v153, v236\n"
    "v_mfma_f32_16x16x32_bf16 v[204:207], a[232:235], v[84:87], v[204:207]\n"
    "ds_read_b64_tr_b16 a[28:29], v254 offset:2560\n"
    "buffer_store_dwordx4 a[164:167], v250, s[80:83], s44 offen offset:3200 nt sc1\n"
    "v_mfma_f32_16x16x32_bf16 v[204:207], a[236:239], v[20:23], v[204:207]\n"
    "v_mul_f32 v154, v154, v236\n"
    "v_mfma_f32_16x16x32_bf16 a[128:131], a[192:195], v[96:99], a[128:131]\n"
    "ds_read_b64_tr_b16 a[30:31], v254 offset:8704\n"
    "v_mul_f32 v155, v155, v236\n"
    "v_cvt_pk_bf16_f32 v152, v152, v153\n"
    "v_mfma_f32_16x16x32_bf16 a[128:131], a[196:199], v[104:107], a[128:131]\n"
    "ds_read_b64_tr_b16 a[204:205], v254 offset:14848\n"
    "v_cvt_pk_bf16_f32 v153, v154, v155\n"
    "v_mul_f32 v156, v156, v236\n"
    "v_mfma_f32_16x16x32_bf16 a[128:131], a[48:51], v[32:35], a[128:131]\n"
    "ds_write_b64 v246, v[152:153] offset:8192\n"
    "v_mfma_f32_16x16x32_bf16 a[128:131], a[52:55], v[40:43], a[128:131]\n"
    "v_mul_f32 v157, v157, v236\n"
    "v_mul_f32 v158, v158, v236\n"
    "v_mfma_f32_16x16x32_bf16 a[128:131], a[56:59], v[100:103], a[128:131]\n"
    "ds_read_b64_tr_b16 a[206:207], v254 offset:20992\n"
    "v_mul_f32 v159, v159, v236\n"
    "v_cvt_pk_bf16_f32 v156, v156, v157\n"
    "v_mfma_f32_16x16x32_bf16 a[128:131], a[60:63], v[36:39], a[128:131]\n"
    "v_cvt_pk_bf16_f32 v157, v158, v159\n"
    "v_mul_f32 v192, v192, v237\n"
    "v_mfma_f32_16x16x32_bf16 a[132:135], a[64:67], v[96:99], a[132:135]\n"
    "ds_write_b64 v247, v[156:157] offset:8192\n"
    "ds_read_b128 v[144:147], v249 offset:8192\n"
    "v_mfma_f32_16x16x32_bf16 a[132:135], a[68:71], v[104:107], a[132:135]\n"
    "s_waitcnt lgkmcnt(15)\n"
    "ds_read_b128 v[148:151], v249 offset:9216\n"
    "v_mfma_f32_16x16x32_bf16 a[132:135], a[72:75], v[32:35], a[132:135]\n"
    "v_mul_f32 v193, v193, v237\n"
    "v_mul_f32 v194, v194, v237\n"
    "v_mul_f32 v195, v195, v237\n"
    "v_mfma_f32_16x16x32_bf16 a[132:135], a[76:79], v[40:43], a[132:135]\n"
    "v_cvt_pk_bf16_f32 v192, v192, v193\n"
    "v_cvt_pk_bf16_f32 v193, v194, v195\n"
    "v_mfma_f32_16x16x32_bf16 a[132:135], a[80:83], v[100:103], a[132:135]\n"
    "ds_write_b64 v248, v[192:193] offset:10240\n"
    "v_mfma_f32_16x16x32_bf16 a[132:135], a[84:87], v[36:39], a[132:135]\n"
    "v_mul_f32 v196, v196, v237\n"
    "v_mul_f32 v197, v197, v237\n"
    "v_mul_f32 v198, v198, v237\n"
    "v_mfma_f32_16x16x32_bf16 a[136:139], a[88:91], v[96:99], a[136:139]\n"
    "v_mul_f32 v199, v199, v237\n"
    "v_cvt_pk_bf16_f32 v196, v196, v197\n"
    "v_mfma_f32_16x16x32_bf16 a[136:139], a[92:95], v[104:107], a[136:139]\n"
    "v_cvt_pk_bf16_f32 v197, v198, v199\n"
    "v_mul_f32 v200, v200, v237\n"
    "v_mul_f32 v201, v201, v237\n"
    "v_mfma_f32_16x16x32_bf16 a[136:139], a[0:3], v[32:35], a[136:139]\n"
    "ds_write_b64 v245, v[196:197] offset:10240\n"
    "v_mfma_f32_16x16x32_bf16 a[136:139], a[4:7], v[40:43], a[136:139]\n"
    "s_waitcnt lgkmcnt(15)\n"
    "v_mul_f32 v202, v202, v237\n"
    "v_mfma_f32_16x16x32_bf16 a[136:139], a[8:11], v[100:103], a[136:139]\n"
    "v_mul_f32 v203, v203, v237\n"
    "v_cvt_pk_bf16_f32 v200, v200, v201\n"
    "v_cvt_pk_bf16_f32 v201, v202, v203\n"
    "v_mfma_f32_16x16x32_bf16 a[136:139], a[12:15], v[36:39], a[136:139]\n"
    "s_waitcnt lgkmcnt(2)\n"
    "buffer_store_dwordx4 v[144:147], v250, s[80:83], 0 offen offset:128 nt sc1\n"
    "v_mul_f32 v204, v204, v237\n"
    "v_mfma_f32_16x16x32_bf16 a[140:143], a[16:19], v[96:99], a[140:143]\n"
    "ds_write_b64 v246, v[200:201] offset:10240\n"
    "v_mfma_f32_16x16x32_bf16 a[140:143], a[20:23], v[104:107], a[140:143]\n"
    "v_mul_f32 v205, v205, v237\n"
    "v_mul_f32 v206, v206, v237\n"
    "v_mfma_f32_16x16x32_bf16 a[140:143], a[24:27], v[32:35], a[140:143]\n"
    "buffer_store_dwordx4 v[148:151], v250, s[80:83], 0 offen offset:3200 nt sc1\n"
    "v_mul_f32 v207, v207, v237\n"
    "v_mfma_f32_16x16x32_bf16 a[140:143], a[200:203], v[40:43], a[140:143]\n"
    "v_cvt_pk_bf16_f32 v204, v204, v205\n"
    "v_cvt_pk_bf16_f32 v205, v206, v207\n"
    "v_accvgpr_read_b32 v108, a128\n"
    "v_mfma_f32_16x16x32_bf16 a[140:143], a[28:31], v[100:103], a[140:143]\n"
    "ds_write_b64 v247, v[204:205] offset:10240\n"
    "v_mfma_f32_16x16x32_bf16 a[140:143], a[204:207], v[36:39], a[140:143]\n"
    "ds_read_b128 v[192:195], v249 offset:10240\n"
    "ds_read_b128 v[196:199], v249 offset:11264\n"
    "v_mfma_f32_16x16x32_bf16 a[176:179], a[192:195], v[112:115], a[176:179]\n"
    "v_accvgpr_read_b32 v109, a129\n"
    "v_accvgpr_read_b32 v110, a130\n"
    "v_accvgpr_read_b32 v111, a131\n"
    "v_mfma_f32_16x16x32_bf16 a[176:179], a[196:199], v[120:123], a[176:179]\n"
    "v_mul_f32 v108, v108, v238\n"
    "v_mul_f32 v109, v109, v238\n"
    "v_mfma_f32_16x16x32_bf16 a[176:179], a[48:51], v[48:51], a[176:179]\n"
    "v_mul_f32 v110, v110, v238\n"
    "v_mul_f32 v111, v111, v238\n"
    "v_cvt_pk_bf16_f32 v108, v108, v109\n"
    "v_mfma_f32_16x16x32_bf16 a[176:179], a[52:55], v[56:59], a[176:179]\n"
    "v_cvt_pk_bf16_f32 v109, v110, v111\n"
    "v_accvgpr_read_b32 v44, a132\n"
    "v_accvgpr_read_b32 v45, a133\n"
    "v_mfma_f32_16x16x32_bf16 a[176:179], a[56:59], v[116:119], a[176:179]\n"
    "ds_write_b64 v248, v[108:109] offset:4096\n"
    "v_mfma_f32_16x16x32_bf16 a[176:179], a[60:63], v[52:55], a[176:179]\n"
    "v_accvgpr_read_b32 v46, a134\n"
    "v_accvgpr_read_b32 v47, a135\n"
    "v_mfma_f32_16x16x32_bf16 a[180:183], a[64:67], v[112:115], a[180:183]\n"
    "v_mul_f32 v44, v44, v238\n"
    "v_mul_f32 v45, v45, v238\n"
    "v_mul_f32 v46, v46, v238\n"
    "v_mfma_f32_16x16x32_bf16 a[180:183], a[68:71], v[120:123], a[180:183]\n"
    "v_mul_f32 v47, v47, v238\n"
    "v_cvt_pk_bf16_f32 v44, v44, v45\n"
    "v_cvt_pk_bf16_f32 v45, v46, v47\n"
    "v_mfma_f32_16x16x32_bf16 a[180:183], a[72:75], v[48:51], a[180:183]\n"
    "ds_write_b64 v245, v[44:45] offset:4096\n"
    "v_mfma_f32_16x16x32_bf16 a[180:183], a[76:79], v[56:59], a[180:183]\n"
    "s_waitcnt lgkmcnt(2)\n"
    "buffer_store_dwordx4 v[192:195], v250, s[80:83], s42 offen offset:128 nt sc1\n"
    "v_mfma_f32_16x16x32_bf16 a[180:183], a[80:83], v[116:119], a[180:183]\n"
    "v_accvgpr_read_b32 v124, a136\n"
    "v_accvgpr_read_b32 v125, a137\n"
    "v_accvgpr_read_b32 v126, a138\n"
    "v_mfma_f32_16x16x32_bf16 a[180:183], a[84:87], v[52:55], a[180:183]\n"
    "v_accvgpr_read_b32 v127, a139\n"
    "v_mul_f32 v124, v124, v238\n"
    "v_mfma_f32_16x16x32_bf16 a[184:187], a[88:91], v[112:115], a[184:187]\n"
    "buffer_store_dwordx4 v[196:199], v250, s[80:83], s42 offen offset:3200 nt sc1\n"
    "v_mul_f32 v125, v125, v238\n"
    "v_mfma_f32_16x16x32_bf16 a[184:187], a[92:95], v[120:123], a[184:187]\n"
    "v_mul_f32 v126, v126, v238\n"
    "v_mul_f32 v127, v127, v238\n"
    "v_cvt_pk_bf16_f32 v124, v124, v125\n"
    "v_mfma_f32_16x16x32_bf16 a[184:187], a[0:3], v[48:51], a[184:187]\n"
    "v_cvt_pk_bf16_f32 v125, v126, v127\n"
    "v_accvgpr_read_b32 v60, a140\n"
    "v_accvgpr_read_b32 v61, a141\n"
    "v_mfma_f32_16x16x32_bf16 a[184:187], a[4:7], v[56:59], a[184:187]\n"
    "ds_write_b64 v246, v[124:125] offset:4096\n"
    "v_mfma_f32_16x16x32_bf16 a[184:187], a[8:11], v[116:119], a[184:187]\n"
    "v_accvgpr_read_b32 v62, a142\n"
    "v_accvgpr_read_b32 v63, a143\n"
    "v_mfma_f32_16x16x32_bf16 a[184:187], a[12:15], v[52:55], a[184:187]\n"
    "v_mul_f32 v60, v60, v238\n"
    "v_mul_f32 v61, v61, v238\n"
    "v_mul_f32 v62, v62, v238\n"
    "v_mfma_f32_16x16x32_bf16 a[188:191], a[16:19], v[112:115], a[188:191]\n"
    "v_mul_f32 v63, v63, v238\n"
    "v_cvt_pk_bf16_f32 v60, v60, v61\n"
    "v_mfma_f32_16x16x32_bf16 a[188:191], a[20:23], v[120:123], a[188:191]\n"
    "v_cvt_pk_bf16_f32 v61, v62, v63\n"
    "v_accvgpr_read_b32 v76, a176\n"
    "v_accvgpr_read_b32 v77, a177\n"
    "v_mfma_f32_16x16x32_bf16 a[188:191], a[24:27], v[48:51], a[188:191]\n"
    "ds_write_b64 v247, v[60:61] offset:4096\n"
    "ds_read_b128 a[128:131], v249 offset:4096\n"
    "v_mfma_f32_16x16x32_bf16 a[188:191], a[200:203], v[56:59], a[188:191]\n"
    "ds_read_b128 a[132:135], v249 offset:5120\n"
    "v_accvgpr_read_b32 v78, a178\n"
    "v_mfma_f32_16x16x32_bf16 a[188:191], a[28:31], v[116:119], a[188:191]\n"
    "v_accvgpr_read_b32 v79, a179\n"
    "v_mul_f32 v76, v76, v239\n"
    "v_mfma_f32_16x16x32_bf16 a[188:191], a[204:207], v[52:55], a[188:191]\n"
    "v_mul_f32 v77, v77, v239\n"
    "v_mul_f32 v78, v78, v239\n"
    "v_mul_f32 v79, v79, v239\n"
    "v_mfma_f32_16x16x32_bf16 v[160:163], a[192:195], v[64:67], v[160:163]\n"
    "v_cvt_pk_bf16_f32 v76, v76, v77\n"
    "v_cvt_pk_bf16_f32 v77, v78, v79\n"
    "v_accvgpr_read_b32 v12, a180\n"
    "v_mfma_f32_16x16x32_bf16 v[160:163], a[196:199], v[72:75], v[160:163]\n"
    "ds_write_b64 v248, v[76:77] offset:6144\n"
    "v_mfma_f32_16x16x32_bf16 v[160:163], a[48:51], v[0:3], v[160:163]\n"
    "v_accvgpr_read_b32 v13, a181\n"
    "v_accvgpr_read_b32 v14, a182\n"
    "v_mfma_f32_16x16x32_bf16 v[160:163], a[52:55], v[8:11], v[160:163]\n"
    "v_accvgpr_read_b32 v15, a183\n"
    "v_mul_f32 v12, v12, v239\n"
    "v_mul_f32 v13, v13, v239\n"
    "v_mfma_f32_16x16x32_bf16 v[160:163], a[56:59], v[68:71], v[160:163]\n"
    "v_mul_f32 v14, v14, v239\n"
    "v_mul_f32 v15, v15, v239\n"
    "v_mfma_f32_16x16x32_bf16 v[160:163], a[60:63], v[4:7], v[160:163]\n"
    "v_cvt_pk_bf16_f32 v12, v12, v13\n"
    "v_cvt_pk_bf16_f32 v13, v14, v15\n"
    "v_accvgpr_read_b32 v92, a184\n"
    "v_mfma_f32_16x16x32_bf16 v[164:167], a[64:67], v[64:67], v[164:167]\n"
    "ds_write_b64 v245, v[12:13] offset:6144\n"
    "v_mfma_f32_16x16x32_bf16 v[164:167], a[68:71], v[72:75], v[164:167]\n"
    "s_waitcnt lgkmcnt(2)\n"
    "buffer_store_dwordx4 a[128:131], v250, s[80:83], s43 offen offset:256 nt sc1\n"
    "v_mfma_f32_16x16x32_bf16 v[164:167], a[72:75], v[0:3], v[164:167]\n"
    "v_accvgpr_read_b32 v93, a185\n"
    "v_accvgpr_read_b32 v94, a186\n"
    "v_accvgpr_read_b32 v95, a187\n"
    "v_mfma_f32_16x16x32_bf16 v[164:167], a[76:79], v[8:11], v[164:167]\n"
    "v_mul_f32 v92, v92, v239\n"
    "v_mul_f32 v93, v93, v239\n"
    "v_mul_f32 v94, v94, v239\n"
    "v_mfma_f32_16x16x32_bf16 v[164:167], a[80:83], v[68:71], v[164:167]\n"
    "buffer_store_dwordx4 a[132:135], v250, s[80:83], s43 offen offset:3328 nt sc1\n"
    "v_mul_f32 v95, v95, v239\n"
    "v_mfma_f32_16x16x32_bf16 v[164:167], a[84:87], v[4:7], v[164:167]\n"
    "v_cvt_pk_bf16_f32 v92, v92, v93\n"
    "v_cvt_pk_bf16_f32 v93, v94, v95\n"
    "v_mfma_f32_16x16x32_bf16 v[168:171], a[88:91], v[64:67], v[168:171]\n"
    "ds_write_b64 v246, v[92:93] offset:6144\n"
    "v_mfma_f32_16x16x32_bf16 v[168:171], a[92:95], v[72:75], v[168:171]\n"
    "v_accvgpr_read_b32 v28, a188\n"
    "v_accvgpr_read_b32 v29, a189\n"
    "v_accvgpr_read_b32 v30, a190\n"
    "v_mfma_f32_16x16x32_bf16 v[168:171], a[0:3], v[0:3], v[168:171]\n"
    "v_accvgpr_read_b32 v31, a191\n"
    "v_mul_f32 v28, v28, v239\n"
    "v_mfma_f32_16x16x32_bf16 v[168:171], a[4:7], v[8:11], v[168:171]\n"
    "v_mul_f32 v29, v29, v239\n"
    "v_mul_f32 v30, v30, v239\n"
    "v_mul_f32 v31, v31, v239\n"
    "v_mfma_f32_16x16x32_bf16 v[168:171], a[8:11], v[68:71], v[168:171]\n"
    "v_cvt_pk_bf16_f32 v28, v28, v29\n"
    "v_cvt_pk_bf16_f32 v29, v30, v31\n"
    "v_mul_f32 v160, v160, v236\n"
    "v_mfma_f32_16x16x32_bf16 v[168:171], a[12:15], v[4:7], v[168:171]\n"
    "ds_write_b64 v247, v[28:29] offset:6144\n"
    "v_mfma_f32_16x16x32_bf16 v[172:175], a[16:19], v[64:67], v[172:175]\n"
    "ds_read_b128 a[176:179], v249 offset:6144\n"
    "ds_read_b128 a[180:183], v249 offset:7168\n"
    "v_mfma_f32_16x16x32_bf16 v[172:175], a[20:23], v[72:75], v[172:175]\n"
    "v_mul_f32 v161, v161, v236\n"
    "v_mul_f32 v162, v162, v236\n"
    "v_mul_f32 v163, v163, v236\n"
    "v_mfma_f32_16x16x32_bf16 v[172:175], a[24:27], v[0:3], v[172:175]\n"
    "v_cvt_pk_bf16_f32 v160, v160, v161\n"
    "v_cvt_pk_bf16_f32 v161, v162, v163\n"
    "v_mul_f32 v164, v164, v236\n"
    "v_mfma_f32_16x16x32_bf16 v[172:175], a[200:203], v[8:11], v[172:175]\n"
    "ds_write_b64 v248, v[160:161] offset:0\n"
    "v_mfma_f32_16x16x32_bf16 v[172:175], a[28:31], v[68:71], v[172:175]\n"
    "v_mul_f32 v165, v165, v236\n"
    "v_mul_f32 v166, v166, v236\n"
    "v_mfma_f32_16x16x32_bf16 v[172:175], a[204:207], v[4:7], v[172:175]\n"
    "v_mul_f32 v167, v167, v236\n"
    "v_cvt_pk_bf16_f32 v164, v164, v165\n"
    "v_cvt_pk_bf16_f32 v165, v166, v167\n"
    "v_mfma_f32_16x16x32_bf16 v[208:211], a[192:195], v[80:83], v[208:211]\n"
    "ds_write_b64 v245, v[164:165] offset:0\n"
    "v_mfma_f32_16x16x32_bf16 v[208:211], a[196:199], v[88:91], v[208:211]\n"
    "v_mul_f32 v168, v168, v236\n"
    "v_mul_f32 v169, v169, v236\n"
    "v_mfma_f32_16x16x32_bf16 v[208:211], a[48:51], v[16:19], v[208:211]\n"
    "v_mul_f32 v170, v170, v236\n"
    "v_mul_f32 v171, v171, v236\n"
    "v_cvt_pk_bf16_f32 v168, v168, v169\n"
    "v_mfma_f32_16x16x32_bf16 v[208:211], a[52:55], v[24:27], v[208:211]\n"
    "v_cvt_pk_bf16_f32 v169, v170, v171\n"
    "v_mul_f32 v172, v172, v236\n"
    "v_mfma_f32_16x16x32_bf16 v[208:211], a[56:59], v[84:87], v[208:211]\n"
    "s_waitcnt lgkmcnt(2)\n"
    "buffer_store_dwordx4 a[176:179], v250, s[80:83], s44 offen offset:256 nt sc1\n"
    "v_mul_f32 v173, v173, v236\n"
    "v_mfma_f32_16x16x32_bf16 v[208:211], a[60:63], v[20:23], v[208:211]\n"
    "ds_write_b64 v246, v[168:169] offset:0\n"
    "v_mfma_f32_16x16x32_bf16 v[212:215], a[64:67], v[80:83], v[212:215]\n"
    "v_mul_f32 v174, v174, v236\n"
    "v_mul_f32 v175, v175, v236\n"
    "v_cvt_pk_bf16_f32 v172, v172, v173\n"
    "v_mfma_f32_16x16x32_bf16 v[212:215], a[68:71], v[88:91], v[212:215]\n"
    "buffer_store_dwordx4 a[180:183], v250, s[80:83], s44 offen offset:3328 nt sc1\n"
    "v_mfma_f32_16x16x32_bf16 v[212:215], a[72:75], v[16:19], v[212:215]\n"
    "v_cvt_pk_bf16_f32 v173, v174, v175\n"
    "v_mul_f32 v208, v208, v237\n"
    "v_mul_f32 v209, v209, v237\n"
    "v_mfma_f32_16x16x32_bf16 v[212:215], a[76:79], v[24:27], v[212:215]\n"
    "ds_write_b64 v247, v[172:173] offset:0\n"
    "ds_read_b128 v[160:163], v249 offset:0\n"
    "v_mfma_f32_16x16x32_bf16 v[212:215], a[80:83], v[84:87], v[212:215]\n"
    "ds_read_b128 v[164:167], v249 offset:1024\n"
    "v_mul_f32 v210, v210, v237\n"
    "v_mfma_f32_16x16x32_bf16 v[212:215], a[84:87], v[20:23], v[212:215]\n"
    "v_mul_f32 v211, v211, v237\n"
    "v_cvt_pk_bf16_f32 v208, v208, v209\n"
    "v_mfma_f32_16x16x32_bf16 v[216:219], a[88:91], v[80:83], v[216:219]\n"
    "v_cvt_pk_bf16_f32 v209, v210, v211\n"
    "v_mfma_f32_16x16x32_bf16 v[216:219], a[92:95], v[88:91], v[216:219]\n"
    "ds_write_b64 v248, v[208:209] offset:2048\n"
    "v_mfma_f32_16x16x32_bf16 v[216:219], a[0:3], v[16:19], v[216:219]\n"
    "v_mfma_f32_16x16x32_bf16 v[216:219], a[4:7], v[24:27], v[216:219]\n"
    "v_mul_f32 v212, v212, v237\n"
    "v_mul_f32 v213, v213, v237\n"
    "v_mul_f32 v214, v214, v237\n"
    "v_mul_f32 v215, v215, v237\n"
    "v_mfma_f32_16x16x32_bf16 v[216:219], a[8:11], v[84:87], v[216:219]\n"
    "v_cvt_pk_bf16_f32 v212, v212, v213\n"
    "v_cvt_pk_bf16_f32 v213, v214, v215\n"
    "v_mfma_f32_16x16x32_bf16 v[216:219], a[12:15], v[20:23], v[216:219]\n"
    "ds_write_b64 v245, v[212:213] offset:2048\n"
    "v_mfma_f32_16x16x32_bf16 v[220:223], a[16:19], v[80:83], v[220:223]\n"
    "v_mfma_f32_16x16x32_bf16 v[220:223], a[20:23], v[88:91], v[220:223]\n"
    "v_mfma_f32_16x16x32_bf16 v[220:223], a[24:27], v[16:19], v[220:223]\n"
    "s_waitcnt lgkmcnt(2)\n"
    "buffer_store_dwordx4 v[160:163], v250, s[80:83], 0 offen offset:256 nt sc1\n"
    "v_mfma_f32_16x16x32_bf16 v[220:223], a[200:203], v[24:27], v[220:223]\n"
    "v_mfma_f32_16x16x32_bf16 v[220:223], a[28:31], v[84:87], v[220:223]\n"
    "v_mfma_f32_16x16x32_bf16 v[220:223], a[204:207], v[20:23], v[220:223]\n"
    "buffer_store_dwordx4 v[164:167], v250, s[80:83], 0 offen offset:3328 nt sc1\n"
    "v_mul_f32 v216, v216, v237\n"
    "v_mul_f32 v217, v217, v237\n"
    "v_mul_f32 v218, v218, v237\n"
    "s_nop 7\n"
    "v_mul_f32 v219, v219, v237\n"
    "v_cvt_pk_bf16_f32 v216, v216, v217\n"
    "v_cvt_pk_bf16_f32 v217, v218, v219\n"
    "ds_write_b64 v246, v[216:217] offset:2048\n"
    "v_mul_f32 v220, v220, v237\n"
    "v_mul_f32 v221, v221, v237\n"
    "v_mul_f32 v222, v222, v237\n"
    "v_mul_f32 v223, v223, v237\n"
    "v_cvt_pk_bf16_f32 v220, v220, v221\n"
    "v_cvt_pk_bf16_f32 v221, v222, v223\n"
    "ds_write_b64 v247, v[220:221] offset:2048\n"
    "ds_read_b128 v[208:211], v249 offset:2048\n"
    "ds_read_b128 v[212:215], v249 offset:3072\n"
    "s_waitcnt lgkmcnt(0)\n"
    "buffer_store_dwordx4 v[208:211], v250, s[80:83], s42 offen offset:256 nt sc1\n"
    "buffer_store_dwordx4 v[212:215], v250, s[80:83], s42 offen offset:3328 nt sc1\n"
    "s_cmp_lg_u64 s[50:51], 0\n"
    "s_cselect_b32 s70, 1, 0\n"
    "v_mov_b32 v231, s70\n"
    "ds_write_b32 v249, v231\n"
    "v_subrev_u32 v255, s48, v249\n"
    "v_and_b32 v255, 48, v255\n"
    "v_lshlrev_b32 v255, 10, v255\n"
    "s_add_u32 s70, %3, 0x18000\n"
    "v_add_u32 v255, s70, v255\n"
    "s_waitcnt lgkmcnt(0)\n"
    "s_barrier\n"
    "ds_read_b32 v231, v255\n"
    "s_waitcnt lgkmcnt(0)\n"
    "v_cmp_ne_u32 vcc, 0, v231\n"
    "s_cbranch_vccnz LREDO_%=\n"
    "s_endpgm\n"
    "LREDO_%=:\n"
    "s_waitcnt vmcnt(0)\n"
    "s_barrier\n"
    "v_mbcnt_lo_u32_b32 v70, -1, 0\n"
    "v_mbcnt_hi_u32_b32 v70, -1, v70\n"
    "s_lshr_b32 s70, %1, 4\n"
    "s_lshl_b32 s70, s70, 3\n"
    "s_and_b32 s71, %1, 7\n"
    "s_or_b32 s70, s70, s71\n"
    "s_bfe_u32 s71, %1, 0x10003\n"
    "s_lshl_b32 s72, %2, 6\n"
    "s_lshl_b32 s73, s71, 8\n"
    "s_add_u32 s73, s73, s72\n"
    "s_mul_i32 s39, s70, 992\n"
    "s_add_u32 s38, s39, s73\n"
    "s_mul_i32 s38, s38, 384\n"
    "s_mov_b64 s[28:29], s[4:5]\n"
    "s_add_u32 s28, s28, s38\n"
    "s_addc_u32 s29, s29, 0\n"
    "s_sub_u32 s30, 496, s73\n"
    "s_mul_i32 s30, s30, 384\n"
    "s_mov_b32 s31, 0x20000\n"
    "v_lshrrev_b32 v71, 3, v70\n"
    "v_mul_u32_u24 v72, 0x180, v71\n"
    "v_and_b32 v73, 7, v70\n"
    "v_lshrrev_b32 v74, 4, v70\n"
    "v_xor_b32 v74, v73, v74\n"
    "v_lshl_add_u32 v228, v74, 4, v72\n"
    "v_xor_b32 v74, 4, v74\n"
    "v_lshl_add_u32 v229, v74, 4, v72\n"
    "v_add_u32 v229, 0x800, v229\n"
    "s_lshl_b32 s75, %2, 14\n"
    "s_add_u32 s75, s75, 0x18000\n"
    "s_add_u32 s48, s75, %3\n"
    "s_movk_i32 s42, 0x1800\n"
    "s_movk_i32 s43, 0x3000\n"
    "s_movk_i32 s44, 0x4800\n"
    "s_mul_i32 s45, s70, 393216\n"
    "s_mov_b64 s[20:21], s[6:7]\n"
    "s_add_u32 s20, s20, s45\n"
    "s_addc_u32 s21, s21, 0\n"
    "s_mov_b32 s22, 393216\n"
    "s_mov_b32 s23, 0x20000\n"
    "s_mul_i32 s36, %2, 6144\n"
    "s_lshl_b32 s52, %2, 11\n"
    "s_add_u32 s52, s52, %3\n"
    "s_add_u32 s53, s52, 8064\n"
    "s_add_u32 s54, s52, 16128\n"
    "s_add_u32 m0, s48, 0\n"
    "s_nop 0\n"
    "buffer_load_dwordx4 v228, s[28:31], 0 offen offset:0 nt lds\n"
    "buffer_load_dwordx4 v229, s[28:31], 0 offen offset:1024 nt lds\n"
    "s_add_u32 m0, s48, 2048\n"
    "s_nop 0\n"
    "buffer_load_dwordx4 v228, s[28:31], s42 offen offset:0 nt lds\n"
    "buffer_load_dwordx4 v229, s[28:31], s42 offen offset:1024 nt lds\n"
    "s_add_u32 m0, s48, 4096\n"
    "s_nop 0\n"
    "buffer_load_dwordx4 v228, s[28:31], s43 offen offset:0 nt lds\n"
    "buffer_load_dwordx4 v229, s[28:31], s43 offen offset:1024 nt lds\n"
    "s_add_u32 m0, s48, 6144\n"
    "s_nop 0\n"
    "buffer_load_dwordx4 v228, s[28:31], s44 offen offset:0 nt lds\n"
    "buffer_load_dwordx4 v229, s[28:31], s44 offen offset:1024 nt lds\n"
    "s_mov_b32 m0, s52\n"
    "s_nop 0\n"
    "buffer_load_dwordx4 v228, s[20:23], s36 offen offset:0 nt lds\n"
    "buffer_load_dwordx4 v229, s[20:23], s36 offen offset:1024 nt lds\n"
    "s_add_u32 m0, s48, 8064\n"
    "s_nop 0\n"
    "buffer_load_dwordx4 v228, s[28:31], 0 offen offset:128 nt lds\n"
    "buffer_load_dwordx4 v229, s[28:31], 0 offen offset:1152 nt lds\n"
    "s_add_u32 m0, s48, 10112\n"
    "s_nop 0\n"
    "buffer_load_dwordx4 v228, s[28:31], s42 offen offset:128 nt lds\n"
    "buffer_load_dwordx4 v229, s[28:31], s42 offen offset:1152 nt lds\n"
    "s_add_u32 m0, s48, 12160\n"
    "s_nop 0\n"
    "buffer_load_dwordx4 v228, s[28:31], s43 offen offset:128 nt lds\n"
    "buffer_load_dwordx4 v229, s[28:31], s43 offen offset:1152 nt lds\n"
    "s_add_u32 m0, s48, 14208\n"
    "s_nop 0\n"
    "buffer_load_dwordx4 v228, s[28:31], s44 offen offset:128 nt lds\n"
    "buffer_load_dwordx4 v229, s[28:31], s44 offen offset:1152 nt lds\n"
    "s_mov_b32 m0, s53\n"
    "s_nop 0\n"
    "buffer_load_dwordx4 v228, s[20:23], s36 offen offset:128 nt lds\n"
    "buffer_load_dwordx4 v229, s[20:23], s36 offen offset:1152 nt lds\n"
    "s_mov_b32 m0, s54\n"
    "s_nop 0\n"
    "buffer_load_dwordx4 v228, s[20:23], s36 offen offset:256 nt lds\n"
    "buffer_load_dwordx4 v229, s[20:23], s36 offen offset:1280 nt lds\n"
    "s_add_u32 s36, s36, 24576\n"
    "s_mov_b64 s[24:25], s[8:9]\n"
    "s_add_u32 s24, s24, s45\n"
    "s_addc_u32 s25, s25, 0\n"
    "s_mov_b32 s26, 393216\n"
    "s_mov_b32 s27, 0x20000\n"
    "s_mov_b64 s[80:81], s[10:11]\n"
    "s_add_u32 s80, s80, s38\n"
    "s_addc_u32 s81, s81, 0\n"
    "s_mov_b32 s82, s30\n"
    "s_mov_b32 s83, 0x20000\n"
    "s_xor_b32 s73, s73, 0x100\n"
    "s_add_u32 s38, s39, s73\n"
    "s_add_u32 s38, s38, 496\n"
    "s_mul_i32 s38, s38, 384\n"
    "s_mov_b64 s[76:77], s[4:5]\n"
    "s_add_u32 s67, s76, s38\n"
    "s_addc_u32 s68, s77, 0\n"
    "s_sub_u32 s69, 496, s73\n"
    "s_mul_i32 s69, s69, 384\n"
    "s_mov_b64 s[76:77], s[10:11]\n"
    "s_add_u32 s64, s76, s38\n"
    "s_addc_u32 s65, s77, 0\n"
    "s_mov_b32 s66, s69\n"
    "s_mul_i32 s37, %2, 6144\n"
    "s_add_u32 s58, s37, %3\n"
    "s_add_u32 s58, s58, 0xc000\n"
    "s_add_u32 s59, s58, 896\n"
    "s_add_u32 s60, s58, 1792\n"
    "s_add_u32 s55, s52, 24576\n"
    "s_add_u32 s61, s58, 24576\n"
    "s_add_u32 s56, s53, 24576\n"
    "s_add_u32 s62, s59, 24576\n"
    "s_add_u32 s57, s54, 24576\n"
    "s_add_u32 s63, s60, 24576\n"
    "s_mov_b32 s40, 0x3dd53b95\n"
    "s_mov_b32 s47, 0xbdd53b95\n"
    "s_mov_b32 s41, 0x41000000\n"
    "s_mov_b64 s[50:51], 0\n"
    "s_mov_b32 s46, 0\n"
    "s_mov_b32 s49, s48\n"
    "s_mov_b64 s[84:85], s[10:11]\n"
    "s_mov_b32 s86, 0\n"
    "s_mov_b32 s87, 0x20000\n"
    "v_xor_b32 v74, v73, v71\n"
    "v_lshl_add_u32 v250, v74, 4, v72\n"
    "v_and_b32 v71, 15, v70\n"
    "v_lshlrev_b32 v71, 7, v71\n"
    "v_bfe_u32 v72, v70, 1, 3\n"
    "v_lshrrev_b32 v74, 4, v70\n"
    "v_xor_b32 v72, v72, v74\n"
    "v_xor_b32 v74, 0, v72\n"
    "v_lshl_add_u32 v74, v74, 4, v71\n"
    "v_add_u32 v224, %3, v74\n"
    "v_add_u32 v253, s48, v74\n"
    "v_xor_b32 v74, 4, v72\n"
    "v_lshl_add_u32 v74, v74, 4, v71\n"
    "v_add_u32 v225, %3, v74\n"
    "v_add_u32 v254, s48, v74\n"
    "v_lshrrev_b32 v72, 5, v70\n"
    "v_xor_b32 v74, v73, v72\n"
    "v_lshl_add_u32 v71, v74, 4, v71\n"
    "v_bfe_u32 v74, v70, 4, 1\n"
    "v_lshl_add_u32 v71, v74, 3, v71\n"
    "v_add_u32 v248, s48, v71\n"
    "v_bfe_u32 v71, v70, 2, 3\n"
    "v_mul_u32_u24 v230, 0x180, v71\n"
    "v_lshl_add_u32 v230, v72, 6, v230\n"
    "v_and_b32 v73, 3, v70\n"
    "v_lshlrev_b32 v231, 1, v74\n"
    "v_xor_b32 v231, v231, v73\n"
    "v_lshl_add_u32 v230, v231, 4, v230\n"
    "v_mul_u32_u24 v231, 0xc00, v72\n"
    "v_lshl_add_u32 v231, v71, 6, v231\n"
    "v_lshl_add_u32 v231, v74, 5, v231\n"
    "v_lshl_add_u32 v231, v73, 3, v231\n"
    "s_add_u32 s70, %3, 0xc000\n"
    "v_add_u32 v226, s70, v231\n"
    "v_xor_b32 v231, 32, v231\n"
    "v_add_u32 v227, s70, v231\n"
    "v_lshlrev_b32 v74, 4, v70\n"
    "v_add_u32 v249, s48, v74\n"
    "v_mov_b32 v240, 0x3f803f80\n"
    "v_mov_b32 v241, 0x3f803f80\n"
    "v_mov_b32 v242, 0x3f803f80\n"
    "v_mov_b32 v243, 0x3f803f80\n"
    "s_waitcnt vmcnt(12)\n"
    "s_barrier\n"
    "ds_read_b128 a[192:195], v224 offset:0\n"
    "ds_read_b128 a[196:199], v225 offset:0\n"
    "ds_read_b128 a[0:3], v253 offset:0\n"
    "ds_read_b128 a[4:7], v254 offset:0\n"
    "ds_read_b128 a[24:27], v253 offset:2048\n"
    "ds_read_b128 a[28:31], v254 offset:2048\n"
    "s_waitcnt lgkmcnt(0)\n"
    "v_mfma_f32_16x16x32_bf16 v[0:3], a[192:195], a[0:3], 0\n"
    "ds_read_b128 a[48:51], v253 offset:4096\n"
    "ds_read_b128 a[52:55], v254 offset:4096\n"
    "v_mfma_f32_16x16x32_bf16 v[0:3], a[196:199], a[4:7], v[0:3]\n"
    "ds_read_b128 a[72:75], v253 offset:6144\n"
    "ds_read_b128 a[76:79], v254 offset:6144\n"
    "v_mfma_f32_16x16x32_bf16 v[16:19], a[192:195], a[24:27], 0\n"
    "ds_read_b128 a[200:203], v224 offset:2048\n"
    "ds_read_b128 a[204:207], v225 offset:2048\n"
    "v_mfma_f32_16x16x32_bf16 v[16:19], a[196:199], a[28:31], v[16:19]\n"
    "ds_read_b128 a[208:211], v224 offset:4096\n"
    "ds_read_b128 a[212:215], v225 offset:4096\n"
    "s_waitcnt lgkmcnt(4)\n"
    "v_mfma_f32_16x16x32_bf16 v[32:35], a[192:195], a[48:51], 0\n"
    "ds_read_b128 a[216:219], v224 offset:6144\n"
    "ds_read_b128 a[220:223], v225 offset:6144\n"
    "v_mfma_f32_16x16x32_bf16 v[32:35], a[196:199], a[52:55], v[32:35]\n"
    "v_mfma_f32_16x16x32_bf16 v[48:51], a[192:195], a[72:75], 0\n"
    "v_mfma_f32_16x16x32_bf16 v[48:51], a[196:199], a[76:79], v[48:51]\n"
    "s_waitcnt lgkmcnt(4)\n"
    "v_mfma_f32_16x16x32_bf16 v[4:7], a[200:203], a[0:3], 0\n"
    "v_mfma_f32_16x16x32_bf16 v[4:7], a[204:207], a[4:7], v[4:7]\n"
    "v_mfma_f32_16x16x32_bf16 v[20:23], a[200:203], a[24:27], 0\n"
    "v_mfma_f32_16x16x32_bf16 v[20:23], a[204:207], a[28:31], v[20:23]\n"
    "v_mfma_f32_16x16x32_bf16 v[36:39], a[200:203], a[48:51], 0\n"
    "v_mfma_f32_16x16x32_bf16 v[36:39], a[204:207], a[52:55], v[36:39]\n"
    "v_mfma_f32_16x16x32_bf16 v[52:55], a[200:203], a[72:75], 0\n"
    "v_mfma_f32_16x16x32_bf16 v[52:55], a[204:207], a[76:79], v[52:55]\n"
    "s_waitcnt lgkmcnt(0)\n"
    "s_add_u32 m0, s48, -256\n"
    "s_nop 0\n"
    "buffer_load_dwordx4 v228, s[28:31], 0 offen offset:256 nt lds\n"
    "buffer_load_dwordx4 v229, s[28:31], 0 offen offset:1280 nt lds\n"
    "s_add_u32 m0, s48, 1792\n"
    "s_nop 0\n"
    "buffer_load_dwordx4 v228, s[28:31], s42 offen offset:256 nt lds\n"
    "buffer_load_dwordx4 v229, s[28:31], s42 offen offset:1280 nt lds\n"
    "v_mfma_f32_16x16x32_bf16 v[8:11], a[208:211], a[0:3], 0\n"
    "s_add_u32 m0, s48, 3840\n"
    "s_nop 0\n"
    "buffer_load_dwordx4 v228, s[28:31], s43 offen offset:256 nt lds\n"
    "buffer_load_dwordx4 v229, s[28:31], s43 offen offset:1280 nt lds\n"
    "s_add_u32 m0, s48, 5888\n"
    "s_nop 0\n"
    "buffer_load_dwordx4 v228, s[28:31], s44 offen offset:256 nt lds\n"
    "v_mfma_f32_16x16x32_bf16 v[8:11], a[212:215], a[4:7], v[8:11]\n"
    "buffer_load_dwordx4 v229, s[28:31], s44 offen offset:1280 nt lds\n"
    "s_mov_b32 s28, s67\n"
    "s_mov_b32 s29, s68\n"
    "s_mov_b32 s30, s69\n"
    "s_mov_b32 m0, s55\n"
    "s_nop 0\n"
    "buffer_load_dwordx4 v228, s[20:23], s36 offen offset:0 nt lds\n"
    "v_mfma_f32_16x16x32_bf16 v[24:27], a[208:211], a[24:27], 0\n"
    "buffer_load_dwordx4 v229, s[20:23], s36 offen offset:1024 nt lds\n"
    "s_mov_b32 m0, s56\n"
    "s_nop 0\n"
    "buffer_load_dwordx4 v228, s[20:23], s36 offen offset:128 nt lds\n"
    "buffer_load_dwordx4 v229, s[20:23], s36 offen offset:1152 nt lds\n"
    "s_mov_b32 m0, s57\n"
    "s_nop 0\n"
    "v_mfma_f32_16x16x32_bf16 v[24:27], a[212:215], a[28:31], v[24:27]\n"
    "buffer_load_dwordx4 v228, s[20:23], s36 offen offset:256 nt lds\n"
    "buffer_load_dwordx4 v229, s[20:23], s36 offen offset:1280 nt lds\n"
    "s_add_u32 s36, s36, 24576\n"
    "s_mov_b32 m0, s58\n"
    "s_nop 0\n"
    "buffer_load_dwordx4 v230, s[24:27], s37 offen offset:0 nt lds\n"
    "buffer_load_dwordx4 v230, s[24:27], s37 offen offset:3072 nt lds\n"
    "s_mov_b32 m0, s59\n"
    "v_mfma_f32_16x16x32_bf16 v[40:43], a[208:211], a[48:51], 0\n"
    "s_nop 0\n"
    "buffer_load_dwordx4 v230, s[24:27], s37 offen offset:128 nt lds\n"
    "buffer_load_dwordx4 v230, s[24:27], s37 offen offset:3200 nt lds\n"
    "s_mov_b32 m0, s60\n"
    "s_nop 0\n"
    "buffer_load_dwordx4 v230, s[24:27], s37 offen offset:256 nt lds\n"
    "buffer_load_dwordx4 v230, s[24:27], s37 offen offset:3328 nt lds\n"
    "v_mfma_f32_16x16x32_bf16 v[40:43], a[212:215], a[52:55], v[40:43]\n"
    "s_add_u32 s37, s37, 24576\n"
    "s_mov_b32 m0, s61\n"
    "s_nop 0\n"
    "buffer_load_dwordx4 v230, s[24:27], s37 offen offset:0 nt lds\n"
    "buffer_load_dwordx4 v230, s[24:27], s37 offen offset:3072 nt lds\n"
    "s_mov_b32 m0, s62\n"
    "s_nop 0\n"
    "v_mfma_f32_16x16x32_bf16 v[56:59], a[208:211], a[72:75], 0\n"
    "buffer_load_dwordx4 v230, s[24:27], s37 offen offset:128 nt lds\n"
    "buffer_load_dwordx4 v230, s[24:27], s37 offen offset:3200 nt lds\n"
    "s_mov_b32 m0, s63\n"
    "s_nop 0\n"
    "buffer_load_dwordx4 v230, s[24:27], s37 offen offset:256 nt lds\n"
    "buffer_load_dwordx4 v230, s[24:27], s37 offen offset:3328 nt lds\n"
    "s_add_u32 s37, s37, 24576\n"
    "v_mfma_f32_16x16x32_bf16 v[56:59], a[212:215], a[76:79], v[56:59]\n"
    "v_mfma_f32_16x16x32_bf16 v[12:15], a[216:219], a[0:3], 0\n"
    "s_waitcnt vmcnt(28)\n"
    "s_barrier\n"
    "ds_read_b128 a[224:227], v224 offset:8192\n"
    "ds_read_b128 a[228:231], v225 offset:8192\n"
    "v_mfma_f32_16x16x32_bf16 v[12:15], a[220:223], a[4:7], v[12:15]\n"
    "ds_read_b128 a[8:11], v253 offset:8192\n"
    "v_mfma_f32_16x16x32_bf16 v[28:31], a[216:219], a[24:27], 0\n"
    "ds_read_b128 a[12:15], v254 offset:8192\n"
    "v_mfma_f32_16x16x32_bf16 v[28:31], a[220:223], a[28:31], v[28:31]\n"
    "ds_read_b128 a[32:35], v253 offset:10240\n"
    "ds_read_b128 a[36:39], v254 offset:10240\n"
    "v_mfma_f32_16x16x32_bf16 v[44:47], a[216:219], a[48:51], 0\n"
    "ds_read_b128 a[56:59], v253 offset:12288\n"
    "v_mfma_f32_16x16x32_bf16 v[44:47], a[220:223], a[52:55], v[44:47]\n"
    "ds_read_b128 a[60:63], v254 offset:12288\n"
    "v_mfma_f32_16x16x32_bf16 v[60:63], a[216:219], a[72:75], 0\n"
    "ds_read_b128 a[80:83], v253 offset:14336\n"
    "ds_read_b128 a[84:87], v254 offset:14336\n"
    "v_mfma_f32_16x16x32_bf16 v[60:63], a[220:223], a[76:79], v[60:63]\n"
    "ds_read_b128 a[232:235], v224 offset:10240\n"
    "s_waitcnt lgkmcnt(5)\n"
    "v_mfma_f32_16x16x32_bf16 v[0:3], a[224:227], a[8:11], v[0:3]\n"
    "ds_read_b128 a[236:239], v225 offset:10240\n"
    "v_mfma_f32_16x16x32_bf16 v[0:3], a[228:231], a[12:15], v[0:3]\n"
    "ds_read_b128 a[192:195], v224 offset:12288\n"
    "ds_read_b128 a[196:199], v225 offset:12288\n"
    "v_mfma_f32_16x16x32_bf16 v[16:19], a[224:227], a[32:35], v[16:19]\n"
    "ds_read_b128 a[200:203], v224 offset:14336\n"
    "v_mfma_f32_16x16x32_bf16 v[16:19], a[228:231], a[36:39], v[16:19]\n"
    "ds_read_b128 a[204:207], v225 offset:14336\n"
    "s_waitcnt lgkmcnt(6)\n"
    "v_mfma_f32_16x16x32_bf16 v[32:35], a[224:227], a[56:59], v[32:35]\n"
    "v_mfma_f32_16x16x32_bf16 v[32:35], a[228:231], a[60:63], v[32:35]\n"
    "v_mfma_f32_16x16x32_bf16 v[48:51], a[224:227], a[80:83], v[48:51]\n"
    "v_mfma_f32_16x16x32_bf16 v[48:51], a[228:231], a[84:87], v[48:51]\n"
    "s_waitcnt lgkmcnt(4)\n"
    "v_mfma_f32_16x16x32_bf16 v[4:7], a[232:235], a[8:11], v[4:7]\n"
    "v_mfma_f32_16x16x32_bf16 v[4:7], a[236:239], a[12:15], v[4:7]\n"
    "v_mfma_f32_16x16x32_bf16 v[20:23], a[232:235], a[32:35], v[20:23]\n"
    "v_mfma_f32_16x16x32_bf16 v[20:23], a[236:239], a[36:39], v[20:23]\n"
    "v_mfma_f32_16x16x32_bf16 v[36:39], a[232:235], a[56:59], v[36:39]\n"
    "v_mfma_f32_16x16x32_bf16 v[36:39], a[236:239], a[60:63], v[36:39]\n"
    "v_mfma_f32_16x16x32_bf16 v[52:55], a[232:235], a[80:83], v[52:55]\n"
    "v_mfma_f32_16x16x32_bf16 v[52:55], a[236:239], a[84:87], v[52:55]\n"
    "s_waitcnt lgkmcnt(0)\n"
    "v_mfma_f32_16x16x32_bf16 v[8:11], a[192:195], a[8:11], v[8:11]\n"
    "v_mfma_f32_16x16x32_bf16 v[8:11], a[196:199], a[12:15], v[8:11]\n"
    "v_mfma_f32_16x16x32_bf16 v[24:27], a[192:195], a[32:35], v[24:27]\n"
    "v_mfma_f32_16x16x32_bf16 v[24:27], a[196:199], a[36:39], v[24:27]\n"
    "v_mfma_f32_16x16x32_bf16 v[40:43], a[192:195], a[56:59], v[40:43]\n"
    "v_mfma_f32_16x16x32_bf16 v[40:43], a[196:199], a[60:63], v[40:43]\n"
    "v_mfma_f32_16x16x32_bf16 v[56:59], a[192:195], a[80:83], v[56:59]\n"
    "v_mfma_f32_16x16x32_bf16 v[56:59], a[196:199], a[84:87], v[56:59]\n"
    "v_mfma_f32_16x16x32_bf16 v[12:15], a[200:203], a[8:11], v[12:15]\n"
    "s_waitcnt vmcnt(18)\n"
    "s_barrier\n"
    "ds_read_b128 a[208:211], v224 offset:16384\n"
    "ds_read_b128 a[212:215], v225 offset:16384\n"
    "v_mfma_f32_16x16x32_bf16 v[12:15], a[204:207], a[12:15], v[12:15]\n"
    "ds_read_b128 a[16:19], v253 offset:0\n"
    "v_mfma_f32_16x16x32_bf16 v[28:31], a[200:203], a[32:35], v[28:31]\n"
    "ds_read_b128 a[20:23], v254 offset:0\n"
    "v_mfma_f32_16x16x32_bf16 v[28:31], a[204:207], a[36:39], v[28:31]\n"
    "ds_read_b128 a[40:43], v253 offset:2048\n"
    "ds_read_b128 a[44:47], v254 offset:2048\n"
    "v_mfma_f32_16x16x32_bf16 v[44:47], a[200:203], a[56:59], v[44:47]\n"
    "ds_read_b128 a[64:67], v253 offset:4096\n"
    "v_mfma_f32_16x16x32_bf16 v[44:47], a[204:207], a[60:63], v[44:47]\n"
    "ds_read_b128 a[68:71], v254 offset:4096\n"
    "v_mfma_f32_16x16x32_bf16 v[60:63], a[200:203], a[80:83], v[60:63]\n"
    "ds_read_b128 a[88:91], v253 offset:6144\n"
    "ds_read_b128 a[92:95], v254 offset:6144\n"
    "v_mfma_f32_16x16x32_bf16 v[60:63], a[204:207], a[84:87], v[60:63]\n"
    "ds_read_b128 a[216:219], v224 offset:18432\n"
    "s_waitcnt lgkmcnt(5)\n"
    "v_mfma_f32_16x16x32_bf16 v[0:3], a[208:211], a[16:19], v[0:3]\n"
    "ds_read_b128 a[220:223], v225 offset:18432\n"
    "v_mfma_f32_16x16x32_bf16 v[0:3], a[212:215], a[20:23], v[0:3]\n"
    "ds_read_b128 a[224:227], v224 offset:20480\n"
    "ds_read_b128 a[228:231], v225 offset:20480\n"
    "v_mfma_f32_16x16x32_bf16 v[16:19], a[208:211], a[40:43], v[16:19]\n"
    "ds_read_b128 a[232:235], v224 offset:22528\n"
    "v_mfma_f32_16x16x32_bf16 v[16:19], a[212:215], a[44:47], v[16:19]\n"
    "ds_read_b128 a[236:239], v225 offset:22528\n"
    "s_waitcnt lgkmcnt(6)\n"
    "v_mfma_f32_16x16x32_bf16 v[32:35], a[208:211], a[64:67], v[32:35]\n"
    "v_mfma_f32_16x16x32_bf16 v[32:35], a[212:215], a[68:71], v[32:35]\n"
    "v_mfma_f32_16x16x32_bf16 v[48:51], a[208:211], a[88:91], v[48:51]\n"
    "v_mfma_f32_16x16x32_bf16 v[48:51], a[212:215], a[92:95], v[48:51]\n"
    "s_waitcnt lgkmcnt(4)\n"
    "v_mfma_f32_16x16x32_bf16 v[4:7], a[216:219], a[16:19], v[4:7]\n"
    "v_mfma_f32_16x16x32_bf16 v[4:7], a[220:223], a[20:23], v[4:7]\n"
    "v_mfma_f32_16x16x32_bf16 v[20:23], a[216:219], a[40:43], v[20:23]\n"
    "v_mfma_f32_16x16x32_bf16 v[20:23], a[220:223], a[44:47], v[20:23]\n"
    "v_mfma_f32_16x16x32_bf16 v[36:39], a[216:219], a[64:67], v[36:39]\n"
    "v_mfma_f32_16x16x32_bf16 v[36:39], a[220:223], a[68:71], v[36:39]\n"
    "v_mfma_f32_16x16x32_bf16 v[52:55], a[216:219], a[88:91], v[52:55]\n"
    "v_mfma_f32_16x16x32_bf16 v[52:55], a[220:223], a[92:95], v[52:55]\n"
    "s_waitcnt lgkmcnt(3)\n"
    "v_mfma_f32_16x16x32_bf16 v[8:11], a[224:227], a[16:19], v[8:11]\n"
    "s_waitcnt vmcnt(12) lgkmcnt(0)\n"
    "s_barrier\n"
    "ds_read_b128 a[192:195], v224 offset:24576\n"
    "v_mfma_f32_16x16x32_bf16 v[8:11], a[228:231], a[20:23], v[8:11]\n"
    "ds_read_b128 a[196:199], v225 offset:24576\n"
    "v_mfma_f32_16x16x32_bf16 v[24:27], a[224:227], a[40:43], v[24:27]\n"
    "ds_read_b128 a[200:203], v224 offset:32768\n"
    "v_mfma_f32_16x16x32_bf16 v[24:27], a[228:231], a[44:47], v[24:27]\n"
    "ds_read_b128 a[204:207], v225 offset:32768\n"
    "v_mfma_f32_16x16x32_bf16 v[40:43], a[224:227], a[64:67], v[40:43]\n"
    "ds_read_b128 a[208:211], v224 offset:40960\n"
    "v_mfma_f32_16x16x32_bf16 v[40:43], a[228:231], a[68:71], v[40:43]\n"
    "ds_read_b128 a[212:215], v225 offset:40960\n"
    "v_mfma_f32_16x16x32_bf16 v[56:59], a[224:227], a[88:91], v[56:59]\n"
    "ds_read_b128 a[216:219], v224 offset:26624\n"
    "v_mfma_f32_16x16x32_bf16 v[56:59], a[228:231], a[92:95], v[56:59]\n"
    "ds_read_b128 a[220:223], v225 offset:26624\n"
    "v_mfma_f32_16x16x32_bf16 v[12:15], a[232:235], a[16:19], v[12:15]\n"
    "ds_read_b128 a[224:227], v224 offset:34816\n"
    "v_mfma_f32_16x16x32_bf16 v[12:15], a[236:239], a[20:23], v[12:15]\n"
    "ds_read_b128 a[228:231], v225 offset:34816\n"
    "v_mfma_f32_16x16x32_bf16 v[28:31], a[232:235], a[40:43], v[28:31]\n"
    "v_mfma_f32_16x16x32_bf16 v[28:31], a[236:239], a[44:47], v[28:31]\n"
    "v_mfma_f32_16x16x32_bf16 v[44:47], a[232:235], a[64:67], v[44:47]\n"
    "v_mfma_f32_16x16x32_bf16 v[44:47], a[236:239], a[68:71], v[44:47]\n"
    "v_mfma_f32_16x16x32_bf16 v[60:63], a[232:235], a[88:91], v[60:63]\n"
    "s_waitcnt lgkmcnt(4)\n"
    "v_mfma_f32_16x16x32_bf16 v[60:63], a[236:239], a[92:95], v[60:63]\n"
    "s_branch LSITEM_%=\n"
    "LSA0X_%=:\n"
    "v_mfma_f32_16x16x32_bf16 v[0:3], a[192:195], a[0:3], 0\n"
    "v_mul_f32 v102, v102, v238\n"
    "v_mul_f32 v110, v110, v239\n"
    "v_mul_f32 v103, v103, v238\n"
    "v_mul_f32 v111, v111, v239\n"
    "v_cvt_pk_bf16_f32 v100, v100, v101\n"
    "v_mfma_f32_16x16x32_bf16 v[0:3], a[196:199], a[4:7], v[0:3]\n"
    "s_mov_b32 m0, s61\n"
    "v_cvt_pk_bf16_f32 v108, v108, v109\n"
    "v_cvt_pk_bf16_f32 v101, v102, v103\n"
    "v_cvt_pk_bf16_f32 v109, v110, v111\n"
    "v_mul_f32 v140, v140, v236\n"
    "v_mfma_f32_16x16x32_bf16 v[16:19], a[192:195], a[24:27], 0\n"
    "ds_read_b128 a[224:227], v224 offset:8192\n"
    "ds_write_b64 v246, v[100:101] offset:12288\n"
    "v_mul_f32 v188, v188, v237\n"
    "v_mfma_f32_16x16x32_bf16 v[16:19], a[196:199], a[28:31], v[16:19]\n"
    "buffer_load_dwordx4 v230, s[24:27], s37 offen offset:0 nt lds\n"
    "v_mul_f32 v141, v141, v236\n"
    "v_mul_f32 v189, v189, v237\n"
    "v_mul_f32 v142, v142, v236\n"
    "v_mfma_f32_16x16x32_bf16 v[32:35], a[192:195], a[48:51], 0\n"
    "ds_read_b128 a[228:231], v225 offset:8192\n"
    "ds_write_b64 v246, v[108:109] offset:14336\n"
    "v_mul_f32 v190, v190, v237\n"
    "v_mfma_f32_16x16x32_bf16 v[32:35], a[196:199], a[52:55], v[32:35]\n"
    "v_mul_f32 v143, v143, v236\n"
    "v_mul_f32 v191, v191, v237\n"
    "v_cvt_pk_bf16_f32 v140, v140, v141\n"
    "v_cvt_pk_bf16_f32 v188, v188, v189\n"
    "v_cvt_pk_bf16_f32 v141, v142, v143\n"
    "v_mfma_f32_16x16x32_bf16 v[48:51], a[192:195], a[72:75], 0\n"
    "ds_read_b128 a[232:235], v224 offset:10240\n"
    "ds_write_b64 v247, v[140:141] offset:8192\n"
    "v_cvt_pk_bf16_f32 v189, v190, v191\n"
    "v_mfma_f32_16x16x32_bf16 v[48:51], a[196:199], a[76:79], v[48:51]\n"
    "v_accvgpr_read_b32 v116, a108\n"
    "v_accvgpr_read_b32 v124, a156\n"
    "v_accvgpr_read_b32 v117, a109\n"
    "v_accvgpr_read_b32 v125, a157\n"
    "v_accvgpr_read_b32 v118, a110\n"
    "v_accvgpr_read_b32 v126, a158\n"
    "v_mfma_f32_16x16x32_bf16 v[4:7], a[200:203], a[0:3], 0\n"
    "buffer_load_dwordx4 v230, s[24:27], s37 offen offset:3072 nt lds\n"
    "ds_write_b64 v247, v[188:189] offset:10240\n"
    "v_mfma_f32_16x16x32_bf16 v[4:7], a[204:207], a[4:7], v[4:7]\n"
    "ds_read_b128 a[236:239], v225 offset:10240\n"
    "s_waitcnt vmcnt(8)\n"
    "ds_read_b128 a[96:99], v249 offset:8192\n"
    "v_accvgpr_read_b32 v119, a111\n"
    "v_accvgpr_read_b32 v127, a159\n"
    "v_mfma_f32_16x16x32_bf16 v[20:23], a[200:203], a[24:27], 0\n"
    "ds_read_b128 a[16:19], v253 offset:0\n"
    "ds_read_b128 a[100:103], v249 offset:9216\n"
    "v_mul_f32 v116, v116, v238\n"
    "v_mul_f32 v124, v124, v239\n"
    "v_mul_f32 v117, v117, v238\n"
    "v_mfma_f32_16x16x32_bf16 v[20:23], a[204:207], a[28:31], v[20:23]\n"
    "s_mov_b32 m0, s62\n"
    "ds_read_b128 a[20:23], v254 offset:0\n"
    "ds_read_b128 a[104:107], v249 offset:10240\n"
    "v_mul_f32 v125, v125, v239\n"
    "v_mul_f32 v118, v118, v238\n"
    "v_mfma_f32_16x16x32_bf16 v[36:39], a[200:203], a[48:51], 0\n"
    "ds_read_b128 a[192:195], v224 offset:12288\n"
    "ds_read_b128 a[40:43], v253 offset:2048\n"
    "v_mul_f32 v126, v126, v239\n"
    "v_mul_f32 v119, v119, v238\n"
    "v_mul_f32 v127, v127, v239\n"
    "v_mfma_f32_16x16x32_bf16 v[36:39], a[204:207], a[52:55], v[36:39]\n"
    "buffer_load_dwordx4 v230, s[24:27], s37 offen offset:128 nt lds\n"
    "ds_read_b128 a[108:111], v249 offset:11264\n"
    "v_cvt_pk_bf16_f32 v116, v116, v117\n"
    "v_cvt_pk_bf16_f32 v124, v124, v125\n"
    "v_mfma_f32_16x16x32_bf16 v[52:55], a[200:203], a[72:75], 0\n"
    "ds_read_b128 a[44:47], v254 offset:2048\n"
    "ds_read_b128 a[64:67], v253 offset:4096\n"
    "v_cvt_pk_bf16_f32 v117, v118, v119\n"
    "v_cvt_pk_bf16_f32 v125, v126, v127\n"
    "v_mul_f32 v144, v144, v236\n"
    "v_mfma_f32_16x16x32_bf16 v[52:55], a[204:207], a[76:79], v[52:55]\n"
    "ds_read_b128 a[196:199], v225 offset:12288\n"
    "ds_write_b64 v247, v[116:117] offset:12288\n"
    "v_mul_f32 v192, v192, v237\n"
    "v_mul_f32 v145, v145, v236\n"
    "v_mfma_f32_16x16x32_bf16 v[8:11], a[208:211], a[0:3], 0\n"
    "ds_read_b128 a[68:71], v254 offset:4096\n"
    "v_mul_f32 v193, v193, v237\n"
    "v_mul_f32 v146, v146, v236\n"
    "v_mul_f32 v194, v194, v237\n"
    "v_mul_f32 v147, v147, v236\n"
    "v_mfma_f32_16x16x32_bf16 v[8:11], a[212:215], a[4:7], v[8:11]\n"
    "ds_read_b128 a[88:91], v253 offset:6144\n"
    "ds_read_b128 a[92:95], v254 offset:6144\n"
    "v_mul_f32 v195, v195, v237\n"
    "v_cvt_pk_bf16_f32 v144, v144, v145\n"
    "v_cvt_pk_bf16_f32 v192, v192, v193\n"
    "v_mfma_f32_16x16x32_bf16 v[24:27], a[208:211], a[24:27], 0\n"
    "buffer_load_dwordx4 v230, s[24:27], s37 offen offset:3200 nt lds\n"
    "ds_write_b64 v247, v[124:125] offset:14336\n"
    "v_mfma_f32_16x16x32_bf16 v[24:27], a[212:215], a[28:31], v[24:27]\n"
    "ds_read_b128 a[200:203], v224 offset:14336\n"
    "ds_read_b128 a[144:147], v249 offset:12288\n"
    "v_cvt_pk_bf16_f32 v145, v146, v147\n"
    "v_cvt_pk_bf16_f32 v193, v194, v195\n"
    "v_accvgpr_read_b32 v68, a112\n"
    "v_mfma_f32_16x16x32_bf16 v[40:43], a[208:211], a[48:51], 0\n"
    "ds_read_b128 a[148:151], v249 offset:13312\n"
    "ds_read_b128 a[152:155], v249 offset:14336\n"
    "v_accvgpr_read_b32 v76, a160\n"
    "v_accvgpr_read_b32 v69, a113\n"
    "v_accvgpr_read_b32 v77, a161\n"
    "v_mfma_f32_16x16x32_bf16 v[40:43], a[212:215], a[52:55], v[40:43]\n"
    "s_mov_b32 m0, s63\n"
    "ds_read_b128 a[156:159], v249 offset:15360\n"
    "ds_write_b64 v248, v[144:145] offset:8192\n"
    "v_mfma_f32_16x16x32_bf16 v[56:59], a[208:211], a[72:75], 0\n"
    "ds_read_b128 a[204:207], v225 offset:14336\n"
    "v_accvgpr_read_b32 v70, a114\n"
    "v_accvgpr_read_b32 v78, a162\n"
    "v_accvgpr_read_b32 v71, a115\n"
    "v_accvgpr_read_b32 v79, a163\n"
    "v_mul_f32 v68, v68, v238\n"
    "v_mfma_f32_16x16x32_bf16 v[56:59], a[212:215], a[76:79], v[56:59]\n"
    "ds_write_b64 v248, v[192:193] offset:10240\n"
    "s_waitcnt lgkmcnt(15)\n"
    "buffer_store_dwordx4 a[96:99], v250, s[80:83], 0 offen offset:0 nt sc1\n"
    "v_mfma_f32_16x16x32_bf16 v[12:15], a[216:219], a[0:3], 0\n"
    "buffer_load_dwordx4 v230, s[24:27], s37 offen offset:256 nt lds\n"
    "v_mul_f32 v76, v76, v239\n"
    "v_mul_f32 v69, v69, v238\n"
    "v_mfma_f32_16x16x32_bf16 v[12:15], a[220:223], a[4:7], v[12:15]\n"
    "v_mul_f32 v77, v77, v239\n"
    "v_mul_f32 v70, v70, v238\n"
    "v_mul_f32 v78, v78, v239\n"
    "v_mul_f32 v71, v71, v238\n"
    "v_mul_f32 v79, v79, v239\n"
    "v_mfma_f32_16x16x32_bf16 v[28:31], a[216:219], a[24:27], 0\n"
    "ds_read_b128 a[208:211], v224 offset:16384\n"
    "v_cvt_pk_bf16_f32 v68, v68, v69\n"
    "v_cvt_pk_bf16_f32 v76, v76, v77\n"
    "v_cvt_pk_bf16_f32 v69, v70, v71\n"
    "v_cvt_pk_bf16_f32 v77, v78, v79\n"
    "v_mfma_f32_16x16x32_bf16 v[28:31], a[220:223], a[28:31], v[28:31]\n"
    "ds_write_b64 v248, v[68:69] offset:12288\n"
    "v_mul_f32 v148, v148, v236\n"
    "v_mul_f32 v196, v196, v237\n"
    "v_mfma_f32_16x16x32_bf16 v[44:47], a[216:219], a[48:51], 0\n"
    "v_mul_f32 v149, v149, v236\n"
    "v_mul_f32 v197, v197, v237\n"
    "v_mul_f32 v150, v150, v236\n"
    "v_mul_f32 v198, v198, v237\n"
    "v_mul_f32 v151, v151, v236\n"
    "v_mfma_f32_16x16x32_bf16 v[44:47], a[220:223], a[52:55], v[44:47]\n"
    "buffer_load_dwordx4 v230, s[24:27], s37 offen offset:3328 nt lds\n"
    "ds_write_b64 v248, v[76:77] offset:14336\n"
    "v_mfma_f32_16x16x32_bf16 v[60:63], a[216:219], a[72:75], 0\n"
    "s_add_u32 s37, s37, 24576\n"
    "v_mul_f32 v199, v199, v237\n"
    "v_cvt_pk_bf16_f32 v148, v148, v149\n"
    "v_cvt_pk_bf16_f32 v196, v196, v197\n"
    "v_cvt_pk_bf16_f32 v149, v150, v151\n"
    "v_cvt_pk_bf16_f32 v197, v198, v199\n"
    "v_mfma_f32_16x16x32_bf16 v[60:63], a[220:223], a[76:79], v[60:63]\n"
    "ds_read_b128 a[212:215], v225 offset:16384\n"
    "s_waitcnt lgkmcnt(15)\n"
    "ds_write_b64 v245, v[148:149] offset:8192\n"
    "v_accvgpr_read_b32 v84, a116\n"
    "v_mfma_f32_16x16x32_bf16 v[0:3], a[224:227], a[8:11], v[0:3]\n"
    "v_accvgpr_read_b32 v92, a164\n"
    "v_accvgpr_read_b32 v85, a117\n"
    "v_accvgpr_read_b32 v93, a165\n"
    "v_accvgpr_read_b32 v86, a118\n"
    "v_accvgpr_read_b32 v94, a166\n"
    "v_mfma_f32_16x16x32_bf16 v[0:3], a[228:231], a[12:15], v[0:3]\n"
    "ds_write_b64 v245, v[196:197] offset:10240\n"
    "v_accvgpr_read_b32 v87, a119\n"
    "v_accvgpr_read_b32 v95, a167\n"
    "v_mfma_f32_16x16x32_bf16 v[16:19], a[224:227], a[32:35], v[16:19]\n"
    "v_mul_f32 v84, v84, v238\n"
    "v_mul_f32 v92, v92, v239\n"
    "v_mul_f32 v85, v85, v238\n"
    "v_mul_f32 v93, v93, v239\n"
    "v_mul_f32 v86, v86, v238\n"
    "v_mfma_f32_16x16x32_bf16 v[16:19], a[228:231], a[36:39], v[16:19]\n"
    "ds_read_b128 a[216:219], v224 offset:18432\n"
    "buffer_store_dwordx4 a[100:103], v250, s[80:83], 0 offen offset:3072 nt sc1\n"
    "v_mul_f32 v94, v94, v239\n"
    "v_mul_f32 v87, v87, v238\n"
    "v_mfma_f32_16x16x32_bf16 v[32:35], a[224:227], a[56:59], v[32:35]\n"
    "v_mul_f32 v95, v95, v239\n"
    "v_cvt_pk_bf16_f32 v84, v84, v85\n"
    "v_cvt_pk_bf16_f32 v92, v92, v93\n"
    "v_cvt_pk_bf16_f32 v85, v86, v87\n"
    "v_cvt_pk_bf16_f32 v93, v94, v95\n"
    "v_mfma_f32_16x16x32_bf16 v[32:35], a[228:231], a[60:63], v[32:35]\n"
    "ds_write_b64 v245, v[84:85] offset:12288\n"
    "v_mul_f32 v152, v152, v236\n"
    "v_mul_f32 v200, v200, v237\n"
    "v_mul_f32 v153, v153, v236\n"
    "v_mfma_f32_16x16x32_bf16 v[48:51], a[224:227], a[80:83], v[48:51]\n"
    "v_mul_f32 v201, v201, v237\n"
    "v_mul_f32 v154, v154, v236\n"
    "v_mul_f32 v202, v202, v237\n"
    "v_mul_f32 v155, v155, v236\n"
    "v_mul_f32 v203, v203, v237\n"
    "v_mfma_f32_16x16x32_bf16 v[48:51], a[228:231], a[84:87], v[48:51]\n"
    "ds_read_b128 a[220:223], v225 offset:18432\n"
    "ds_write_b64 v245, v[92:93] offset:14336\n"
    "v_cvt_pk_bf16_f32 v152, v152, v153\n"
    "v_mfma_f32_16x16x32_bf16 v[4:7], a[232:235], a[8:11], v[4:7]\n"
    "v_cvt_pk_bf16_f32 v200, v200, v201\n"
    "v_cvt_pk_bf16_f32 v153, v154, v155\n"
    "v_cvt_pk_bf16_f32 v201, v202, v203\n"
    "v_accvgpr_read_b32 v100, a120\n"
    "v_accvgpr_read_b32 v108, a168\n"
    "v_mfma_f32_16x16x32_bf16 v[4:7], a[236:239], a[12:15], v[4:7]\n"
    "ds_write_b64 v246, v[152:153] offset:8192\n"
    "v_accvgpr_read_b32 v101, a121\n"
    "v_accvgpr_read_b32 v109, a169\n"
    "v_mfma_f32_16x16x32_bf16 v[20:23], a[232:235], a[32:35], v[20:23]\n"
    "v_accvgpr_read_b32 v102, a122\n"
    "v_accvgpr_read_b32 v110, a170\n"
    "v_accvgpr_read_b32 v103, a123\n"
    "v_accvgpr_read_b32 v111, a171\n"
    "v_mul_f32 v100, v100, v238\n"
    "v_mfma_f32_16x16x32_bf16 v[20:23], a[236:239], a[36:39], v[20:23]\n"
    "ds_read_b128 a[224:227], v224 offset:20480\n"
    "ds_write_b64 v246, v[200:201] offset:10240\n"
    "v_mul_f32 v108, v108, v239\n"
    "v_mfma_f32_16x16x32_bf16 v[36:39], a[232:235], a[56:59], v[36:39]\n"
    "v_mul_f32 v101, v101, v238\n"
    "v_mul_f32 v109, v109, v239\n"
    "v_mul_f32 v102, v102, v238\n"
    "v_mul_f32 v110, v110, v239\n"
    "v_mul_f32 v103, v103, v238\n"
    "v_mfma_f32_16x16x32_bf16 v[36:39], a[236:239], a[60:63], v[36:39]\n"
    "v_mul_f32 v111, v111, v239\n"
    "v_cvt_pk_bf16_f32 v100, v100, v101\n"
    "v_cvt_pk_bf16_f32 v108, v108, v109\n"
    "v_cvt_pk_bf16_f32 v101, v102, v103\n"
    "v_cvt_pk_bf16_f32 v109, v110, v111\n"
    "v_mul_f32 v156, v156, v236\n"
    "v_mfma_f32_16x16x32_bf16 v[52:55], a[232:235], a[80:83], v[52:55]\n"
    "ds_write_b64 v246, v[100:101] offset:12288\n"
    "v_mul_f32 v204, v204, v237\n"
    "v_mul_f32 v157, v157, v236\n"
    "v_mfma_f32_16x16x32_bf16 v[52:55], a[236:239], a[84:87], v[52:55]\n"
    "s_waitcnt lgkmcnt(15)\n"
    "buffer_store_dwordx4 a[104:107], v250, s[80:83], s42 offen offset:0 nt sc1\n"
    "v_mul_f32 v205, v205, v237\n"
    "v_mul_f32 v158, v158, v236\n"
    "v_mfma_f32_16x16x32_bf16 v[8:11], a[192:195], a[8:11], v[8:11]\n"
    "ds_read_b128 a[228:231], v225 offset:20480\n"
    "ds_write_b64 v246, v[108:109] offset:14336\n"
    "v_mul_f32 v206, v206, v237\n"
    "v_mfma_f32_16x16x32_bf16 v[8:11], a[196:199], a[12:15], v[8:11]\n"
    "v_mul_f32 v159, v159, v236\n"
    "v_mul_f32 v207, v207, v237\n"
    "v_cvt_pk_bf16_f32 v156, v156, v157\n"
    "v_cvt_pk_bf16_f32 v204, v204, v205\n"
    "v_cvt_pk_bf16_f32 v157, v158, v159\n"
    "v_mfma_f32_16x16x32_bf16 v[24:27], a[192:195], a[32:35], v[24:27]\n"
    "ds_write_b64 v247, v[156:157] offset:8192\n"
    "v_cvt_pk_bf16_f32 v205, v206, v207\n"
    "v_accvgpr_read_b32 v116, a124\n"
    "v_mfma_f32_16x16x32_bf16 v[24:27], a[196:199], a[36:39], v[24:27]\n"
    "v_accvgpr_read_b32 v124, a172\n"
    "v_accvgpr_read_b32 v117, a125\n"
    "v_accvgpr_read_b32 v125, a173\n"
    "v_accvgpr_read_b32 v118, a126\n"
    "v_accvgpr_read_b32 v126, a174\n"
    "v_mfma_f32_16x16x32_bf16 v[40:43], a[192:195], a[56:59], v[40:43]\n"
    "ds_write_b64 v247, v[204:205] offset:10240\n"
    "v_accvgpr_read_b32 v119, a127\n"
    "v_accvgpr_read_b32 v127, a175\n"
    "v_mul_f32 v116, v116, v238\n"
    "v_mfma_f32_16x16x32_bf16 v[40:43], a[196:199], a[60:63], v[40:43]\n"
    "ds_read_b128 a[232:235], v224 offset:22528\n"
    "v_mul_f32 v124, v124, v239\n"
    "v_mul_f32 v117, v117, v238\n"
    "v_mul_f32 v125, v125, v239\n"
    "v_mul_f32 v118, v118, v238\n"
    "v_mfma_f32_16x16x32_bf16 v[56:59], a[192:195], a[80:83], v[56:59]\n"
    "v_mul_f32 v126, v126, v239\n"
    "v_mul_f32 v119, v119, v238\n"
    "v_mul_f32 v127, v127, v239\n"
    "v_cvt_pk_bf16_f32 v116, v116, v117\n"
    "v_cvt_pk_bf16_f32 v124, v124, v125\n"
    "v_mfma_f32_16x16x32_bf16 v[56:59], a[196:199], a[84:87], v[56:59]\n"
    "v_cvt_pk_bf16_f32 v117, v118, v119\n"
    "v_cvt_pk_bf16_f32 v125, v126, v127\n"
    "v_mul_f32 v160, v160, v236\n"
    "v_mul_f32 v208, v208, v237\n"
    "v_mul_f32 v161, v161, v236\n"
    "v_mfma_f32_16x16x32_bf16 v[12:15], a[200:203], a[8:11], v[12:15]\n"
    "ds_write_b64 v247, v[116:117] offset:12288\n"
    "v_mul_f32 v209, v209, v237\n"
    "v_mul_f32 v162, v162, v236\n"
    "v_mfma_f32_16x16x32_bf16 v[12:15], a[204:207], a[12:15], v[12:15]\n"
    "v_mul_f32 v210, v210, v237\n"
    "v_mul_f32 v163, v163, v236\n"
    "v_mul_f32 v211, v211, v237\n"
    "v_cvt_pk_bf16_f32 v160, v160, v161\n"
    "v_cvt_pk_bf16_f32 v208, v208, v209\n"
    "v_mfma_f32_16x16x32_bf16 v[28:31], a[200:203], a[32:35], v[28:31]\n"
    "ds_write_b64 v247, v[124:125] offset:14336\n"
    "v_cvt_pk_bf16_f32 v161, v162, v163\n"
    "v_cvt_pk_bf16_f32 v209, v210, v211\n"
    "v_mfma_f32_16x16x32_bf16 v[28:31], a[204:207], a[36:39], v[28:31]\n"
    "buffer_store_dwordx4 a[108:111], v250, s[80:83], s42 offen offset:3072 nt sc1\n"
    "v_accvgpr_read_b32 v68, a128\n"
    "v_accvgpr_read_b32 v76, a176\n"
    "v_accvgpr_read_b32 v69, a129\n"
    "v_mfma_f32_16x16x32_bf16 v[44:47], a[200:203], a[56:59], v[44:47]\n"
    "ds_read_b128 a[236:239], v225 offset:22528\n"
    "ds_write_b64 v248, v[160:161] offset:0\n"
    "v_accvgpr_read_b32 v77, a177\n"
    "v_accvgpr_read_b32 v70, a130\n"
    "v_mfma_f32_16x16x32_bf16 v[44:47], a[204:207], a[60:63], v[44:47]\n"
    "v_accvgpr_read_b32 v78, a178\n"
    "v_accvgpr_read_b32 v71, a131\n"
    "v_accvgpr_read_b32 v79, a179\n"
    "v_mul_f32 v68, v68, v238\n"
    "v_mul_f32 v76, v76, v239\n"
    "v_mfma_f32_16x16x32_bf16 v[60:63], a[200:203], a[80:83], v[60:63]\n"
    "ds_write_b64 v248, v[208:209] offset:2048\n"
    "v_mul_f32 v69, v69, v238\n"
    "v_mul_f32 v77, v77, v239\n"
    "v_mfma_f32_16x16x32_bf16 v[60:63], a[204:207], a[84:87], v[60:63]\n"
    "s_waitcnt lgkmcnt(15)\n"
    "v_mul_f32 v70, v70, v238\n"
    "v_mul_f32 v78, v78, v239\n"
    "v_mul_f32 v71, v71, v238\n"
    "v_mul_f32 v79, v79, v239\n"
    "v_mfma_f32_16x16x32_bf16 v[0:3], a[208:211], a[16:19], v[0:3]\n"
    "v_cvt_pk_bf16_f32 v68, v68, v69\n"
    "v_cvt_pk_bf16_f32 v76, v76, v77\n"
    "v_cvt_pk_bf16_f32 v69, v70, v71\n"
    "v_cvt_pk_bf16_f32 v77, v78, v79\n"
    "v_mul_f32 v164, v164, v236\n"
    "v_mfma_f32_16x16x32_bf16 v[0:3], a[212:215], a[20:23], v[0:3]\n"
    "ds_write_b64 v248, v[68:69] offset:4096\n"
    "v_mul_f32 v212, v212, v237\n"
    "v_mul_f32 v165, v165, v236\n"
    "v_mfma_f32_16x16x32_bf16 v[16:19], a[208:211], a[40:43], v[16:19]\n"
    "v_mul_f32 v213, v213, v237\n"
    "v_mul_f32 v166, v166, v236\n"
    "v_mul_f32 v214, v214, v237\n"
    "v_mul_f32 v167, v167, v236\n"
    "v_mul_f32 v215, v215, v237\n"
    "v_mfma_f32_16x16x32_bf16 v[16:19], a[212:215], a[44:47], v[16:19]\n"
    "ds_write_b64 v248, v[76:77] offset:6144\n"
    "v_cvt_pk_bf16_f32 v164, v164, v165\n"
    "v_cvt_pk_bf16_f32 v212, v212, v213\n"
    "v_cvt_pk_bf16_f32 v165, v166, v167\n"
    "v_mfma_f32_16x16x32_bf16 v[32:35], a[208:211], a[64:67], v[32:35]\n"
    "v_cvt_pk_bf16_f32 v213, v214, v215\n"
    "v_accvgpr_read_b32 v84, a132\n"
    "v_accvgpr_read_b32 v92, a180\n"
    "v_accvgpr_read_b32 v85, a133\n"
    "v_accvgpr_read_b32 v93, a181\n"
    "v_mfma_f32_16x16x32_bf16 v[32:35], a[212:215], a[68:71], v[32:35]\n"
    "ds_write_b64 v245, v[164:165] offset:0\n"
    "v_accvgpr_read_b32 v86, a134\n"
    "v_accvgpr_read_b32 v94, a182\n"
    "v_mfma_f32_16x16x32_bf16 v[48:51], a[208:211], a[88:91], v[48:51]\n"
    "v_accvgpr_read_b32 v87, a135\n"
    "v_accvgpr_read_b32 v95, a183\n"
    "v_mul_f32 v84, v84, v238\n"
    "v_mul_f32 v92, v92, v239\n"
    "v_mul_f32 v85, v85, v238\n"
    "v_mfma_f32_16x16x32_bf16 v[48:51], a[212:215], a[92:95], v[48:51]\n"
    "s_waitcnt vmcnt(27) lgkmcnt(5)\n"
    "ds_write_b64 v245, v[212:213] offset:2048\n"
    "v_mul_f32 v93, v93, v239\n"
    "v_mfma_f32_16x16x32_bf16 v[4:7], a[216:219], a[16:19], v[4:7]\n"
    "s_barrier\n"
    "v_mul_f32 v86, v86, v238\n"
    "v_mul_f32 v94, v94, v239\n"
    "v_mul_f32 v87, v87, v238\n"
    "v_mul_f32 v95, v95, v239\n"
    "v_mfma_f32_16x16x32_bf16 v[4:7], a[220:223], a[20:23], v[4:7]\n"
    "ds_read_b128 a[192:195], v224 offset:24576\n"
    "v_cvt_pk_bf16_f32 v84, v84, v85\n"
    "v_cvt_pk_bf16_f32 v92, v92, v93\n"
    "v_cvt_pk_bf16_f32 v85, v86, v87\n"
    "v_cvt_pk_bf16_f32 v93, v94, v95\n"
    "v_mfma_f32_16x16x32_bf16 v[20:23], a[216:219], a[40:43], v[20:23]\n"
    "ds_read_b128 a[196:199], v225 offset:24576\n"
    "ds_write_b64 v245, v[84:85] offset:4096\n"
    "v_mul_f32 v168, v168, v236\n"
    "v_mfma_f32_16x16x32_bf16 v[20:23], a[220:223], a[44:47], v[20:23]\n"
    "ds_read_b128 a[200:203], v224 offset:32768\n"
    "v_mul_f32 v216, v216, v237\n"
    "v_mul_f32 v169, v169, v236\n"
    "v_mul_f32 v217, v217, v237\n"
    "v_mul_f32 v170, v170, v236\n"
    "v_mul_f32 v218, v218, v237\n"
    "v_mfma_f32_16x16x32_bf16 v[36:39], a[216:219], a[64:67], v[36:39]\n"
    "ds_read_b128 a[204:207], v225 offset:32768\n"
    "ds_write_b64 v245, v[92:93] offset:6144\n"
    "v_mul_f32 v171, v171, v236\n"
    "v_mfma_f32_16x16x32_bf16 v[36:39], a[220:223], a[68:71], v[36:39]\n"
    "v_mul_f32 v219, v219, v237\n"
    "v_cvt_pk_bf16_f32 v168, v168, v169\n"
    "v_cvt_pk_bf16_f32 v216, v216, v217\n"
    "v_cvt_pk_bf16_f32 v169, v170, v171\n"
    "v_cvt_pk_bf16_f32 v217, v218, v219\n"
    "v_mfma_f32_16x16x32_bf16 v[52:55], a[216:219], a[88:91], v[52:55]\n"
    "ds_read_b128 a[208:211], v224 offset:40960\n"
    "ds_write_b64 v246, v[168:169] offset:0\n"
    "v_accvgpr_read_b32 v100, a136\n"
    "v_mfma_f32_16x16x32_bf16 v[52:55], a[220:223], a[92:95], v[52:55]\n"
    "v_accvgpr_read_b32 v108, a184\n"
    "v_accvgpr_read_b32 v101, a137\n"
    "v_accvgpr_read_b32 v109, a185\n"
    "v_accvgpr_read_b32 v102, a138\n"
    "v_accvgpr_read_b32 v110, a186\n"
    "v_mfma_f32_16x16x32_bf16 v[8:11], a[224:227], a[16:19], v[8:11]\n"
    "ds_write_b64 v246, v[216:217] offset:2048\n"
    "v_accvgpr_read_b32 v103, a139\n"
    "v_accvgpr_read_b32 v111, a187\n"
    "v_mfma_f32_16x16x32_bf16 v[8:11], a[228:231], a[20:23], v[8:11]\n"
    "ds_read_b128 a[212:215], v225 offset:40960\n"
    "v_mul_f32 v100, v100, v238\n"
    "v_mul_f32 v108, v108, v239\n"
    "v_mul_f32 v101, v101, v238\n"
    "v_mul_f32 v109, v109, v239\n"
    "v_mfma_f32_16x16x32_bf16 v[24:27], a[224:227], a[40:43], v[24:27]\n"
    "v_mul_f32 v102, v102, v238\n"
    "v_mul_f32 v110, v110, v239\n"
    "v_mul_f32 v103, v103, v238\n"
    "v_mul_f32 v111, v111, v239\n"
    "v_cvt_pk_bf16_f32 v100, v100, v101\n"
    "v_cvt_pk_bf16_f32 v108, v108, v109\n"
    "v_mfma_f32_16x16x32_bf16 v[24:27], a[228:231], a[44:47], v[24:27]\n"
    "ds_read_b128 a[216:219], v224 offset:26624\n"
    "v_cvt_pk_bf16_f32 v101, v102, v103\n"
    "v_cvt_pk_bf16_f32 v109, v110, v111\n"
    "v_mul_f32 v172, v172, v236\n"
    "v_mul_f32 v220, v220, v237\n"
    "v_mfma_f32_16x16x32_bf16 v[40:43], a[224:227], a[64:67], v[40:43]\n"
    "ds_write_b64 v246, v[100:101] offset:4096\n"
    "v_mul_f32 v173, v173, v236\n"
    "v_mul_f32 v221, v221, v237\n"
    "v_mfma_f32_16x16x32_bf16 v[40:43], a[228:231], a[68:71], v[40:43]\n"
    "ds_read_b128 a[220:223], v225 offset:26624\n"
    "v_mul_f32 v174, v174, v236\n"
    "v_mul_f32 v222, v222, v237\n"
    "v_mul_f32 v175, v175, v236\n"
    "v_mul_f32 v223, v223, v237\n"
    "v_mfma_f32_16x16x32_bf16 v[56:59], a[224:227], a[88:91], v[56:59]\n"
    "ds_write_b64 v246, v[108:109] offset:6144\n"
    "v_cvt_pk_bf16_f32 v172, v172, v173\n"
    "v_cvt_pk_bf16_f32 v220, v220, v221\n"
    "v_mfma_f32_16x16x32_bf16 v[56:59], a[228:231], a[92:95], v[56:59]\n"
    "v_cvt_pk_bf16_f32 v173, v174, v175\n"
    "v_cvt_pk_bf16_f32 v221, v222, v223\n"
    "v_accvgpr_read_b32 v116, a140\n"
    "v_accvgpr_read_b32 v124, a188\n"
    "v_accvgpr_read_b32 v117, a141\n"
    "v_mfma_f32_16x16x32_bf16 v[12:15], a[232:235], a[16:19], v[12:15]\n"
    "ds_write_b64 v247, v[172:173] offset:0\n"
    "v_accvgpr_read_b32 v125, a189\n"
    "v_accvgpr_read_b32 v118, a142\n"
    "v_mfma_f32_16x16x32_bf16 v[12:15], a[236:239], a[20:23], v[12:15]\n"
    "ds_read_b128 a[224:227], v224 offset:34816\n"
    "v_accvgpr_read_b32 v126, a190\n"
    "v_accvgpr_read_b32 v119, a143\n"
    "v_accvgpr_read_b32 v127, a191\n"
    "v_mul_f32 v116, v116, v238\n"
    "v_mul_f32 v124, v124, v239\n"
    "v_mfma_f32_16x16x32_bf16 v[28:31], a[232:235], a[40:43], v[28:31]\n"
    "ds_write_b64 v247, v[220:221] offset:2048\n"
    "v_mul_f32 v117, v117, v238\n"
    "v_mul_f32 v125, v125, v239\n"
    "v_mfma_f32_16x16x32_bf16 v[28:31], a[236:239], a[44:47], v[28:31]\n"
    "v_mul_f32 v118, v118, v238\n"
    "v_mul_f32 v126, v126, v239\n"
    "v_mul_f32 v119, v119, v238\n"
    "v_mul_f32 v127, v127, v239\n"
    "v_cvt_pk_bf16_f32 v116, v116, v117\n"
    "v_mfma_f32_16x16x32_bf16 v[44:47], a[232:235], a[64:67], v[44:47]\n"
    "v_cvt_pk_bf16_f32 v124, v124, v125\n"
    "v_cvt_pk_bf16_f32 v117, v118, v119\n"
    "v_cvt_pk_bf16_f32 v125, v126, v127\n"
    "v_mfma_f32_16x16x32_bf16 v[44:47], a[236:239], a[68:71], v[44:47]\n"
    "ds_read_b128 a[228:231], v225 offset:34816\n"
    "ds_write_b64 v247, v[116:117] offset:4096\n"
    "v_mfma_f32_16x16x32_bf16 v[60:63], a[232:235], a[88:91], v[60:63]\n"
    "v_mfma_f32_16x16x32_bf16 v[60:63], a[236:239], a[92:95], v[60:63]\n"
    "s_waitcnt lgkmcnt(9)\n"
    "ds_write_b64 v247, v[124:125] offset:6144\n"
    "s_mov_b32 s84, s80\n"
    "s_mov_b32 s85, s81\n"
    "s_mov_b32 s86, s82\n"
    "s_mov_b32 s80, s64\n"
    "s_mov_b32 s81, s65\n"
    "s_mov_b32 s82, s66\n"
    "s_mov_b32 s30, 0\n"
    "s_mov_b32 s46, 1\n"
    "s_mov_b32 s49, 0x30000\n"
    "LSITEM_%=:\n"
    "v_mfma_f32_16x16x32_bf16 v[64:67], a[192:195], a[0:3], 0\n"
    "s_mov_b32 s39, s49\n"
    "v_max3_f32 v236, v0, v1, v2\n"
    "v_max3_f32 v237, v16, v17, v18\n"
    "v_mfma_f32_16x16x32_bf16 v[64:67], a[196:199], a[4:7], v[64:67]\n"
    "s_mov_b32 s38, 0\n"
    "v_max3_f32 v238, v32, v33, v34\n"
    "v_max3_f32 v239, v48, v49, v50\n"
    "v_max3_f32 v236, v236, v3, v4\n"
    "v_mfma_f32_16x16x32_bf16 v[64:67], a[200:203], a[8:11], v[64:67]\n"
    "s_mov_b32 m0, s52\n"
    "v_max3_f32 v237, v237, v19, v20\n"
    "v_max3_f32 v238, v238, v35, v36\n"
    "v_max3_f32 v239, v239, v51, v52\n"
    "v_mfma_f32_16x16x32_bf16 v[64:67], a[204:207], a[12:15], v[64:67]\n"
    "ds_read_b128 a[232:235], v224 offset:43008\n"
    "v_max3_f32 v236, v236, v5, v6\n"
    "v_max3_f32 v237, v237, v21, v22\n"
    "v_max3_f32 v238, v238, v37, v38\n"
    "v_mfma_f32_16x16x32_bf16 v[64:67], a[208:211], a[16:19], v[64:67]\n"
    "buffer_load_dwordx4 v228, s[20:23], s36 offen offset:0 nt lds\n"
    "v_max3_f32 v239, v239, v53, v54\n"
    "v_max3_f32 v236, v236, v7, v8\n"
    "v_mfma_f32_16x16x32_bf16 v[64:67], a[212:215], a[20:23], v[64:67]\n"
    "v_max3_f32 v237, v237, v23, v24\n"
    "v_max3_f32 v238, v238, v39, v40\n"
    "v_max3_f32 v239, v239, v55, v56\n"
    "v_mfma_f32_16x16x32_bf16 v[80:83], a[192:195], a[24:27], 0\n"
    "v_max3_f32 v236, v236, v9, v10\n"
    "v_max3_f32 v237, v237, v25, v26\n"
    "v_max3_f32 v238, v238, v41, v42\n"
    "v_max3_f32 v239, v239, v57, v58\n"
    "v_mfma_f32_16x16x32_bf16 v[80:83], a[196:199], a[28:31], v[80:83]\n"
    "ds_read_b128 a[236:239], v225 offset:43008\n"
    "v_max3_f32 v236, v236, v11, v12\n"
    "v_max3_f32 v237, v237, v27, v28\n"
    "v_max3_f32 v238, v238, v43, v44\n"
    "v_mfma_f32_16x16x32_bf16 v[80:83], a[200:203], a[32:35], v[80:83]\n"
    "v_max3_f32 v239, v239, v59, v60\n"
    "v_max3_f32 v236, v236, v13, v14\n"
    "v_max3_f32 v237, v237, v29, v30\n"
    "v_max3_f32 v238, v238, v45, v46\n"
    "v_mfma_f32_16x16x32_bf16 v[80:83], a[204:207], a[36:39], v[80:83]\n"
    "buffer_load_dwordx4 v229, s[20:23], s36 offen offset:1024 nt lds\n"
    "v_max3_f32 v239, v239, v61, v62\n"
    "v_max_f32 v236, v236, v15\n"
    "v_mfma_f32_16x16x32_bf16 v[80:83], a[208:211], a[40:43], v[80:83]\n"
    "v_max_f32 v237, v237, v31\n"
    "v_max_f32 v238, v238, v47\n"
    "v_max_f32 v239, v239, v63\n"
    "v_mov_b32 v231, v236\n"
    "v_mfma_f32_16x16x32_bf16 v[80:83], a[212:215], a[44:47], v[80:83]\n"
    "v_mov_b32 v255, v237\n"
    "v_mov_b32 v253, v238\n"
    "v_mov_b32 v254, v239\n"
    "v_mfma_f32_16x16x32_bf16 v[96:99], a[192:195], a[48:51], 0\n"
    "s_mov_b32 m0, s53\n"
    "v_permlane16_swap_b32 v236, v231\n"
    "v_max_f32 v236, v236, v231\n"
    "v_mfma_f32_16x16x32_bf16 v[96:99], a[196:199], a[52:55], v[96:99]\n"
    "buffer_store_dwordx4 a[144:147], v250, s[84:87], s43 offen offset:0 nt sc1\n"
    "v_permlane16_swap_b32 v237, v255\n"
    "v_mfma_f32_16x16x32_bf16 v[96:99], a[200:203], a[56:59], v[96:99]\n"
    "buffer_load_dwordx4 v228, s[20:23], s36 offen offset:128 nt lds\n"
    "v_permlane16_swap_b32 v238, v253\n"
    "v_mfma_f32_16x16x32_bf16 v[96:99], a[204:207], a[60:63], v[96:99]\n"
    "s_mov_b32 s78, 0\n"
    "v_permlane16_swap_b32 v239, v254\n"
    "v_max_f32 v237, v237, v255\n"
    "v_mfma_f32_16x16x32_bf16 v[96:99], a[208:211], a[64:67], v[96:99]\n"
    "s_mov_b32 s79, s43\n"
    "v_max_f32 v238, v238, v253\n"
    "v_max_f32 v239, v239, v254\n"
    "v_mov_b32 v231, v236\n"
    "v_mfma_f32_16x16x32_bf16 v[96:99], a[212:215], a[68:71], v[96:99]\n"
    "v_mov_b32 v255, v237\n"
    "v_mov_b32 v253, v238\n"
    "v_mov_b32 v254, v239\n"
    "v_mfma_f32_16x16x32_bf16 v[112:115], a[192:195], a[72:75], 0\n"
    "v_permlane32_swap_b32 v236, v231\n"
    "v_permlane32_swap_b32 v237, v255\n"
    "v_mfma_f32_16x16x32_bf16 v[112:115], a[196:199], a[76:79], v[112:115]\n"
    "buffer_load_dwordx4 v229, s[20:23], s36 offen offset:1152 nt lds\n"
    "v_permlane32_swap_b32 v238, v253\n"
    "v_mfma_f32_16x16x32_bf16 v[112:115], a[200:203], a[80:83], v[112:115]\n"
    "ds_read_b128 a[192:195], v224 offset:28672\n"
    "v_permlane32_swap_b32 v239, v254\n"
    "v_max_f32 v236, v236, v231\n"
    "v_mfma_f32_16x16x32_bf16 v[112:115], a[204:207], a[84:87], v[112:115]\n"
    "v_max_f32 v237, v237, v255\n"
    "v_max_f32 v238, v238, v253\n"
    "v_max_f32 v239, v239, v254\n"
    "v_mul_f32 v232, s47, v236\n"
    "v_mfma_f32_16x16x32_bf16 v[112:115], a[208:211], a[88:91], v[112:115]\n"
    "s_mov_b32 m0, s54\n"
    "ds_read_b128 a[196:199], v225 offset:28672\n"
    "v_mul_f32 v233, s47, v237\n"
    "v_mfma_f32_16x16x32_bf16 v[112:115], a[212:215], a[92:95], v[112:115]\n"
    "s_waitcnt lgkmcnt(2)\n"
    "v_mul_f32 v234, s47, v238\n"
    "v_mul_f32 v235, s47, v239\n"
    "v_fma_f32 v0, v0, s40, v232\n"
    "v_mfma_f32_16x16x32_bf16 v[68:71], a[216:219], a[0:3], 0\n"
    "buffer_load_dwordx4 v228, s[20:23], s36 offen offset:256 nt lds\n"
    "v_exp_f32 v0, v0\n"
    "v_mfma_f32_16x16x32_bf16 v[68:71], a[220:223], a[4:7], v[68:71]\n"
    "ds_read_b128 a[200:203], v224 offset:36864\n"
    "v_fma_f32 v16, v16, s40, v233\n"
    "v_exp_f32 v16, v16\n"
    "v_mfma_f32_16x16x32_bf16 v[68:71], a[224:227], a[8:11], v[68:71]\n"
    "v_fma_f32 v32, v32, s40, v234\n"
    "v_exp_f32 v32, v32\n"
    "v_fma_f32 v48, v48, s40, v235\n"
    "v_mfma_f32_16x16x32_bf16 v[68:71], a[228:231], a[12:15], v[68:71]\n"
    "ds_read_b128 a[204:207], v225 offset:36864\n"
    "v_exp_f32 v48, v48\n"
    "v_fma_f32 v1, v1, s40, v232\n"
    "v_mfma_f32_16x16x32_bf16 v[68:71], a[232:235], a[16:19], v[68:71]\n"
    "v_exp_f32 v1, v1\n"
    "v_fma_f32 v17, v17, s40, v233\n"
    "v_mfma_f32_16x16x32_bf16 v[68:71], a[236:239], a[20:23], v[68:71]\n"
    "buffer_load_dwordx4 v229, s[20:23], s36 offen offset:1280 nt lds\n"
    "v_exp_f32 v17, v17\n"
    "v_mfma_f32_16x16x32_bf16 v[84:87], a[216:219], a[24:27], 0\n"
    "s_add_u32 s36, s36, 24576\n"
    "v_fma_f32 v33, v33, s40, v234\n"
    "v_exp_f32 v33, v33\n"
    "v_mfma_f32_16x16x32_bf16 v[84:87], a[220:223], a[28:31], v[84:87]\n"
    "ds_read_b128 a[208:211], v224 offset:45056\n"
    "v_fma_f32 v49, v49, s40, v235\n"
    "v_exp_f32 v49, v49\n"
    "v_mfma_f32_16x16x32_bf16 v[84:87], a[224:227], a[32:35], v[84:87]\n"
    "buffer_store_dwordx4 a[148:151], v250, s[84:87], s43 offen offset:3072 nt sc1\n"
    "v_fma_f32 v2, v2, s40, v232\n"
    "v_fma_f32 v18, v18, s40, v233\n"
    "v_mfma_f32_16x16x32_bf16 v[84:87], a[228:231], a[36:39], v[84:87]\n"
    "ds_read_b128 a[212:215], v225 offset:45056\n"
    "v_exp_f32 v2, v2\n"
    "v_fma_f32 v34, v34, s40, v234\n"
    "v_mfma_f32_16x16x32_bf16 v[84:87], a[232:235], a[40:43], v[84:87]\n"
    "v_exp_f32 v18, v18\n"
    "v_fma_f32 v50, v50, s40, v235\n"
    "v_mfma_f32_16x16x32_bf16 v[84:87], a[236:239], a[44:47], v[84:87]\n"
    "v_exp_f32 v34, v34\n"
    "v_exp_f32 v50, v50\n"
    "v_mfma_f32_16x16x32_bf16 v[100:103], a[216:219], a[48:51], 0\n"
    "v_cvt_pk_bf16_f32 v0, v0, v1\n"
    "v_fma_f32 v3, v3, s40, v232\n"
    "v_exp_f32 v3, v3\n"
    "v_mfma_f32_16x16x32_bf16 v[100:103], a[220:223], a[52:55], v[100:103]\n"
    "v_cvt_pk_bf16_f32 v16, v16, v17\n"
    "v_fma_f32 v19, v19, s40, v233\n"
    "v_exp_f32 v19, v19\n"
    "v_mfma_f32_16x16x32_bf16 v[100:103], a[224:227], a[56:59], v[100:103]\n"
    "s_add_u32 m0, s39, 0\n"
    "v_cvt_pk_bf16_f32 v32, v32, v33\n"
    "v_fma_f32 v35, v35, s40, v234\n"
    "v_cvt_pk_bf16_f32 v48, v48, v49\n"
    "v_mfma_f32_16x16x32_bf16 v[100:103], a[228:231], a[60:63], v[100:103]\n"
    "v_exp_f32 v35, v35\n"
    "v_fma_f32 v51, v51, s40, v235\n"
    "v_fma_f32 v4, v4, s40, v232\n"
    "v_mfma_f32_16x16x32_bf16 v[100:103], a[232:235], a[64:67], v[100:103]\n"
    "buffer_load_dwordx4 v228, s[28:31], s38 offen offset:0 nt lds\n"
    "v_fma_f32 v20, v20, s40, v233\n"
    "v_mfma_f32_16x16x32_bf16 v[100:103], a[236:239], a[68:71], v[100:103]\n"
    "v_exp_f32 v51, v51\n"
    "v_exp_f32 v4, v4\n"
    "v_mfma_f32_16x16x32_bf16 v[116:119], a[216:219], a[72:75], 0\n"
    "v_exp_f32 v20, v20\n"
    "v_fma_f32 v36, v36, s40, v234\n"
    "v_fma_f32 v52, v52, s40, v235\n"
    "v_mfma_f32_16x16x32_bf16 v[116:119], a[220:223], a[76:79], v[116:119]\n"
    "ds_read_b128 a[216:219], v224 offset:30720\n"
    "v_exp_f32 v36, v36\n"
    "v_cvt_pk_bf16_f32 v1, v2, v3\n"
    "v_mfma_f32_16x16x32_bf16 v[116:119], a[224:227], a[80:83], v[116:119]\n"
    "ds_read_b128 a[220:223], v225 offset:30720\n"
    "v_exp_f32 v52, v52\n"
    "v_fma_f32 v5, v5, s40, v232\n"
    "v_mfma_f32_16x16x32_bf16 v[116:119], a[228:231], a[84:87], v[116:119]\n"
    "buffer_load_dwordx4 v229, s[28:31], s38 offen offset:1024 nt lds\n"
    "v_exp_f32 v5, v5\n"
    "v_mfma_f32_16x16x32_bf16 v[116:119], a[232:235], a[88:91], v[116:119]\n"
    "ds_read_b128 a[224:227], v224 offset:38912\n"
    "v_cvt_pk_bf16_f32 v17, v18, v19\n"
    "v_fma_f32 v21, v21, s40, v233\n"
    "v_mfma_f32_16x16x32_bf16 v[116:119], a[236:239], a[92:95], v[116:119]\n"
    "s_waitcnt lgkmcnt(3)\n"
    "v_exp_f32 v21, v21\n"
    "v_cvt_pk_bf16_f32 v33, v34, v35\n"
    "v_mfma_f32_16x16x32_bf16 v[72:75], a[192:195], a[0:3], 0\n"
    "s_add_u32 m0, s39, 8064\n"
    "v_fma_f32 v37, v37, s40, v234\n"
    "v_exp_f32 v37, v37\n"
    "v_mfma_f32_16x16x32_bf16 v[72:75], a[196:199], a[4:7], v[72:75]\n"
    "ds_read_b128 a[228:231], v225 offset:38912\n"
    "v_cvt_pk_bf16_f32 v49, v50, v51\n"
    "v_fma_f32 v53, v53, s40, v235\n"
    "v_fma_f32 v6, v6, s40, v232\n"
    "v_mfma_f32_16x16x32_bf16 v[72:75], a[200:203], a[8:11], v[72:75]\n"
    "buffer_load_dwordx4 v228, s[28:31], s38 offen offset:128 nt lds\n"
    "v_exp_f32 v53, v53\n"
    "v_mfma_f32_16x16x32_bf16 v[72:75], a[204:207], a[12:15], v[72:75]\n"
    "ds_read_b128 a[232:235], v224 offset:47104\n"
    "v_exp_f32 v6, v6\n"
    "v_mfma_f32_16x16x32_bf16 v[72:75], a[208:211], a[16:19], v[72:75]\n"
    "buffer_store_dwordx4 a[152:155], v250, s[84:87], s44 offen offset:0 nt sc1\n"
    "v_fma_f32 v22, v22, s40, v233\n"
    "v_fma_f32 v38, v38, s40, v234\n"
    "v_mfma_f32_16x16x32_bf16 v[72:75], a[212:215], a[20:23], v[72:75]\n"
    "v_exp_f32 v22, v22\n"
    "v_exp_f32 v38, v38\n"
    "v_mfma_f32_16x16x32_bf16 v[88:91], a[192:195], a[24:27], 0\n"
    "ds_read_b128 a[236:239], v225 offset:47104\n"
    "v_fma_f32 v54, v54, s40, v235\n"
    "v_exp_f32 v54, v54\n"
    "v_mfma_f32_16x16x32_bf16 v[88:91], a[196:199], a[28:31], v[88:91]\n"
    "buffer_load_dwordx4 v229, s[28:31], s38 offen offset:1152 nt lds\n"
    "v_cvt_pk_bf16_f32 v2, v4, v5\n"
    "v_fma_f32 v7, v7, s40, v232\n"
    "v_mfma_f32_16x16x32_bf16 v[88:91], a[200:203], a[32:35], v[88:91]\n"
    "s_add_u32 s39, s39, 2048\n"
    "v_exp_f32 v7, v7\n"
    "v_cvt_pk_bf16_f32 v18, v20, v21\n"
    "v_mfma_f32_16x16x32_bf16 v[88:91], a[204:207], a[36:39], v[88:91]\n"
    "s_add_u32 s38, s38, 6144\n"
    "v_fma_f32 v23, v23, s40, v233\n"
    "v_cvt_pk_bf16_f32 v34, v36, v37\n"
    "v_mfma_f32_16x16x32_bf16 v[88:91], a[208:211], a[40:43], v[88:91]\n"
    "v_exp_f32 v23, v23\n"
    "v_fma_f32 v39, v39, s40, v234\n"
    "v_cvt_pk_bf16_f32 v50, v52, v53\n"
    "v_mfma_f32_16x16x32_bf16 v[88:91], a[212:215], a[44:47], v[88:91]\n"
    "v_exp_f32 v39, v39\n"
    "v_fma_f32 v55, v55, s40, v235\n"
    "v_fma_f32 v8, v8, s40, v232\n"
    "v_mfma_f32_16x16x32_bf16 v[104:107], a[192:195], a[48:51], 0\n"
    "v_exp_f32 v55, v55\n"
    "v_exp_f32 v8, v8\n"
    "v_mfma_f32_16x16x32_bf16 v[104:107], a[196:199], a[52:55], v[104:107]\n"
    "v_fma_f32 v24, v24, s40, v233\n"
    "v_exp_f32 v24, v24\n"
    "v_fma_f32 v40, v40, s40, v234\n"
    "v_mfma_f32_16x16x32_bf16 v[104:107], a[200:203], a[56:59], v[104:107]\n"
    "v_exp_f32 v40, v40\n"
    "v_fma_f32 v56, v56, s40, v235\n"
    "v_cvt_pk_bf16_f32 v3, v6, v7\n"
    "v_mfma_f32_16x16x32_bf16 v[104:107], a[204:207], a[60:63], v[104:107]\n"
    "v_exp_f32 v56, v56\n"
    "v_fma_f32 v9, v9, s40, v232\n"
    "v_mfma_f32_16x16x32_bf16 v[104:107], a[208:211], a[64:67], v[104:107]\n"
    "v_exp_f32 v9, v9\n"
    "v_cvt_pk_bf16_f32 v19, v22, v23\n"
    "v_fma_f32 v25, v25, s40, v233\n"
    "v_mfma_f32_16x16x32_bf16 v[104:107], a[212:215], a[68:71], v[104:107]\n"
    "s_waitcnt vmcnt(19) lgkmcnt(0)\n"
    "v_exp_f32 v25, v25\n"
    "v_cvt_pk_bf16_f32 v35, v38, v39\n"
    "v_mfma_f32_16x16x32_bf16 v[120:123], a[192:195], a[72:75], 0\n"
    "s_barrier\n"
    "v_fma_f32 v41, v41, s40, v234\n"
    "v_exp_f32 v41, v41\n"
    "v_mfma_f32_16x16x32_bf16 v[120:123], a[196:199], a[76:79], v[120:123]\n"
    "ds_read_b64_tr_b16 a[192:193], v226 offset:0\n"
    "v_cvt_pk_bf16_f32 v51, v54, v55\n"
    "v_fma_f32 v57, v57, s40, v235\n"
    "v_fma_f32 v10, v10, s40, v232\n"
    "v_mfma_f32_16x16x32_bf16 v[120:123], a[200:203], a[80:83], v[120:123]\n"
    "ds_read_b64_tr_b16 a[194:195], v226 offset:6144\n"
    "v_exp_f32 v57, v57\n"
    "v_fma_f32 v26, v26, s40, v233\n"
    "v_mfma_f32_16x16x32_bf16 v[120:123], a[204:207], a[84:87], v[120:123]\n"
    "ds_read_b64_tr_b16 a[196:197], v226 offset:12288\n"
    "v_exp_f32 v10, v10\n"
    "v_mfma_f32_16x16x32_bf16 v[120:123], a[208:211], a[88:91], v[120:123]\n"
    "ds_read_b64_tr_b16 a[198:199], v226 offset:18432\n"
    "v_exp_f32 v26, v26\n"
    "v_fma_f32 v42, v42, s40, v234\n"
    "v_mfma_f32_16x16x32_bf16 v[120:123], a[212:215], a[92:95], v[120:123]\n"
    "ds_read_b64_tr_b16 a[200:201], v227 offset:0\n"
    "v_exp_f32 v42, v42\n"
    "v_fma_f32 v58, v58, s40, v235\n"
    "v_mfma_f32_16x16x32_bf16 v[76:79], a[216:219], a[0:3], 0\n"
    "ds_read_b64_tr_b16 a[202:203], v227 offset:6144\n"
    "v_exp_f32 v58, v58\n"
    "v_cvt_pk_bf16_f32 v8, v8, v9\n"
    "v_mfma_f32_16x16x32_bf16 v[76:79], a[220:223], a[4:7], v[76:79]\n"
    "ds_read_b64_tr_b16 a[204:205], v227 offset:12288\n"
    "v_fma_f32 v11, v11, s40, v232\n"
    "v_exp_f32 v11, v11\n"
    "v_mfma_f32_16x16x32_bf16 v[76:79], a[224:227], a[8:11], v[76:79]\n"
    "ds_read_b64_tr_b16 a[206:207], v227 offset:18432\n"
    "v_cvt_pk_bf16_f32 v24, v24, v25\n"
    "v_fma_f32 v27, v27, s40, v233\n"
    "v_mfma_f32_16x16x32_bf16 v[76:79], a[228:231], a[12:15], v[76:79]\n"
    "ds_read_b64_tr_b16 a[208:209], v226 offset:512\n"
    "v_exp_f32 v27, v27\n"
    "v_cvt_pk_bf16_f32 v40, v40, v41\n"
    "v_mfma_f32_16x16x32_bf16 v[76:79], a[232:235], a[16:19], v[76:79]\n"
    "buffer_store_dwordx4 a[156:159], v250, s[84:87], s44 offen offset:3072 nt sc1\n"
    "v_fma_f32 v43, v43, s40, v234\n"
    "v_cvt_pk_bf16_f32 v56, v56, v57\n"
    "v_mfma_f32_16x16x32_bf16 v[76:79], a[236:239], a[20:23], v[76:79]\n"
    "ds_read_b64_tr_b16 a[210:211], v226 offset:6656\n"
    "v_exp_f32 v43, v43\n"
    "v_fma_f32 v59, v59, s40, v235\n"
    "v_mfma_f32_16x16x32_bf16 v[92:95], a[216:219], a[24:27], 0\n"
    "v_exp_f32 v59, v59\n"
    "v_fma_f32 v12, v12, s40, v232\n"
    "v_fma_f32 v28, v28, s40, v233\n"
    "v_mfma_f32_16x16x32_bf16 v[92:95], a[220:223], a[28:31], v[92:95]\n"
    "v_exp_f32 v12, v12\n"
    "v_exp_f32 v28, v28\n"
    "v_mfma_f32_16x16x32_bf16 v[92:95], a[224:227], a[32:35], v[92:95]\n"
    "ds_read_b64_tr_b16 a[212:213], v226 offset:12800\n"
    "v_fma_f32 v44, v44, s40, v234\n"
    "v_fma_f32 v60, v60, s40, v235\n"
    "v_mfma_f32_16x16x32_bf16 v[92:95], a[228:231], a[36:39], v[92:95]\n"
    "v_exp_f32 v44, v44\n"
    "v_exp_f32 v60, v60\n"
    "v_mfma_f32_16x16x32_bf16 v[92:95], a[232:235], a[40:43], v[92:95]\n"
    "v_cvt_pk_bf16_f32 v9, v10, v11\n"
    "v_fma_f32 v13, v13, s40, v232\n"
    "v_exp_f32 v13, v13\n"
    "v_mfma_f32_16x16x32_bf16 v[92:95], a[236:239], a[44:47], v[92:95]\n"
    "v_cvt_pk_bf16_f32 v25, v26, v27\n"
    "v_fma_f32 v29, v29, s40, v233\n"
    "v_exp_f32 v29, v29\n"
    "v_mfma_f32_16x16x32_bf16 v[108:111], a[216:219], a[48:51], 0\n"
    "ds_read_b64_tr_b16 a[214:215], v226 offset:18944\n"
    "v_cvt_pk_bf16_f32 v41, v42, v43\n"
    "v_fma_f32 v45, v45, s40, v234\n"
    "v_cvt_pk_bf16_f32 v57, v58, v59\n"
    "v_mfma_f32_16x16x32_bf16 v[108:111], a[220:223], a[52:55], v[108:111]\n"
    "v_exp_f32 v45, v45\n"
    "v_fma_f32 v61, v61, s40, v235\n"
    "v_fma_f32 v14, v14, s40, v232\n"
    "v_mfma_f32_16x16x32_bf16 v[108:111], a[224:227], a[56:59], v[108:111]\n"
    "v_exp_f32 v61, v61\n"
    "v_fma_f32 v30, v30, s40, v233\n"
    "v_mfma_f32_16x16x32_bf16 v[108:111], a[228:231], a[60:63], v[108:111]\n"
    "v_exp_f32 v14, v14\n"
    "v_exp_f32 v30, v30\n"
    "v_mfma_f32_16x16x32_bf16 v[108:111], a[232:235], a[64:67], v[108:111]\n"
    "v_fma_f32 v46, v46, s40, v234\n"
    "v_exp_f32 v46, v46\n"
    "v_fma_f32 v62, v62, s40, v235\n"
    "v_mfma_f32_16x16x32_bf16 v[108:111], a[236:239], a[68:71], v[108:111]\n"
    "v_exp_f32 v62, v62\n"
    "v_cvt_pk_bf16_f32 v10, v12, v13\n"
    "v_fma_f32 v15, v15, s40, v232\n"
    "v_mfma_f32_16x16x32_bf16 v[124:127], a[216:219], a[72:75], 0\n"
    "v_exp_f32 v15, v15\n"
    "v_cvt_pk_bf16_f32 v26, v28, v29\n"
    "v_fma_f32 v31, v31, s40, v233\n"
    "v_mfma_f32_16x16x32_bf16 v[124:127], a[220:223], a[76:79], v[124:127]\n"
    "v_exp_f32 v31, v31\n"
    "v_cvt_pk_bf16_f32 v42, v44, v45\n"
    "v_mfma_f32_16x16x32_bf16 v[124:127], a[224:227], a[80:83], v[124:127]\n"
    "v_fma_f32 v47, v47, s40, v234\n"
    "v_exp_f32 v47, v47\n"
    "v_cvt_pk_bf16_f32 v58, v60, v61\n"
    "v_mfma_f32_16x16x32_bf16 v[124:127], a[228:231], a[84:87], v[124:127]\n"
    "v_fma_f32 v63, v63, s40, v235\n"
    "v_exp_f32 v63, v63\n"
    "v_cvt_pk_bf16_f32 v11, v14, v15\n"
    "v_mfma_f32_16x16x32_bf16 v[124:127], a[232:235], a[88:91], v[124:127]\n"
    "v_cvt_pk_bf16_f32 v27, v30, v31\n"
    "v_cvt_pk_bf16_f32 v43, v46, v47\n"
    "v_cvt_pk_bf16_f32 v59, v62, v63\n"
    "v_mov_b32 v251, v249\n"
    "v_mfma_f32_16x16x32_bf16 v[124:127], a[236:239], a[92:95], v[124:127]\n"
    "s_waitcnt lgkmcnt(0)\n"
    "LSB1_%=:\n"
    "v_mfma_f32_16x16x32_bf16 v[128:131], a[192:195], v[0:3], 0\n"
    "ds_read_b64_tr_b16 a[216:217], v227 offset:512\n"
    "v_max3_f32 v236, v64, v65, v66\n"
    "v_mfma_f32_16x16x32_bf16 v[128:131], a[196:199], v[8:11], v[128:131]\n"
    "s_mov_b32 m0, s55\n"
    "v_max3_f32 v237, v80, v81, v82\n"
    "v_mfma_f32_16x16x32_bf16 v[176:179], a[192:195], v[16:19], 0\n"
    "ds_read_b64_tr_b16 a[218:219], v227 offset:6656\n"
    "v_max3_f32 v238, v96, v97, v98\n"
    "v_mfma_f32_16x16x32_bf16 v[176:179], a[196:199], v[24:27], v[176:179]\n"
    "buffer_load_dwordx4 v228, s[20:23], s36 offen offset:0 nt lds\n"
    "v_mfma_f32_16x16x32_bf16 a[96:99], a[192:195], v[32:35], 0\n"
    "ds_read_b64_tr_b16 a[220:221], v227 offset:12800\n"
    "v_max3_f32 v239, v112, v113, v114\n"
    "v_mfma_f32_16x16x32_bf16 a[96:99], a[196:199], v[40:43], a[96:99]\n"
    "ds_read_b64_tr_b16 a[222:223], v227 offset:18944\n"
    "v_max3_f32 v236, v236, v67, v68\n"
    "v_mfma_f32_16x16x32_bf16 a[144:147], a[192:195], v[48:51], 0\n"
    "ds_read_b64_tr_b16 a[224:225], v226 offset:1024\n"
    "v_max3_f32 v237, v237, v83, v84\n"
    "v_mfma_f32_16x16x32_bf16 a[144:147], a[196:199], v[56:59], a[144:147]\n"
    "ds_read_b64_tr_b16 a[226:227], v226 offset:7168\n"
    "v_max3_f32 v238, v238, v99, v100\n"
    "v_mfma_f32_16x16x32_bf16 v[132:135], a[200:203], v[0:3], 0\n"
    "buffer_load_dwordx4 v229, s[20:23], s36 offen offset:1024 nt lds\n"
    "v_mfma_f32_16x16x32_bf16 v[132:135], a[204:207], v[8:11], v[132:135]\n"
    "ds_read_b64_tr_b16 a[228:229], v226 offset:13312\n"
    "v_max3_f32 v239, v239, v115, v116\n"
    "v_mfma_f32_16x16x32_bf16 v[180:183], a[200:203], v[16:19], 0\n"
    "ds_read_b64_tr_b16 a[230:231], v226 offset:19456\n"
    "v_max3_f32 v236, v236, v69, v70\n"
    "v_mfma_f32_16x16x32_bf16 v[180:183], a[204:207], v[24:27], v[180:183]\n"
    "s_mov_b32 m0, s56\n"
    "v_max3_f32 v237, v237, v85, v86\n"
    "v_mfma_f32_16x16x32_bf16 a[100:103], a[200:203], v[32:35], 0\n"
    "ds_read_b64_tr_b16 a[232:233], v227 offset:1024\n"
    "v_max3_f32 v238, v238, v101, v102\n"
    "v_mfma_f32_16x16x32_bf16 a[100:103], a[204:207], v[40:43], a[100:103]\n"
    "buffer_load_dwordx4 v228, s[20:23], s36 offen offset:128 nt lds\n"
    "ds_read_b64_tr_b16 a[234:235], v227 offset:7168\n"
    "v_mfma_f32_16x16x32_bf16 a[148:151], a[200:203], v[48:51], 0\n"
    "ds_read_b64_tr_b16 a[236:237], v227 offset:13312\n"
    "v_max3_f32 v239, v239, v117, v118\n"
    "v_mfma_f32_16x16x32_bf16 a[148:151], a[204:207], v[56:59], a[148:151]\n"
    "ds_read_b64_tr_b16 a[238:239], v227 offset:19456\n"
    "v_max3_f32 v236, v236, v71, v72\n"
    "v_mfma_f32_16x16x32_bf16 a[240:243], v[240:243], v[0:3], 0\n"
    "ds_read_b64_tr_b16 a[192:193], v226 offset:1536\n"
    "v_max3_f32 v237, v237, v87, v88\n"
    "v_mfma_f32_16x16x32_bf16 a[240:243], v[240:243], v[8:11], a[240:243]\n"
    "ds_read_b64_tr_b16 a[194:195], v226 offset:7680\n"
    "v_max3_f32 v238, v238, v103, v104\n"
    "v_mfma_f32_16x16x32_bf16 v[136:139], a[208:211], v[0:3], 0\n"
    "buffer_load_dwordx4 v229, s[20:23], s36 offen offset:1152 nt lds\n"
    "v_mfma_f32_16x16x32_bf16 v[136:139], a[212:215], v[8:11], v[136:139]\n"
    "ds_read_b64_tr_b16 a[196:197], v226 offset:13824\n"
    "v_max3_f32 v239, v239, v119, v120\n"
    "v_mfma_f32_16x16x32_bf16 v[184:187], a[208:211], v[16:19], 0\n"
    "ds_read_b64_tr_b16 a[198:199], v226 offset:19968\n"
    "v_max3_f32 v236, v236, v73, v74\n"
    "v_mfma_f32_16x16x32_bf16 v[184:187], a[212:215], v[24:27], v[184:187]\n"
    "s_mov_b32 m0, s57\n"
    "v_max3_f32 v237, v237, v89, v90\n"
    "v_mfma_f32_16x16x32_bf16 a[104:107], a[208:211], v[32:35], 0\n"
    "ds_read_b64_tr_b16 a[200:201], v227 offset:1536\n"
    "v_max3_f32 v238, v238, v105, v106\n"
    "v_mfma_f32_16x16x32_bf16 a[104:107], a[212:215], v[40:43], a[104:107]\n"
    "buffer_load_dwordx4 v228, s[20:23], s36 offen offset:256 nt lds\n"
    "v_mfma_f32_16x16x32_bf16 a[152:155], a[208:211], v[48:51], 0\n"
    "ds_read_b64_tr_b16 a[202:203], v227 offset:7680\n"
    "v_max3_f32 v239, v239, v121, v122\n"
    "v_mfma_f32_16x16x32_bf16 a[152:155], a[212:215], v[56:59], a[152:155]\n"
    "s_waitcnt lgkmcnt(6)\n"
    "v_max3_f32 v236, v236, v75, v76\n"
    "v_mfma_f32_16x16x32_bf16 v[140:143], a[216:219], v[0:3], 0\n"
    "ds_read_b64_tr_b16 a[204:205], v227 offset:13824\n"
    "v_max3_f32 v237, v237, v91, v92\n"
    "v_mfma_f32_16x16x32_bf16 v[140:143], a[220:223], v[8:11], v[140:143]\n"
    "ds_read_b64_tr_b16 a[206:207], v227 offset:19968\n"
    "v_max3_f32 v238, v238, v107, v108\n"
    "v_mfma_f32_16x16x32_bf16 v[188:191], a[216:219], v[16:19], 0\n"
    "buffer_load_dwordx4 v229, s[20:23], s36 offen offset:1280 nt lds\n"
    "v_mfma_f32_16x16x32_bf16 v[188:191], a[220:223], v[24:27], v[188:191]\n"
    "s_add_u32 s36, s36, 24576\n"
    "v_max3_f32 v239, v239, v123, v124\n"
    "v_mfma_f32_16x16x32_bf16 a[108:111], a[216:219], v[32:35], 0\n"
    "ds_read_b64_tr_b16 a[208:209], v226 offset:2048\n"
    "v_max3_f32 v236, v236, v77, v78\n"
    "v_mfma_f32_16x16x32_bf16 a[108:111], a[220:223], v[40:43], a[108:111]\n"
    "ds_read_b64_tr_b16 a[210:211], v226 offset:8192\n"
    "v_max3_f32 v237, v237, v93, v94\n"
    "v_mfma_f32_16x16x32_bf16 a[156:159], a[216:219], v[48:51], 0\n"
    "ds_read_b64_tr_b16 a[212:213], v226 offset:14336\n"
    "v_max3_f32 v238, v238, v109, v110\n"
    "v_mfma_f32_16x16x32_bf16 a[156:159], a[220:223], v[56:59], a[156:159]\n"
    "ds_read_b64_tr_b16 a[214:215], v226 offset:20480\n"
    "v_max3_f32 v239, v239, v125, v126\n"
    "v_mfma_f32_16x16x32_bf16 v[144:147], a[224:227], v[0:3], 0\n"
    "ds_read_b64_tr_b16 a[216:217], v227 offset:2048\n"
    "v_max_f32 v236, v236, v79\n"
    "v_mfma_f32_16x16x32_bf16 v[144:147], a[228:231], v[8:11], v[144:147]\n"
    "ds_read_b64_tr_b16 a[218:219], v227 offset:8192\n"
    "v_max_f32 v237, v237, v95\n"
    "v_mfma_f32_16x16x32_bf16 v[192:195], a[224:227], v[16:19], 0\n"
    "ds_read_b64_tr_b16 a[220:221], v227 offset:14336\n"
    "v_max_f32 v238, v238, v111\n"
    "v_mfma_f32_16x16x32_bf16 v[192:195], a[228:231], v[24:27], v[192:195]\n"
    "v_max_f32 v239, v239, v127\n"
    "v_fma_f32 v236, v236, s40, v232\n"
    "v_mfma_f32_16x16x32_bf16 a[112:115], a[224:227], v[32:35], 0\n"
    "ds_read_b64_tr_b16 a[222:223], v227 offset:20480\n"
    "v_fma_f32 v237, v237, s40, v233\n"
    "v_mfma_f32_16x16x32_bf16 a[112:115], a[228:231], v[40:43], a[112:115]\n"
    "v_fma_f32 v238, v238, s40, v234\n"
    "v_fma_f32 v239, v239, s40, v235\n"
    "v_mfma_f32_16x16x32_bf16 a[160:163], a[224:227], v[48:51], 0\n"
    "v_max3_f32 v231, v236, v237, v238\n"
    "v_max_f32 v231, v231, v239\n"
    "v_mfma_f32_16x16x32_bf16 a[160:163], a[228:231], v[56:59], a[160:163]\n"
    "v_cmp_lt_f32 vcc, s41, v231\n"
    "s_cbranch_vccnz LSRAf_%=\n"
    "LSRAbf_%=:\n"
    "v_mfma_f32_16x16x32_bf16 a[244:247], v[240:243], v[16:19], 0\n"
    "ds_read_b64_tr_b16 a[224:225], v226 offset:2560\n"
    "v_fma_f32 v64, v64, s40, v232\n"
    "v_mfma_f32_16x16x32_bf16 a[244:247], v[240:243], v[24:27], a[244:247]\n"
    "v_exp_f32 v64, v64\n"
    "v_mfma_f32_16x16x32_bf16 v[148:151], a[232:235], v[0:3], 0\n"
    "ds_read_b64_tr_b16 a[226:227], v226 offset:8704\n"
    "v_fma_f32 v80, v80, s40, v233\n"
    "v_mfma_f32_16x16x32_bf16 v[148:151], a[236:239], v[8:11], v[148:151]\n"
    "v_exp_f32 v80, v80\n"
    "v_mfma_f32_16x16x32_bf16 v[196:199], a[232:235], v[16:19], 0\n"
    "v_fma_f32 v96, v96, s40, v234\n"
    "v_fma_f32 v112, v112, s40, v235\n"
    "v_mfma_f32_16x16x32_bf16 v[196:199], a[236:239], v[24:27], v[196:199]\n"
    "ds_read_b64_tr_b16 a[228:229], v226 offset:14848\n"
    "v_fma_f32 v65, v65, s40, v232\n"
    "v_mfma_f32_16x16x32_bf16 a[116:119], a[232:235], v[32:35], 0\n"
    "v_exp_f32 v96, v96\n"
    "v_mfma_f32_16x16x32_bf16 a[116:119], a[236:239], v[40:43], a[116:119]\n"
    "v_exp_f32 v112, v112\n"
    "v_mfma_f32_16x16x32_bf16 a[164:167], a[232:235], v[48:51], 0\n"
    "ds_read_b64_tr_b16 a[230:231], v226 offset:20992\n"
    "v_fma_f32 v81, v81, s40, v233\n"
    "v_mfma_f32_16x16x32_bf16 a[164:167], a[236:239], v[56:59], a[164:167]\n"
    "s_waitcnt lgkmcnt(4)\n"
    "v_fma_f32 v97, v97, s40, v234\n"
    "v_mfma_f32_16x16x32_bf16 v[152:155], a[192:195], v[0:3], 0\n"
    "v_exp_f32 v65, v65\n"
    "v_mfma_f32_16x16x32_bf16 v[152:155], a[196:199], v[8:11], v[152:155]\n"
    "ds_read_b64_tr_b16 a[232:233], v227 offset:2560\n"
    "v_fma_f32 v113, v113, s40, v235\n"
    "v_mfma_f32_16x16x32_bf16 v[200:203], a[192:195], v[16:19], 0\n"
    "v_exp_f32 v81, v81\n"
    "v_mfma_f32_16x16x32_bf16 v[200:203], a[196:199], v[24:27], v[200:203]\n"
    "v_exp_f32 v97, v97\n"
    "v_mfma_f32_16x16x32_bf16 a[120:123], a[192:195], v[32:35], 0\n"
    "v_exp_f32 v113, v113\n"
    "v_mfma_f32_16x16x32_bf16 a[120:123], a[196:199], v[40:43], a[120:123]\n"
    "ds_read_b64_tr_b16 a[234:235], v227 offset:8704\n"
    "v_fma_f32 v66, v66, s40, v232\n"
    "v_mfma_f32_16x16x32_bf16 a[168:171], a[192:195], v[48:51], 0\n"
    "v_exp_f32 v66, v66\n"
    "v_mfma_f32_16x16x32_bf16 a[168:171], a[196:199], v[56:59], a[168:171]\n"
    "v_fma_f32 v82, v82, s40, v233\n"
    "v_fma_f32 v98, v98, s40, v234\n"
    "v_mfma_f32_16x16x32_bf16 v[156:159], a[200:203], v[0:3], 0\n"
    "v_exp_f32 v82, v82\n"
    "v_mfma_f32_16x16x32_bf16 v[156:159], a[204:207], v[8:11], v[156:159]\n"
    "v_exp_f32 v98, v98\n"
    "v_mfma_f32_16x16x32_bf16 v[204:207], a[200:203], v[16:19], 0\n"
    "ds_read_b64_tr_b16 a[236:237], v227 offset:14848\n"
    "v_fma_f32 v114, v114, s40, v235\n"
    "v_mfma_f32_16x16x32_bf16 v[204:207], a[204:207], v[24:27], v[204:207]\n"
    "ds_read_b128 v[244:247], v251 offset:8192\n"
    "v_fma_f32 v67, v67, s40, v232\n"
    "v_mfma_f32_16x16x32_bf16 a[124:127], a[200:203], v[32:35], 0\n"
    "v_exp_f32 v114, v114\n"
    "v_mfma_f32_16x16x32_bf16 a[124:127], a[204:207], v[40:43], a[124:127]\n"
    "v_exp_f32 v67, v67\n"
    "v_mfma_f32_16x16x32_bf16 a[172:175], a[200:203], v[48:51], 0\n"
    "v_fma_f32 v83, v83, s40, v233\n"
    "v_fma_f32 v99, v99, s40, v234\n"
    "v_mfma_f32_16x16x32_bf16 a[172:175], a[204:207], v[56:59], a[172:175]\n"
    "v_exp_f32 v83, v83\n"
    "v_mfma_f32_16x16x32_bf16 a[248:251], v[240:243], v[32:35], 0\n"
    "ds_read_b64_tr_b16 a[238:239], v227 offset:20992\n"
    "v_fma_f32 v115, v115, s40, v235\n"
    "v_mfma_f32_16x16x32_bf16 a[248:251], v[240:243], v[40:43], a[248:251]\n"
    "v_exp_f32 v99, v99\n"
    "v_mfma_f32_16x16x32_bf16 v[160:163], a[208:211], v[0:3], 0\n"
    "v_exp_f32 v115, v115\n"
    "v_mfma_f32_16x16x32_bf16 v[160:163], a[212:215], v[8:11], v[160:163]\n"
    "v_fma_f32 v68, v68, s40, v232\n"
    "v_fma_f32 v84, v84, s40, v233\n"
    "v_mfma_f32_16x16x32_bf16 v[208:211], a[208:211], v[16:19], 0\n"
    "v_exp_f32 v68, v68\n"
    "v_mfma_f32_16x16x32_bf16 v[208:211], a[212:215], v[24:27], v[208:211]\n"
    "v_exp_f32 v84, v84\n"
    "v_mfma_f32_16x16x32_bf16 a[128:131], a[208:211], v[32:35], 0\n"
    "v_fma_f32 v100, v100, s40, v234\n"
    "v_fma_f32 v116, v116, s40, v235\n"
    "v_mfma_f32_16x16x32_bf16 a[128:131], a[212:215], v[40:43], a[128:131]\n"
    "v_exp_f32 v100, v100\n"
    "v_mfma_f32_16x16x32_bf16 a[176:179], a[208:211], v[48:51], 0\n"
    "v_exp_f32 v116, v116\n"
    "v_mfma_f32_16x16x32_bf16 a[176:179], a[212:215], v[56:59], a[176:179]\n"
    "v_fma_f32 v69, v69, s40, v232\n"
    "v_fma_f32 v85, v85, s40, v233\n"
    "v_mfma_f32_16x16x32_bf16 v[164:167], a[216:219], v[0:3], 0\n"
    "v_exp_f32 v69, v69\n"
    "v_mfma_f32_16x16x32_bf16 v[164:167], a[220:223], v[8:11], v[164:167]\n"
    "s_waitcnt vmcnt(13) lgkmcnt(0)\n"
    "v_fma_f32 v101, v101, s40, v234\n"
    "v_mfma_f32_16x16x32_bf16 v[212:215], a[216:219], v[16:19], 0\n"
    "s_barrier\n"
    "v_fma_f32 v117, v117, s40, v235\n"
    "v_mfma_f32_16x16x32_bf16 v[212:215], a[220:223], v[24:27], v[212:215]\n"
    "ds_read_b128 a[192:195], v224 offset:0\n"
    "v_fma_f32 v70, v70, s40, v232\n"
    "v_mfma_f32_16x16x32_bf16 a[132:135], a[216:219], v[32:35], 0\n"
    "ds_read_b128 a[196:199], v225 offset:0\n"
    "v_fma_f32 v86, v86, s40, v233\n"
    "v_mfma_f32_16x16x32_bf16 a[132:135], a[220:223], v[40:43], a[132:135]\n"
    "ds_read_b128 a[200:203], v224 offset:8192\n"
    "v_fma_f32 v102, v102, s40, v234\n"
    "v_mfma_f32_16x16x32_bf16 a[180:183], a[216:219], v[48:51], 0\n"
    "ds_read_b128 a[204:207], v225 offset:8192\n"
    "v_fma_f32 v118, v118, s40, v235\n"
    "v_mfma_f32_16x16x32_bf16 a[180:183], a[220:223], v[56:59], a[180:183]\n"
    "buffer_store_dwordx4 v[244:247], v250, s[84:87], s78 offen offset:128 nt sc1\n"
    "v_mfma_f32_16x16x32_bf16 v[168:171], a[224:227], v[0:3], 0\n"
    "ds_read_b128 a[208:211], v224 offset:16384\n"
    "v_fma_f32 v71, v71, s40, v232\n"
    "v_mfma_f32_16x16x32_bf16 v[168:171], a[228:231], v[8:11], v[168:171]\n"
    "s_mov_b32 s45, 2\n"
    "v_fma_f32 v87, v87, s40, v233\n"
    "v_mfma_f32_16x16x32_bf16 v[216:219], a[224:227], v[16:19], 0\n"
    "v_exp_f32 v85, v85\n"
    "v_mfma_f32_16x16x32_bf16 v[216:219], a[228:231], v[24:27], v[216:219]\n"
    "ds_read_b128 a[212:215], v225 offset:16384\n"
    "v_fma_f32 v103, v103, s40, v234\n"
    "v_mfma_f32_16x16x32_bf16 a[136:139], a[224:227], v[32:35], 0\n"
    "v_exp_f32 v101, v101\n"
    "v_mfma_f32_16x16x32_bf16 a[136:139], a[228:231], v[40:43], a[136:139]\n"
    "ds_read_b128 a[216:219], v224 offset:2048\n"
    "v_fma_f32 v119, v119, s40, v235\n"
    "v_mfma_f32_16x16x32_bf16 a[184:187], a[224:227], v[48:51], 0\n"
    "v_exp_f32 v117, v117\n"
    "v_mfma_f32_16x16x32_bf16 a[184:187], a[228:231], v[56:59], a[184:187]\n"
    "ds_read_b128 a[220:223], v225 offset:2048\n"
    "v_mfma_f32_16x16x32_bf16 a[252:255], v[240:243], v[48:51], 0\n"
    "v_exp_f32 v70, v70\n"
    "v_mfma_f32_16x16x32_bf16 a[252:255], v[240:243], v[56:59], a[252:255]\n"
    "v_exp_f32 v86, v86\n"
    "v_mfma_f32_16x16x32_bf16 v[172:175], a[232:235], v[0:3], 0\n"
    "ds_read_b128 a[224:227], v224 offset:10240\n"
    "v_mfma_f32_16x16x32_bf16 v[172:175], a[236:239], v[8:11], v[172:175]\n"
    "v_exp_f32 v102, v102\n"
    "v_mfma_f32_16x16x32_bf16 v[220:223], a[232:235], v[16:19], 0\n"
    "v_exp_f32 v118, v118\n"
    "v_mfma_f32_16x16x32_bf16 v[220:223], a[236:239], v[24:27], v[220:223]\n"
    "v_exp_f32 v71, v71\n"
    "v_mfma_f32_16x16x32_bf16 a[140:143], a[232:235], v[32:35], 0\n"
    "v_exp_f32 v87, v87\n"
    "v_mfma_f32_16x16x32_bf16 a[140:143], a[236:239], v[40:43], a[140:143]\n"
    "ds_read_b128 a[228:231], v225 offset:10240\n"
    "v_mfma_f32_16x16x32_bf16 a[188:191], a[232:235], v[48:51], 0\n"
    "v_exp_f32 v103, v103\n"
    "v_mfma_f32_16x16x32_bf16 a[188:191], a[236:239], v[56:59], a[188:191]\n"
    "s_waitcnt lgkmcnt(4)\n"
    "v_exp_f32 v119, v119\n"
    "LSLOOP_%=:\n"
    "v_mfma_f32_16x16x32_bf16 v[0:3], a[192:195], a[0:3], 0\n"
    "s_cbranch_vccnz LSRBe_%=\n"
    "LSRBbe_%=:\n"
    "v_fma_f32 v72, v72, s40, v232\n"
    "v_mfma_f32_16x16x32_bf16 v[0:3], a[196:199], a[4:7], v[0:3]\n"
    "s_mov_b32 m0, s58\n"
    "v_fma_f32 v88, v88, s40, v233\n"
    "v_mfma_f32_16x16x32_bf16 v[0:3], a[200:203], a[8:11], v[0:3]\n"
    "v_exp_f32 v72, v72\n"
    "v_mfma_f32_16x16x32_bf16 v[0:3], a[204:207], a[12:15], v[0:3]\n"
    "buffer_load_dwordx4 v230, s[24:27], s37 offen offset:0 nt lds\n"
    "v_mfma_f32_16x16x32_bf16 v[0:3], a[208:211], a[16:19], v[0:3]\n"
    "ds_read_b128 a[232:235], v224 offset:18432\n"
    "v_cvt_pk_bf16_f32 v64, v64, v65\n"
    "v_mfma_f32_16x16x32_bf16 v[0:3], a[212:215], a[20:23], v[0:3]\n"
    "v_exp_f32 v88, v88\n"
    "v_mfma_f32_16x16x32_bf16 v[16:19], a[192:195], a[24:27], 0\n"
    "v_fma_f32 v104, v104, s40, v234\n"
    "v_fma_f32 v120, v120, s40, v235\n"
    "v_mfma_f32_16x16x32_bf16 v[16:19], a[196:199], a[28:31], v[16:19]\n"
    "v_exp_f32 v104, v104\n"
    "v_mfma_f32_16x16x32_bf16 v[16:19], a[200:203], a[32:35], v[16:19]\n"
    "buffer_load_dwordx4 v230, s[24:27], s37 offen offset:3072 nt lds\n"
    "v_mfma_f32_16x16x32_bf16 v[16:19], a[204:207], a[36:39], v[16:19]\n"
    "ds_read_b128 a[236:239], v225 offset:18432\n"
    "v_cvt_pk_bf16_f32 v80, v80, v81\n"
    "v_mfma_f32_16x16x32_bf16 v[16:19], a[208:211], a[40:43], v[16:19]\n"
    "v_exp_f32 v120, v120\n"
    "v_mfma_f32_16x16x32_bf16 v[16:19], a[212:215], a[44:47], v[16:19]\n"
    "s_mov_b32 m0, s59\n"
    "v_fma_f32 v73, v73, s40, v232\n"
    "v_mfma_f32_16x16x32_bf16 v[32:35], a[192:195], a[48:51], 0\n"
    "v_exp_f32 v73, v73\n"
    "v_mfma_f32_16x16x32_bf16 v[32:35], a[196:199], a[52:55], v[32:35]\n"
    "buffer_load_dwordx4 v230, s[24:27], s37 offen offset:128 nt lds\n"
    "v_mfma_f32_16x16x32_bf16 v[32:35], a[200:203], a[56:59], v[32:35]\n"
    "v_fma_f32 v89, v89, s40, v233\n"
    "v_cvt_pk_bf16_f32 v96, v96, v97\n"
    "v_mfma_f32_16x16x32_bf16 v[32:35], a[204:207], a[60:63], v[32:35]\n"
    "v_exp_f32 v89, v89\n"
    "v_mfma_f32_16x16x32_bf16 v[32:35], a[208:211], a[64:67], v[32:35]\n"
    "v_fma_f32 v105, v105, s40, v234\n"
    "v_cvt_pk_bf16_f32 v112, v112, v113\n"
    "v_mfma_f32_16x16x32_bf16 v[32:35], a[212:215], a[68:71], v[32:35]\n"
    "v_exp_f32 v105, v105\n"
    "v_mfma_f32_16x16x32_bf16 v[48:51], a[192:195], a[72:75], 0\n"
    "buffer_load_dwordx4 v230, s[24:27], s37 offen offset:3200 nt lds\n"
    "v_mfma_f32_16x16x32_bf16 v[48:51], a[196:199], a[76:79], v[48:51]\n"
    "ds_read_b128 a[192:195], v224 offset:4096\n"
    "v_fma_f32 v121, v121, s40, v235\n"
    "v_mfma_f32_16x16x32_bf16 v[48:51], a[200:203], a[80:83], v[48:51]\n"
    "v_exp_f32 v121, v121\n"
    "v_mfma_f32_16x16x32_bf16 v[48:51], a[204:207], a[84:87], v[48:51]\n"
    "s_mov_b32 m0, s60\n"
    "v_cvt_pk_bf16_f32 v65, v66, v67\n"
    "v_mfma_f32_16x16x32_bf16 v[48:51], a[208:211], a[88:91], v[48:51]\n"
    "ds_read_b128 a[196:199], v225 offset:4096\n"
    "v_fma_f32 v74, v74, s40, v232\n"
    "v_mfma_f32_16x16x32_bf16 v[48:51], a[212:215], a[92:95], v[48:51]\n"
    "s_waitcnt lgkmcnt(2)\n"
    "v_cvt_pk_bf16_f32 v81, v82, v83\n"
    "v_mfma_f32_16x16x32_bf16 v[4:7], a[216:219], a[0:3], 0\n"
    "buffer_load_dwordx4 v230, s[24:27], s37 offen offset:256 nt lds\n"
    "v_mfma_f32_16x16x32_bf16 v[4:7], a[220:223], a[4:7], v[4:7]\n"
    "ds_read_b128 a[200:203], v224 offset:12288\n"
    "v_fma_f32 v90, v90, s40, v233\n"
    "v_mfma_f32_16x16x32_bf16 v[4:7], a[224:227], a[8:11], v[4:7]\n"
    "v_exp_f32 v74, v74\n"
    "v_mfma_f32_16x16x32_bf16 v[4:7], a[228:231], a[12:15], v[4:7]\n"
    "ds_read_b128 a[204:207], v225 offset:12288\n"
    "v_cvt_pk_bf16_f32 v97, v98, v99\n"
    "v_mfma_f32_16x16x32_bf16 v[4:7], a[232:235], a[16:19], v[4:7]\n"
    "v_exp_f32 v90, v90\n"
    "v_mfma_f32_16x16x32_bf16 v[4:7], a[236:239], a[20:23], v[4:7]\n"
    "buffer_load_dwordx4 v230, s[24:27], s37 offen offset:3328 nt lds\n"
    "v_mfma_f32_16x16x32_bf16 v[20:23], a[216:219], a[24:27], 0\n"
    "s_add_u32 s37, s37, 24576\n"
    "v_fma_f32 v106, v106, s40, v234\n"
    "v_mfma_f32_16x16x32_bf16 v[20:23], a[220:223], a[28:31], v[20:23]\n"
    "ds_read_b128 a[208:211], v224 offset:20480\n"
    "v_cvt_pk_bf16_f32 v113, v114, v115\n"
    "v_mfma_f32_16x16x32_bf16 v[20:23], a[224:227], a[32:35], v[20:23]\n"
    "ds_read_b128 v[244:247], v251 offset:12288\n"
    "v_fma_f32 v122, v122, s40, v235\n"
    "v_mfma_f32_16x16x32_bf16 v[20:23], a[228:231], a[36:39], v[20:23]\n"
    "ds_read_b128 a[212:215], v225 offset:20480\n"
    "v_cvt_pk_bf16_f32 v72, v72, v73\n"
    "v_mfma_f32_16x16x32_bf16 v[20:23], a[232:235], a[40:43], v[20:23]\n"
    "v_exp_f32 v106, v106\n"
    "v_mfma_f32_16x16x32_bf16 v[20:23], a[236:239], a[44:47], v[20:23]\n"
    "v_exp_f32 v122, v122\n"
    "v_mfma_f32_16x16x32_bf16 v[36:39], a[216:219], a[48:51], 0\n"
    "v_cvt_pk_bf16_f32 v66, v68, v69\n"
    "v_fma_f32 v75, v75, s40, v232\n"
    "v_mfma_f32_16x16x32_bf16 v[36:39], a[220:223], a[52:55], v[36:39]\n"
    "v_exp_f32 v75, v75\n"
    "v_mfma_f32_16x16x32_bf16 v[36:39], a[224:227], a[56:59], v[36:39]\n"
    "s_add_u32 m0, s39, 0\n"
    "v_cvt_pk_bf16_f32 v82, v84, v85\n"
    "v_mfma_f32_16x16x32_bf16 v[36:39], a[228:231], a[60:63], v[36:39]\n"
    "v_cvt_pk_bf16_f32 v88, v88, v89\n"
    "v_cvt_pk_bf16_f32 v98, v100, v101\n"
    "v_mfma_f32_16x16x32_bf16 v[36:39], a[232:235], a[64:67], v[36:39]\n"
    "buffer_load_dwordx4 v228, s[28:31], s38 offen offset:0 nt lds\n"
    "v_mfma_f32_16x16x32_bf16 v[36:39], a[236:239], a[68:71], v[36:39]\n"
    "v_fma_f32 v91, v91, s40, v233\n"
    "v_cvt_pk_bf16_f32 v104, v104, v105\n"
    "v_mfma_f32_16x16x32_bf16 v[52:55], a[216:219], a[72:75], 0\n"
    "v_exp_f32 v91, v91\n"
    "v_mfma_f32_16x16x32_bf16 v[52:55], a[220:223], a[76:79], v[52:55]\n"
    "ds_read_b128 a[216:219], v224 offset:6144\n"
    "v_cvt_pk_bf16_f32 v114, v116, v117\n"
    "v_mfma_f32_16x16x32_bf16 v[52:55], a[224:227], a[80:83], v[52:55]\n"
    "ds_read_b128 a[220:223], v225 offset:6144\n"
    "v_fma_f32 v107, v107, s40, v234\n"
    "v_mfma_f32_16x16x32_bf16 v[52:55], a[228:231], a[84:87], v[52:55]\n"
    "buffer_load_dwordx4 v229, s[28:31], s38 offen offset:1024 nt lds\n"
    "v_mfma_f32_16x16x32_bf16 v[52:55], a[232:235], a[88:91], v[52:55]\n"
    "ds_read_b128 a[224:227], v224 offset:14336\n"
    "v_cvt_pk_bf16_f32 v67, v70, v71\n"
    "v_mfma_f32_16x16x32_bf16 v[52:55], a[236:239], a[92:95], v[52:55]\n"
    "s_waitcnt lgkmcnt(3)\n"
    "v_cvt_pk_bf16_f32 v120, v120, v121\n"
    "v_mfma_f32_16x16x32_bf16 v[8:11], a[192:195], a[0:3], 0\n"
    "ds_read_b128 a[228:231], v225 offset:14336\n"
    "v_cvt_pk_bf16_f32 v83, v86, v87\n"
    "v_mfma_f32_16x16x32_bf16 v[8:11], a[196:199], a[4:7], v[8:11]\n"
    "buffer_store_dwordx4 v[244:247], v250, s[84:87], s79 offen offset:128 nt sc1\n"
    "v_mfma_f32_16x16x32_bf16 v[8:11], a[200:203], a[8:11], v[8:11]\n"
    "ds_read_b128 a[232:235], v224 offset:22528\n"
    "v_fma_f32 v123, v123, s40, v235\n"
    "v_mfma_f32_16x16x32_bf16 v[8:11], a[204:207], a[12:15], v[8:11]\n"
    "v_exp_f32 v107, v107\n"
    "v_mfma_f32_16x16x32_bf16 v[8:11], a[208:211], a[16:19], v[8:11]\n"
    "v_exp_f32 v123, v123\n"
    "v_mfma_f32_16x16x32_bf16 v[8:11], a[212:215], a[20:23], v[8:11]\n"
    "v_cvt_pk_bf16_f32 v99, v102, v103\n"
    "v_fma_f32 v76, v76, s40, v232\n"
    "v_mfma_f32_16x16x32_bf16 v[24:27], a[192:195], a[24:27], 0\n"
    "ds_read_b128 a[236:239], v225 offset:22528\n"
    "v_cvt_pk_bf16_f32 v115, v118, v119\n"
    "v_mfma_f32_16x16x32_bf16 v[24:27], a[196:199], a[28:31], v[24:27]\n"
    "v_exp_f32 v76, v76\n"
    "v_mfma_f32_16x16x32_bf16 v[24:27], a[200:203], a[32:35], v[24:27]\n"
    "v_fma_f32 v92, v92, s40, v233\n"
    "v_fma_f32 v108, v108, s40, v234\n"
    "v_mfma_f32_16x16x32_bf16 v[24:27], a[204:207], a[36:39], v[24:27]\n"
    "v_exp_f32 v92, v92\n"
    "v_mfma_f32_16x16x32_bf16 v[24:27], a[208:211], a[40:43], v[24:27]\n"
    "v_exp_f32 v108, v108\n"
    "v_mfma_f32_16x16x32_bf16 v[24:27], a[212:215], a[44:47], v[24:27]\n"
    "v_fma_f32 v124, v124, s40, v235\n"
    "v_cvt_pk_bf16_f32 v73, v74, v75\n"
    "v_mfma_f32_16x16x32_bf16 v[40:43], a[192:195], a[48:51], 0\n"
    "v_exp_f32 v124, v124\n"
    "v_mfma_f32_16x16x32_bf16 v[40:43], a[196:199], a[52:55], v[40:43]\n"
    "v_fma_f32 v77, v77, s40, v232\n"
    "v_cvt_pk_bf16_f32 v89, v90, v91\n"
    "v_mfma_f32_16x16x32_bf16 v[40:43], a[200:203], a[56:59], v[40:43]\n"
    "v_exp_f32 v77, v77\n"
    "v_mfma_f32_16x16x32_bf16 v[40:43], a[204:207], a[60:63], v[40:43]\n"
    "v_fma_f32 v93, v93, s40, v233\n"
    "v_cvt_pk_bf16_f32 v105, v106, v107\n"
    "v_mfma_f32_16x16x32_bf16 v[40:43], a[208:211], a[64:67], v[40:43]\n"
    "v_exp_f32 v93, v93\n"
    "v_mfma_f32_16x16x32_bf16 v[40:43], a[212:215], a[68:71], v[40:43]\n"
    "s_waitcnt vmcnt(19) lgkmcnt(0)\n"
    "v_fma_f32 v109, v109, s40, v234\n"
    "v_mfma_f32_16x16x32_bf16 v[56:59], a[192:195], a[72:75], 0\n"
    "s_barrier\n"
    "v_cvt_pk_bf16_f32 v121, v122, v123\n"
    "v_mfma_f32_16x16x32_bf16 v[56:59], a[196:199], a[76:79], v[56:59]\n"
    "ds_read_b64_tr_b16 a[192:193], v226 offset:24576\n"
    "v_fma_f32 v125, v125, s40, v235\n"
    "v_mfma_f32_16x16x32_bf16 v[56:59], a[200:203], a[80:83], v[56:59]\n"
    "ds_read_b64_tr_b16 a[194:195], v226 offset:30720\n"
    "v_fma_f32 v78, v78, s40, v232\n"
    "v_mfma_f32_16x16x32_bf16 v[56:59], a[204:207], a[84:87], v[56:59]\n"
    "ds_read_b64_tr_b16 a[196:197], v226 offset:36864\n"
    "v_fma_f32 v94, v94, s40, v233\n"
    "v_mfma_f32_16x16x32_bf16 v[56:59], a[208:211], a[88:91], v[56:59]\n"
    "ds_read_b64_tr_b16 a[198:199], v226 offset:43008\n"
    "v_fma_f32 v110, v110, s40, v234\n"
    "v_mfma_f32_16x16x32_bf16 v[56:59], a[212:215], a[92:95], v[56:59]\n"
    "ds_read_b64_tr_b16 a[200:201], v227 offset:24576\n"
    "v_fma_f32 v126, v126, s40, v235\n"
    "v_mfma_f32_16x16x32_bf16 v[12:15], a[216:219], a[0:3], 0\n"
    "ds_read_b64_tr_b16 a[202:203], v227 offset:30720\n"
    "v_cvt_pk_bf16_f32 v74, v76, v77\n"
    "v_mfma_f32_16x16x32_bf16 v[12:15], a[220:223], a[4:7], v[12:15]\n"
    "ds_read_b64_tr_b16 a[204:205], v227 offset:36864\n"
    "v_fma_f32 v79, v79, s40, v232\n"
    "v_mfma_f32_16x16x32_bf16 v[12:15], a[224:227], a[8:11], v[12:15]\n"
    "ds_read_b64_tr_b16 a[206:207], v227 offset:43008\n"
    "v_cvt_pk_bf16_f32 v90, v92, v93\n"
    "v_mfma_f32_16x16x32_bf16 v[12:15], a[228:231], a[12:15], v[12:15]\n"
    "ds_read_b64_tr_b16 a[208:209], v226 offset:25088\n"
    "v_fma_f32 v95, v95, s40, v233\n"
    "v_mfma_f32_16x16x32_bf16 v[12:15], a[232:235], a[16:19], v[12:15]\n"
    "v_exp_f32 v109, v109\n"
    "v_mfma_f32_16x16x32_bf16 v[12:15], a[236:239], a[20:23], v[12:15]\n"
    "ds_read_b64_tr_b16 a[210:211], v226 offset:31232\n"
    "v_cvt_pk_bf16_f32 v106, v108, v109\n"
    "v_mfma_f32_16x16x32_bf16 v[28:31], a[216:219], a[24:27], 0\n"
    "v_exp_f32 v125, v125\n"
    "v_mfma_f32_16x16x32_bf16 v[28:31], a[220:223], a[28:31], v[28:31]\n"
    "v_exp_f32 v78, v78\n"
    "v_mfma_f32_16x16x32_bf16 v[28:31], a[224:227], a[32:35], v[28:31]\n"
    "ds_read_b64_tr_b16 a[212:213], v226 offset:37376\n"
    "v_fma_f32 v111, v111, s40, v234\n"
    "v_mfma_f32_16x16x32_bf16 v[28:31], a[228:231], a[36:39], v[28:31]\n"
    "v_exp_f32 v94, v94\n"
    "v_mfma_f32_16x16x32_bf16 v[28:31], a[232:235], a[40:43], v[28:31]\n"
    "v_exp_f32 v110, v110\n"
    "v_mfma_f32_16x16x32_bf16 v[28:31], a[236:239], a[44:47], v[28:31]\n"
    "v_exp_f32 v126, v126\n"
    "v_mfma_f32_16x16x32_bf16 v[44:47], a[216:219], a[48:51], 0\n"
    "ds_read_b64_tr_b16 a[214:215], v226 offset:43520\n"
    "v_cvt_pk_bf16_f32 v122, v124, v125\n"
    "v_mfma_f32_16x16x32_bf16 v[44:47], a[220:223], a[52:55], v[44:47]\n"
    "v_exp_f32 v79, v79\n"
    "v_mfma_f32_16x16x32_bf16 v[44:47], a[224:227], a[56:59], v[44:47]\n"
    "v_exp_f32 v95, v95\n"
    "v_mfma_f32_16x16x32_bf16 v[44:47], a[228:231], a[60:63], v[44:47]\n"
    "v_exp_f32 v111, v111\n"
    "v_mfma_f32_16x16x32_bf16 v[44:47], a[232:235], a[64:67], v[44:47]\n"
    "v_fma_f32 v127, v127, s40, v235\n"
    "v_cvt_pk_bf16_f32 v75, v78, v79\n"
    "v_mfma_f32_16x16x32_bf16 v[44:47], a[236:239], a[68:71], v[44:47]\n"
    "v_exp_f32 v127, v127\n"
    "v_mfma_f32_16x16x32_bf16 v[60:63], a[216:219], a[72:75], 0\n"
    "v_cvt_pk_bf16_f32 v91, v94, v95\n"
    "v_cvt_pk_bf16_f32 v107, v110, v111\n"
    "v_mfma_f32_16x16x32_bf16 v[60:63], a[220:223], a[76:79], v[60:63]\n"
    "v_cvt_pk_bf16_f32 v123, v126, v127\n"
    "v_mfma_f32_16x16x32_bf16 v[60:63], a[224:227], a[80:83], v[60:63]\n"
    "v_mfma_f32_16x16x32_bf16 v[60:63], a[228:231], a[84:87], v[60:63]\n"
    "v_mfma_f32_16x16x32_bf16 v[60:63], a[232:235], a[88:91], v[60:63]\n"
    "v_mfma_f32_16x16x32_bf16 v[60:63], a[236:239], a[92:95], v[60:63]\n"
    "s_waitcnt lgkmcnt(0)\n"
    "v_mfma_f32_16x16x32_bf16 v[128:131], a[192:195], v[64:67], v[128:131]\n"
    "ds_read_b64_tr_b16 a[216:217], v227 offset:25088\n"
    "v_max3_f32 v236, v0, v1, v2\n"
    "v_mfma_f32_16x16x32_bf16 v[128:131], a[196:199], v[72:75], v[128:131]\n"
    "s_mov_b32 m0, s52\n"
    "v_max3_f32 v237, v16, v17, v18\n"
    "v_mfma_f32_16x16x32_bf16 v[176:179], a[192:195], v[80:83], v[176:179]\n"
    "ds_read_b64_tr_b16 a[218:219], v227 offset:31232\n"
    "v_max3_f32 v238, v32, v33, v34\n"
    "v_mfma_f32_16x16x32_bf16 v[176:179], a[196:199], v[88:91], v[176:179]\n"
    "buffer_load_dwordx4 v228, s[20:23], s36 offen offset:0 nt lds\n"
    "v_mfma_f32_16x16x32_bf16 a[96:99], a[192:195], v[96:99], a[96:99]\n"
    "ds_read_b64_tr_b16 a[220:221], v227 offset:37376\n"
    "v_max3_f32 v239, v48, v49, v50\n"
    "v_mfma_f32_16x16x32_bf16 a[96:99], a[196:199], v[104:107], a[96:99]\n"
    "ds_read_b64_tr_b16 a[222:223], v227 offset:43520\n"
    "v_max3_f32 v236, v236, v3, v4\n"
    "v_mfma_f32_16x16x32_bf16 a[144:147], a[192:195], v[112:115], a[144:147]\n"
    "ds_read_b64_tr_b16 a[224:225], v226 offset:25600\n"
    "v_max3_f32 v237, v237, v19, v20\n"
    "v_mfma_f32_16x16x32_bf16 a[144:147], a[196:199], v[120:123], a[144:147]\n"
    "ds_read_b64_tr_b16 a[226:227], v226 offset:31744\n"
    "v_max3_f32 v238, v238, v35, v36\n"
    "v_mfma_f32_16x16x32_bf16 v[132:135], a[200:203], v[64:67], v[132:135]\n"
    "buffer_load_dwordx4 v229, s[20:23], s36 offen offset:1024 nt lds\n"
    "v_mfma_f32_16x16x32_bf16 v[132:135], a[204:207], v[72:75], v[132:135]\n"
    "ds_read_b64_tr_b16 a[228:229], v226 offset:37888\n"
    "v_max3_f32 v239, v239, v51, v52\n"
    "v_mfma_f32_16x16x32_bf16 v[180:183], a[200:203], v[80:83], v[180:183]\n"
    "ds_read_b64_tr_b16 a[230:231], v226 offset:44032\n"
    "v_max3_f32 v236, v236, v5, v6\n"
    "v_mfma_f32_16x16x32_bf16 v[180:183], a[204:207], v[88:91], v[180:183]\n"
    "s_mov_b32 m0, s53\n"
    "v_max3_f32 v237, v237, v21, v22\n"
    "v_mfma_f32_16x16x32_bf16 a[100:103], a[200:203], v[96:99], a[100:103]\n"
    "ds_read_b64_tr_b16 a[232:233], v227 offset:25600\n"
    "v_max3_f32 v238, v238, v37, v38\n"
    "v_mfma_f32_16x16x32_bf16 a[100:103], a[204:207], v[104:107], a[100:103]\n"
    "buffer_load_dwordx4 v228, s[20:23], s36 offen offset:128 nt lds\n"
    "ds_read_b64_tr_b16 a[234:235], v227 offset:31744\n"
    "v_mfma_f32_16x16x32_bf16 a[148:151], a[200:203], v[112:115], a[148:151]\n"
    "ds_read_b64_tr_b16 a[236:237], v227 offset:37888\n"
    "v_max3_f32 v239, v239, v53, v54\n"
    "v_mfma_f32_16x16x32_bf16 a[148:151], a[204:207], v[120:123], a[148:151]\n"
    "ds_read_b64_tr_b16 a[238:239], v227 offset:44032\n"
    "v_max3_f32 v236, v236, v7, v8\n"
    "v_mfma_f32_16x16x32_bf16 a[240:243], v[240:243], v[64:67], a[240:243]\n"
    "ds_read_b64_tr_b16 a[192:193], v226 offset:26112\n"
    "v_max3_f32 v237, v237, v23, v24\n"
    "v_mfma_f32_16x16x32_bf16 a[240:243], v[240:243], v[72:75], a[240:243]\n"
    "ds_read_b64_tr_b16 a[194:195], v226 offset:32256\n"
    "v_max3_f32 v238, v238, v39, v40\n"
    "v_mfma_f32_16x16x32_bf16 v[136:139], a[208:211], v[64:67], v[136:139]\n"
    "buffer_load_dwordx4 v229, s[20:23], s36 offen offset:1152 nt lds\n"
    "v_mfma_f32_16x16x32_bf16 v[136:139], a[212:215], v[72:75], v[136:139]\n"
    "ds_read_b64_tr_b16 a[196:197], v226 offset:38400\n"
    "v_max3_f32 v239, v239, v55, v56\n"
    "v_mfma_f32_16x16x32_bf16 v[184:187], a[208:211], v[80:83], v[184:187]\n"
    "ds_read_b64_tr_b16 a[198:199], v226 offset:44544\n"
    "v_max3_f32 v236, v236, v9, v10\n"
    "v_mfma_f32_16x16x32_bf16 v[184:187], a[212:215], v[88:91], v[184:187]\n"
    "s_mov_b32 m0, s54\n"
    "v_max3_f32 v237, v237, v25, v26\n"
    "v_mfma_f32_16x16x32_bf16 a[104:107], a[208:211], v[96:99], a[104:107]\n"
    "ds_read_b64_tr_b16 a[200:201], v227 offset:26112\n"
    "v_max3_f32 v238, v238, v41, v42\n"
    "v_mfma_f32_16x16x32_bf16 a[104:107], a[212:215], v[104:107], a[104:107]\n"
    "buffer_load_dwordx4 v228, s[20:23], s36 offen offset:256 nt lds\n"
    "v_mfma_f32_16x16x32_bf16 a[152:155], a[208:211], v[112:115], a[152:155]\n"
    "ds_read_b64_tr_b16 a[202:203], v227 offset:32256\n"
    "v_max3_f32 v239, v239, v57, v58\n"
    "v_mfma_f32_16x16x32_bf16 a[152:155], a[212:215], v[120:123], a[152:155]\n"
    "s_waitcnt lgkmcnt(6)\n"
    "v_max3_f32 v236, v236, v11, v12\n"
    "v_mfma_f32_16x16x32_bf16 v[140:143], a[216:219], v[64:67], v[140:143]\n"
    "ds_read_b64_tr_b16 a[204:205], v227 offset:38400\n"
    "v_max3_f32 v237, v237, v27, v28\n"
    "v_mfma_f32_16x16x32_bf16 v[140:143], a[220:223], v[72:75], v[140:143]\n"
    "ds_read_b64_tr_b16 a[206:207], v227 offset:44544\n"
    "v_max3_f32 v238, v238, v43, v44\n"
    "v_mfma_f32_16x16x32_bf16 v[188:191], a[216:219], v[80:83], v[188:191]\n"
    "buffer_load_dwordx4 v229, s[20:23], s36 offen offset:1280 nt lds\n"
    "v_mfma_f32_16x16x32_bf16 v[188:191], a[220:223], v[88:91], v[188:191]\n"
    "s_add_u32 s36, s36, 24576\n"
    "v_max3_f32 v239, v239, v59, v60\n"
    "v_mfma_f32_16x16x32_bf16 a[108:111], a[216:219], v[96:99], a[108:111]\n"
    "ds_read_b64_tr_b16 a[208:209], v226 offset:26624\n"
    "v_max3_f32 v236, v236, v13, v14\n"
    "v_mfma_f32_16x16x32_bf16 a[108:111], a[220:223], v[104:107], a[108:111]\n"
    "ds_read_b64_tr_b16 a[210:211], v226 offset:32768\n"
    "v_max3_f32 v237, v237, v29, v30\n"
    "v_mfma_f32_16x16x32_bf16 a[156:159], a[216:219], v[112:115], a[156:159]\n"
    "ds_read_b64_tr_b16 a[212:213], v226 offset:38912\n"
    "v_max3_f32 v238, v238, v45, v46\n"
    "v_mfma_f32_16x16x32_bf16 a[156:159], a[220:223], v[120:123], a[156:159]\n"
    "ds_read_b64_tr_b16 a[214:215], v226 offset:45056\n"
    "v_max3_f32 v239, v239, v61, v62\n"
    "v_mfma_f32_16x16x32_bf16 v[144:147], a[224:227], v[64:67], v[144:147]\n"
    "ds_read_b64_tr_b16 a[216:217], v227 offset:26624\n"
    "v_max_f32 v236, v236, v15\n"
    "v_mfma_f32_16x16x32_bf16 v[144:147], a[228:231], v[72:75], v[144:147]\n"
    "ds_read_b64_tr_b16 a[218:219], v227 offset:32768\n"
    "v_max_f32 v237, v237, v31\n"
    "v_mfma_f32_16x16x32_bf16 v[192:195], a[224:227], v[80:83], v[192:195]\n"
    "ds_read_b64_tr_b16 a[220:221], v227 offset:38912\n"
    "v_max_f32 v238, v238, v47\n"
    "v_mfma_f32_16x16x32_bf16 v[192:195], a[228:231], v[88:91], v[192:195]\n"
    "v_max_f32 v239, v239, v63\n"
    "v_fma_f32 v236, v236, s40, v232\n"
    "v_mfma_f32_16x16x32_bf16 a[112:115], a[224:227], v[96:99], a[112:115]\n"
    "ds_read_b64_tr_b16 a[222:223], v227 offset:45056\n"
    "v_fma_f32 v237, v237, s40, v233\n"
    "v_mfma_f32_16x16x32_bf16 a[112:115], a[228:231], v[104:107], a[112:115]\n"
    "v_fma_f32 v238, v238, s40, v234\n"
    "v_fma_f32 v239, v239, s40, v235\n"
    "v_mfma_f32_16x16x32_bf16 a[160:163], a[224:227], v[112:115], a[160:163]\n"
    "v_max3_f32 v231, v236, v237, v238\n"
    "v_max_f32 v231, v231, v239\n"
    "v_mfma_f32_16x16x32_bf16 a[160:163], a[228:231], v[120:123], a[160:163]\n"
    "v_cmp_lt_f32 vcc, s41, v231\n"
    "s_cbranch_vccnz LSRAe_%=\n"
    "LSRAbe_%=:\n"
    "v_mfma_f32_16x16x32_bf16 a[244:247], v[240:243], v[80:83], a[244:247]\n"
    "ds_read_b64_tr_b16 a[224:225], v226 offset:27136\n"
    "v_fma_f32 v0, v0, s40, v232\n"
    "v_mfma_f32_16x16x32_bf16 a[244:247], v[240:243], v[88:91], a[244:247]\n"
    "v_exp_f32 v0, v0\n"
    "v_mfma_f32_16x16x32_bf16 v[148:151], a[232:235], v[64:67], v[148:151]\n"
    "ds_read_b64_tr_b16 a[226:227], v226 offset:33280\n"
    "v_fma_f32 v16, v16, s40, v233\n"
    "v_mfma_f32_16x16x32_bf16 v[148:151], a[236:239], v[72:75], v[148:151]\n"
    "v_exp_f32 v16, v16\n"
    "v_mfma_f32_16x16x32_bf16 v[196:199], a[232:235], v[80:83], v[196:199]\n"
    "v_fma_f32 v32, v32, s40, v234\n"
    "v_fma_f32 v48, v48, s40, v235\n"
    "v_mfma_f32_16x16x32_bf16 v[196:199], a[236:239], v[88:91], v[196:199]\n"
    "ds_read_b64_tr_b16 a[228:229], v226 offset:39424\n"
    "v_fma_f32 v1, v1, s40, v232\n"
    "v_mfma_f32_16x16x32_bf16 a[116:119], a[232:235], v[96:99], a[116:119]\n"
    "v_exp_f32 v32, v32\n"
    "v_mfma_f32_16x16x32_bf16 a[116:119], a[236:239], v[104:107], a[116:119]\n"
    "v_exp_f32 v48, v48\n"
    "v_mfma_f32_16x16x32_bf16 a[164:167], a[232:235], v[112:115], a[164:167]\n"
    "ds_read_b64_tr_b16 a[230:231], v226 offset:45568\n"
    "v_fma_f32 v17, v17, s40, v233\n"
    "v_mfma_f32_16x16x32_bf16 a[164:167], a[236:239], v[120:123], a[164:167]\n"
    "s_waitcnt lgkmcnt(4)\n"
    "v_fma_f32 v33, v33, s40, v234\n"
    "v_mfma_f32_16x16x32_bf16 v[152:155], a[192:195], v[64:67], v[152:155]\n"
    "v_exp_f32 v1, v1\n"
    "v_mfma_f32_16x16x32_bf16 v[152:155], a[196:199], v[72:75], v[152:155]\n"
    "ds_read_b64_tr_b16 a[232:233], v227 offset:27136\n"
    "v_fma_f32 v49, v49, s40, v235\n"
    "v_mfma_f32_16x16x32_bf16 v[200:203], a[192:195], v[80:83], v[200:203]\n"
    "v_exp_f32 v17, v17\n"
    "v_mfma_f32_16x16x32_bf16 v[200:203], a[196:199], v[88:91], v[200:203]\n"
    "v_exp_f32 v33, v33\n"
    "v_mfma_f32_16x16x32_bf16 a[120:123], a[192:195], v[96:99], a[120:123]\n"
    "v_exp_f32 v49, v49\n"
    "v_mfma_f32_16x16x32_bf16 a[120:123], a[196:199], v[104:107], a[120:123]\n"
    "ds_read_b64_tr_b16 a[234:235], v227 offset:33280\n"
    "v_fma_f32 v2, v2, s40, v232\n"
    "v_mfma_f32_16x16x32_bf16 a[168:171], a[192:195], v[112:115], a[168:171]\n"
    "v_exp_f32 v2, v2\n"
    "v_mfma_f32_16x16x32_bf16 a[168:171], a[196:199], v[120:123], a[168:171]\n"
    "v_fma_f32 v18, v18, s40, v233\n"
    "v_fma_f32 v34, v34, s40, v234\n"
    "v_mfma_f32_16x16x32_bf16 v[156:159], a[200:203], v[64:67], v[156:159]\n"
    "v_exp_f32 v18, v18\n"
    "v_mfma_f32_16x16x32_bf16 v[156:159], a[204:207], v[72:75], v[156:159]\n"
    "v_exp_f32 v34, v34\n"
    "v_mfma_f32_16x16x32_bf16 v[204:207], a[200:203], v[80:83], v[204:207]\n"
    "ds_read_b64_tr_b16 a[236:237], v227 offset:39424\n"
    "v_fma_f32 v50, v50, s40, v235\n"
    "v_mfma_f32_16x16x32_bf16 v[204:207], a[204:207], v[88:91], v[204:207]\n"
    "ds_read_b128 v[244:247], v251 offset:0\n"
    "v_fma_f32 v3, v3, s40, v232\n"
    "v_mfma_f32_16x16x32_bf16 a[124:127], a[200:203], v[96:99], a[124:127]\n"
    "v_exp_f32 v50, v50\n"
    "v_mfma_f32_16x16x32_bf16 a[124:127], a[204:207], v[104:107], a[124:127]\n"
    "v_exp_f32 v3, v3\n"
    "v_mfma_f32_16x16x32_bf16 a[172:175], a[200:203], v[112:115], a[172:175]\n"
    "v_fma_f32 v19, v19, s40, v233\n"
    "v_fma_f32 v35, v35, s40, v234\n"
    "v_mfma_f32_16x16x32_bf16 a[172:175], a[204:207], v[120:123], a[172:175]\n"
    "v_exp_f32 v19, v19\n"
    "v_mfma_f32_16x16x32_bf16 a[248:251], v[240:243], v[96:99], a[248:251]\n"
    "ds_read_b64_tr_b16 a[238:239], v227 offset:45568\n"
    "v_fma_f32 v51, v51, s40, v235\n"
    "v_mfma_f32_16x16x32_bf16 a[248:251], v[240:243], v[104:107], a[248:251]\n"
    "v_exp_f32 v35, v35\n"
    "v_mfma_f32_16x16x32_bf16 v[160:163], a[208:211], v[64:67], v[160:163]\n"
    "v_exp_f32 v51, v51\n"
    "v_mfma_f32_16x16x32_bf16 v[160:163], a[212:215], v[72:75], v[160:163]\n"
    "v_fma_f32 v4, v4, s40, v232\n"
    "v_fma_f32 v20, v20, s40, v233\n"
    "v_mfma_f32_16x16x32_bf16 v[208:211], a[208:211], v[80:83], v[208:211]\n"
    "v_exp_f32 v4, v4\n"
    "v_mfma_f32_16x16x32_bf16 v[208:211], a[212:215], v[88:91], v[208:211]\n"
    "v_exp_f32 v20, v20\n"
    "v_mfma_f32_16x16x32_bf16 a[128:131], a[208:211], v[96:99], a[128:131]\n"
    "v_fma_f32 v36, v36, s40, v234\n"
    "v_fma_f32 v52, v52, s40, v235\n"
    "v_mfma_f32_16x16x32_bf16 a[128:131], a[212:215], v[104:107], a[128:131]\n"
    "v_exp_f32 v36, v36\n"
    "v_mfma_f32_16x16x32_bf16 a[176:179], a[208:211], v[112:115], a[176:179]\n"
    "v_exp_f32 v52, v52\n"
    "v_mfma_f32_16x16x32_bf16 a[176:179], a[212:215], v[120:123], a[176:179]\n"
    "v_fma_f32 v5, v5, s40, v232\n"
    "v_fma_f32 v21, v21, s40, v233\n"
    "v_mfma_f32_16x16x32_bf16 v[164:167], a[216:219], v[64:67], v[164:167]\n"
    "v_exp_f32 v5, v5\n"
    "v_mfma_f32_16x16x32_bf16 v[164:167], a[220:223], v[72:75], v[164:167]\n"
    "s_waitcnt vmcnt(16) lgkmcnt(0)\n"
    "v_fma_f32 v37, v37, s40, v234\n"
    "v_mfma_f32_16x16x32_bf16 v[212:215], a[216:219], v[80:83], v[212:215]\n"
    "s_barrier\n"
    "v_fma_f32 v53, v53, s40, v235\n"
    "v_mfma_f32_16x16x32_bf16 v[212:215], a[220:223], v[88:91], v[212:215]\n"
    "ds_read_b128 a[192:195], v224 offset:24576\n"
    "v_fma_f32 v6, v6, s40, v232\n"
    "v_mfma_f32_16x16x32_bf16 a[132:135], a[216:219], v[96:99], a[132:135]\n"
    "ds_read_b128 a[196:199], v225 offset:24576\n"
    "v_fma_f32 v22, v22, s40, v233\n"
    "v_mfma_f32_16x16x32_bf16 a[132:135], a[220:223], v[104:107], a[132:135]\n"
    "ds_read_b128 a[200:203], v224 offset:32768\n"
    "v_fma_f32 v38, v38, s40, v234\n"
    "v_mfma_f32_16x16x32_bf16 a[180:183], a[216:219], v[112:115], a[180:183]\n"
    "ds_read_b128 a[204:207], v225 offset:32768\n"
    "v_fma_f32 v54, v54, s40, v235\n"
    "v_mfma_f32_16x16x32_bf16 a[180:183], a[220:223], v[120:123], a[180:183]\n"
    "buffer_store_dwordx4 v[244:247], v250, s[84:87], s78 offen offset:256 nt sc1\n"
    "v_mfma_f32_16x16x32_bf16 v[168:171], a[224:227], v[64:67], v[168:171]\n"
    "ds_read_b128 a[208:211], v224 offset:40960\n"
    "v_fma_f32 v7, v7, s40, v232\n"
    "v_mfma_f32_16x16x32_bf16 v[168:171], a[228:231], v[72:75], v[168:171]\n"
    "v_exp_f32 v21, v21\n"
    "v_mfma_f32_16x16x32_bf16 v[216:219], a[224:227], v[80:83], v[216:219]\n"
    "v_exp_f32 v37, v37\n"
    "v_mfma_f32_16x16x32_bf16 v[216:219], a[228:231], v[88:91], v[216:219]\n"
    "ds_read_b128 a[212:215], v225 offset:40960\n"
    "v_fma_f32 v23, v23, s40, v233\n"
    "v_mfma_f32_16x16x32_bf16 a[136:139], a[224:227], v[96:99], a[136:139]\n"
    "v_exp_f32 v53, v53\n"
    "v_mfma_f32_16x16x32_bf16 a[136:139], a[228:231], v[104:107], a[136:139]\n"
    "ds_read_b128 a[216:219], v224 offset:26624\n"
    "v_fma_f32 v39, v39, s40, v234\n"
    "v_mfma_f32_16x16x32_bf16 a[184:187], a[224:227], v[112:115], a[184:187]\n"
    "v_exp_f32 v6, v6\n"
    "v_mfma_f32_16x16x32_bf16 a[184:187], a[228:231], v[120:123], a[184:187]\n"
    "ds_read_b128 a[220:223], v225 offset:26624\n"
    "v_fma_f32 v55, v55, s40, v235\n"
    "v_mfma_f32_16x16x32_bf16 a[252:255], v[240:243], v[112:115], a[252:255]\n"
    "v_exp_f32 v22, v22\n"
    "v_mfma_f32_16x16x32_bf16 a[252:255], v[240:243], v[120:123], a[252:255]\n"
    "v_exp_f32 v38, v38\n"
    "v_mfma_f32_16x16x32_bf16 v[172:175], a[232:235], v[64:67], v[172:175]\n"
    "ds_read_b128 a[224:227], v224 offset:34816\n"
    "v_mfma_f32_16x16x32_bf16 v[172:175], a[236:239], v[72:75], v[172:175]\n"
    "v_exp_f32 v54, v54\n"
    "v_mfma_f32_16x16x32_bf16 v[220:223], a[232:235], v[80:83], v[220:223]\n"
    "v_exp_f32 v7, v7\n"
    "v_mfma_f32_16x16x32_bf16 v[220:223], a[236:239], v[88:91], v[220:223]\n"
    "v_exp_f32 v23, v23\n"
    "v_mfma_f32_16x16x32_bf16 a[140:143], a[232:235], v[96:99], a[140:143]\n"
    "v_exp_f32 v39, v39\n"
    "v_mfma_f32_16x16x32_bf16 a[140:143], a[236:239], v[104:107], a[140:143]\n"
    "ds_read_b128 a[228:231], v225 offset:34816\n"
    "v_mfma_f32_16x16x32_bf16 a[188:191], a[232:235], v[112:115], a[188:191]\n"
    "v_exp_f32 v55, v55\n"
    "v_mfma_f32_16x16x32_bf16 a[188:191], a[236:239], v[120:123], a[188:191]\n"
    "s_waitcnt lgkmcnt(4)\n"
    "v_mfma_f32_16x16x32_bf16 v[64:67], a[192:195], a[0:3], 0\n"
    "s_cbranch_vccnz LSRBo_%=\n"
    "LSRBbo_%=:\n"
    "v_fma_f32 v8, v8, s40, v232\n"
    "v_mfma_f32_16x16x32_bf16 v[64:67], a[196:199], a[4:7], v[64:67]\n"
    "s_mov_b32 m0, s61\n"
    "v_fma_f32 v24, v24, s40, v233\n"
    "v_mfma_f32_16x16x32_bf16 v[64:67], a[200:203], a[8:11], v[64:67]\n"
    "v_exp_f32 v8, v8\n"
    "v_mfma_f32_16x16x32_bf16 v[64:67], a[204:207], a[12:15], v[64:67]\n"
    "buffer_load_dwordx4 v230, s[24:27], s37 offen offset:0 nt lds\n"
    "v_mfma_f32_16x16x32_bf16 v[64:67], a[208:211], a[16:19], v[64:67]\n"
    "ds_read_b128 a[232:235], v224 offset:43008\n"
    "v_cvt_pk_bf16_f32 v0, v0, v1\n"
    "v_mfma_f32_16x16x32_bf16 v[64:67], a[212:215], a[20:23], v[64:67]\n"
    "v_exp_f32 v24, v24\n"
    "v_mfma_f32_16x16x32_bf16 v[80:83], a[192:195], a[24:27], 0\n"
    "v_fma_f32 v40, v40, s40, v234\n"
    "v_fma_f32 v56, v56, s40, v235\n"
    "v_mfma_f32_16x16x32_bf16 v[80:83], a[196:199], a[28:31], v[80:83]\n"
    "v_exp_f32 v40, v40\n"
    "v_mfma_f32_16x16x32_bf16 v[80:83], a[200:203], a[32:35], v[80:83]\n"
    "buffer_load_dwordx4 v230, s[24:27], s37 offen offset:3072 nt lds\n"
    "v_mfma_f32_16x16x32_bf16 v[80:83], a[204:207], a[36:39], v[80:83]\n"
    "ds_read_b128 a[236:239], v225 offset:43008\n"
    "v_cvt_pk_bf16_f32 v16, v16, v17\n"
    "v_mfma_f32_16x16x32_bf16 v[80:83], a[208:211], a[40:43], v[80:83]\n"
    "v_exp_f32 v56, v56\n"
    "v_mfma_f32_16x16x32_bf16 v[80:83], a[212:215], a[44:47], v[80:83]\n"
    "s_mov_b32 m0, s62\n"
    "v_fma_f32 v9, v9, s40, v232\n"
    "v_mfma_f32_16x16x32_bf16 v[96:99], a[192:195], a[48:51], 0\n"
    "v_exp_f32 v9, v9\n"
    "v_mfma_f32_16x16x32_bf16 v[96:99], a[196:199], a[52:55], v[96:99]\n"
    "buffer_load_dwordx4 v230, s[24:27], s37 offen offset:128 nt lds\n"
    "v_mfma_f32_16x16x32_bf16 v[96:99], a[200:203], a[56:59], v[96:99]\n"
    "v_fma_f32 v25, v25, s40, v233\n"
    "v_cvt_pk_bf16_f32 v32, v32, v33\n"
    "v_mfma_f32_16x16x32_bf16 v[96:99], a[204:207], a[60:63], v[96:99]\n"
    "v_exp_f32 v25, v25\n"
    "v_mfma_f32_16x16x32_bf16 v[96:99], a[208:211], a[64:67], v[96:99]\n"
    "v_fma_f32 v41, v41, s40, v234\n"
    "v_cvt_pk_bf16_f32 v48, v48, v49\n"
    "v_mfma_f32_16x16x32_bf16 v[96:99], a[212:215], a[68:71], v[96:99]\n"
    "v_exp_f32 v41, v41\n"
    "v_mfma_f32_16x16x32_bf16 v[112:115], a[192:195], a[72:75], 0\n"
    "buffer_load_dwordx4 v230, s[24:27], s37 offen offset:3200 nt lds\n"
    "v_mfma_f32_16x16x32_bf16 v[112:115], a[196:199], a[76:79], v[112:115]\n"
    "ds_read_b128 a[192:195], v224 offset:28672\n"
    "v_fma_f32 v57, v57, s40, v235\n"
    "v_mfma_f32_16x16x32_bf16 v[112:115], a[200:203], a[80:83], v[112:115]\n"
    "v_exp_f32 v57, v57\n"
    "v_mfma_f32_16x16x32_bf16 v[112:115], a[204:207], a[84:87], v[112:115]\n"
    "s_mov_b32 m0, s63\n"
    "v_cvt_pk_bf16_f32 v1, v2, v3\n"
    "v_mfma_f32_16x16x32_bf16 v[112:115], a[208:211], a[88:91], v[112:115]\n"
    "ds_read_b128 a[196:199], v225 offset:28672\n"
    "v_fma_f32 v10, v10, s40, v232\n"
    "v_mfma_f32_16x16x32_bf16 v[112:115], a[212:215], a[92:95], v[112:115]\n"
    "s_waitcnt lgkmcnt(2)\n"
    "v_cvt_pk_bf16_f32 v17, v18, v19\n"
    "v_mfma_f32_16x16x32_bf16 v[68:71], a[216:219], a[0:3], 0\n"
    "buffer_load_dwordx4 v230, s[24:27], s37 offen offset:256 nt lds\n"
    "v_mfma_f32_16x16x32_bf16 v[68:71], a[220:223], a[4:7], v[68:71]\n"
    "ds_read_b128 a[200:203], v224 offset:36864\n"
    "v_fma_f32 v26, v26, s40, v233\n"
    "v_mfma_f32_16x16x32_bf16 v[68:71], a[224:227], a[8:11], v[68:71]\n"
    "v_exp_f32 v10, v10\n"
    "v_mfma_f32_16x16x32_bf16 v[68:71], a[228:231], a[12:15], v[68:71]\n"
    "ds_read_b128 a[204:207], v225 offset:36864\n"
    "v_cvt_pk_bf16_f32 v33, v34, v35\n"
    "v_mfma_f32_16x16x32_bf16 v[68:71], a[232:235], a[16:19], v[68:71]\n"
    "v_exp_f32 v26, v26\n"
    "v_mfma_f32_16x16x32_bf16 v[68:71], a[236:239], a[20:23], v[68:71]\n"
    "buffer_load_dwordx4 v230, s[24:27], s37 offen offset:3328 nt lds\n"
    "v_mfma_f32_16x16x32_bf16 v[84:87], a[216:219], a[24:27], 0\n"
    "s_add_u32 s37, s37, 24576\n"
    "v_fma_f32 v42, v42, s40, v234\n"
    "v_mfma_f32_16x16x32_bf16 v[84:87], a[220:223], a[28:31], v[84:87]\n"
    "ds_read_b128 a[208:211], v224 offset:45056\n"
    "v_cvt_pk_bf16_f32 v49, v50, v51\n"
    "v_mfma_f32_16x16x32_bf16 v[84:87], a[224:227], a[32:35], v[84:87]\n"
    "ds_read_b128 v[244:247], v251 offset:4096\n"
    "v_fma_f32 v58, v58, s40, v235\n"
    "v_mfma_f32_16x16x32_bf16 v[84:87], a[228:231], a[36:39], v[84:87]\n"
    "ds_read_b128 a[212:215], v225 offset:45056\n"
    "v_cvt_pk_bf16_f32 v8, v8, v9\n"
    "v_mfma_f32_16x16x32_bf16 v[84:87], a[232:235], a[40:43], v[84:87]\n"
    "v_exp_f32 v42, v42\n"
    "v_mfma_f32_16x16x32_bf16 v[84:87], a[236:239], a[44:47], v[84:87]\n"
    "v_exp_f32 v58, v58\n"
    "v_mfma_f32_16x16x32_bf16 v[100:103], a[216:219], a[48:51], 0\n"
    "v_cvt_pk_bf16_f32 v2, v4, v5\n"
    "v_fma_f32 v11, v11, s40, v232\n"
    "v_mfma_f32_16x16x32_bf16 v[100:103], a[220:223], a[52:55], v[100:103]\n"
    "v_exp_f32 v11, v11\n"
    "v_mfma_f32_16x16x32_bf16 v[100:103], a[224:227], a[56:59], v[100:103]\n"
    "s_add_u32 m0, s39, 8064\n"
    "v_cvt_pk_bf16_f32 v18, v20, v21\n"
    "v_mfma_f32_16x16x32_bf16 v[100:103], a[228:231], a[60:63], v[100:103]\n"
    "v_cvt_pk_bf16_f32 v24, v24, v25\n"
    "v_cvt_pk_bf16_f32 v34, v36, v37\n"
    "v_mfma_f32_16x16x32_bf16 v[100:103], a[232:235], a[64:67], v[100:103]\n"
    "buffer_load_dwordx4 v228, s[28:31], s38 offen offset:128 nt lds\n"
    "v_mfma_f32_16x16x32_bf16 v[100:103], a[236:239], a[68:71], v[100:103]\n"
    "v_fma_f32 v27, v27, s40, v233\n"
    "v_cvt_pk_bf16_f32 v40, v40, v41\n"
    "v_mfma_f32_16x16x32_bf16 v[116:119], a[216:219], a[72:75], 0\n"
    "v_exp_f32 v27, v27\n"
    "v_mfma_f32_16x16x32_bf16 v[116:119], a[220:223], a[76:79], v[116:119]\n"
    "ds_read_b128 a[216:219], v224 offset:30720\n"
    "v_cvt_pk_bf16_f32 v50, v52, v53\n"
    "v_mfma_f32_16x16x32_bf16 v[116:119], a[224:227], a[80:83], v[116:119]\n"
    "ds_read_b128 a[220:223], v225 offset:30720\n"
    "v_fma_f32 v43, v43, s40, v234\n"
    "v_mfma_f32_16x16x32_bf16 v[116:119], a[228:231], a[84:87], v[116:119]\n"
    "buffer_load_dwordx4 v229, s[28:31], s38 offen offset:1152 nt lds\n"
    "v_mfma_f32_16x16x32_bf16 v[116:119], a[232:235], a[88:91], v[116:119]\n"
    "s_add_u32 s39, s39, 2048\n"
    "ds_read_b128 a[224:227], v224 offset:38912\n"
    "v_mfma_f32_16x16x32_bf16 v[116:119], a[236:239], a[92:95], v[116:119]\n"
    "s_waitcnt lgkmcnt(3)\n"
    "v_cvt_pk_bf16_f32 v3, v6, v7\n"
    "v_mfma_f32_16x16x32_bf16 v[72:75], a[192:195], a[0:3], 0\n"
    "s_add_u32 s38, s38, 6144\n"
    "v_cvt_pk_bf16_f32 v56, v56, v57\n"
    "v_mfma_f32_16x16x32_bf16 v[72:75], a[196:199], a[4:7], v[72:75]\n"
    "ds_read_b128 a[228:231], v225 offset:38912\n"
    "v_cvt_pk_bf16_f32 v19, v22, v23\n"
    "v_mfma_f32_16x16x32_bf16 v[72:75], a[200:203], a[8:11], v[72:75]\n"
    "buffer_store_dwordx4 v[244:247], v250, s[84:87], s79 offen offset:256 nt sc1\n"
    "v_mfma_f32_16x16x32_bf16 v[72:75], a[204:207], a[12:15], v[72:75]\n"
    "ds_read_b128 a[232:235], v224 offset:47104\n"
    "v_fma_f32 v59, v59, s40, v235\n"
    "v_mfma_f32_16x16x32_bf16 v[72:75], a[208:211], a[16:19], v[72:75]\n"
    "s_add_u32 s78, s78, 3072\n"
    "v_cvt_pk_bf16_f32 v35, v38, v39\n"
    "v_mfma_f32_16x16x32_bf16 v[72:75], a[212:215], a[20:23], v[72:75]\n"
    "s_add_u32 s79, s79, 3072\n"
    "v_fma_f32 v12, v12, s40, v232\n"
    "v_mfma_f32_16x16x32_bf16 v[88:91], a[192:195], a[24:27], 0\n"
    "ds_read_b128 a[236:239], v225 offset:47104\n"
    "v_cvt_pk_bf16_f32 v51, v54, v55\n"
    "v_mfma_f32_16x16x32_bf16 v[88:91], a[196:199], a[28:31], v[88:91]\n"
    "v_exp_f32 v43, v43\n"
    "v_mfma_f32_16x16x32_bf16 v[88:91], a[200:203], a[32:35], v[88:91]\n"
    "v_exp_f32 v59, v59\n"
    "v_mfma_f32_16x16x32_bf16 v[88:91], a[204:207], a[36:39], v[88:91]\n"
    "v_exp_f32 v12, v12\n"
    "v_mfma_f32_16x16x32_bf16 v[88:91], a[208:211], a[40:43], v[88:91]\n"
    "v_fma_f32 v28, v28, s40, v233\n"
    "v_fma_f32 v44, v44, s40, v234\n"
    "v_mfma_f32_16x16x32_bf16 v[88:91], a[212:215], a[44:47], v[88:91]\n"
    "v_exp_f32 v28, v28\n"
    "v_mfma_f32_16x16x32_bf16 v[104:107], a[192:195], a[48:51], 0\n"
    "v_exp_f32 v44, v44\n"
    "v_mfma_f32_16x16x32_bf16 v[104:107], a[196:199], a[52:55], v[104:107]\n"
    "v_fma_f32 v60, v60, s40, v235\n"
    "v_cvt_pk_bf16_f32 v9, v10, v11\n"
    "v_mfma_f32_16x16x32_bf16 v[104:107], a[200:203], a[56:59], v[104:107]\n"
    "v_exp_f32 v60, v60\n"
    "v_mfma_f32_16x16x32_bf16 v[104:107], a[204:207], a[60:63], v[104:107]\n"
    "v_fma_f32 v13, v13, s40, v232\n"
    "v_cvt_pk_bf16_f32 v25, v26, v27\n"
    "v_mfma_f32_16x16x32_bf16 v[104:107], a[208:211], a[64:67], v[104:107]\n"
    "v_exp_f32 v13, v13\n"
    "v_mfma_f32_16x16x32_bf16 v[104:107], a[212:215], a[68:71], v[104:107]\n"
    "s_waitcnt vmcnt(19) lgkmcnt(0)\n"
    "v_fma_f32 v29, v29, s40, v233\n"
    "v_mfma_f32_16x16x32_bf16 v[120:123], a[192:195], a[72:75], 0\n"
    "s_barrier\n"
    "v_cvt_pk_bf16_f32 v41, v42, v43\n"
    "v_mfma_f32_16x16x32_bf16 v[120:123], a[196:199], a[76:79], v[120:123]\n"
    "ds_read_b64_tr_b16 a[192:193], v226 offset:0\n"
    "v_fma_f32 v45, v45, s40, v234\n"
    "v_mfma_f32_16x16x32_bf16 v[120:123], a[200:203], a[80:83], v[120:123]\n"
    "ds_read_b64_tr_b16 a[194:195], v226 offset:6144\n"
    "v_cvt_pk_bf16_f32 v57, v58, v59\n"
    "v_mfma_f32_16x16x32_bf16 v[120:123], a[204:207], a[84:87], v[120:123]\n"
    "ds_read_b64_tr_b16 a[196:197], v226 offset:12288\n"
    "v_fma_f32 v61, v61, s40, v235\n"
    "v_mfma_f32_16x16x32_bf16 v[120:123], a[208:211], a[88:91], v[120:123]\n"
    "ds_read_b64_tr_b16 a[198:199], v226 offset:18432\n"
    "v_fma_f32 v14, v14, s40, v232\n"
    "v_mfma_f32_16x16x32_bf16 v[120:123], a[212:215], a[92:95], v[120:123]\n"
    "ds_read_b64_tr_b16 a[200:201], v227 offset:0\n"
    "v_fma_f32 v30, v30, s40, v233\n"
    "v_mfma_f32_16x16x32_bf16 v[76:79], a[216:219], a[0:3], 0\n"
    "ds_read_b64_tr_b16 a[202:203], v227 offset:6144\n"
    "v_fma_f32 v46, v46, s40, v234\n"
    "v_mfma_f32_16x16x32_bf16 v[76:79], a[220:223], a[4:7], v[76:79]\n"
    "ds_read_b64_tr_b16 a[204:205], v227 offset:12288\n"
    "v_fma_f32 v62, v62, s40, v235\n"
    "v_mfma_f32_16x16x32_bf16 v[76:79], a[224:227], a[8:11], v[76:79]\n"
    "ds_read_b64_tr_b16 a[206:207], v227 offset:18432\n"
    "v_cvt_pk_bf16_f32 v10, v12, v13\n"
    "v_mfma_f32_16x16x32_bf16 v[76:79], a[228:231], a[12:15], v[76:79]\n"
    "ds_read_b64_tr_b16 a[208:209], v226 offset:512\n"
    "v_fma_f32 v15, v15, s40, v232\n"
    "v_mfma_f32_16x16x32_bf16 v[76:79], a[232:235], a[16:19], v[76:79]\n"
    "v_exp_f32 v29, v29\n"
    "v_mfma_f32_16x16x32_bf16 v[76:79], a[236:239], a[20:23], v[76:79]\n"
    "ds_read_b64_tr_b16 a[210:211], v226 offset:6656\n"
    "v_cvt_pk_bf16_f32 v26, v28, v29\n"
    "v_mfma_f32_16x16x32_bf16 v[92:95], a[216:219], a[24:27], 0\n"
    "v_exp_f32 v45, v45\n"
    "v_mfma_f32_16x16x32_bf16 v[92:95], a[220:223], a[28:31], v[92:95]\n"
    "v_exp_f32 v61, v61\n"
    "v_mfma_f32_16x16x32_bf16 v[92:95], a[224:227], a[32:35], v[92:95]\n"
    "ds_read_b64_tr_b16 a[212:213], v226 offset:12800\n"
    "v_fma_f32 v31, v31, s40, v233\n"
    "v_mfma_f32_16x16x32_bf16 v[92:95], a[228:231], a[36:39], v[92:95]\n"
    "v_exp_f32 v14, v14\n"
    "v_mfma_f32_16x16x32_bf16 v[92:95], a[232:235], a[40:43], v[92:95]\n"
    "v_exp_f32 v30, v30\n"
    "v_mfma_f32_16x16x32_bf16 v[92:95], a[236:239], a[44:47], v[92:95]\n"
    "v_exp_f32 v46, v46\n"
    "v_mfma_f32_16x16x32_bf16 v[108:111], a[216:219], a[48:51], 0\n"
    "ds_read_b64_tr_b16 a[214:215], v226 offset:18944\n"
    "v_cvt_pk_bf16_f32 v42, v44, v45\n"
    "v_mfma_f32_16x16x32_bf16 v[108:111], a[220:223], a[52:55], v[108:111]\n"
    "v_exp_f32 v62, v62\n"
    "v_mfma_f32_16x16x32_bf16 v[108:111], a[224:227], a[56:59], v[108:111]\n"
    "s_sub_u32 s45, s45, 1\n"
    "v_fma_f32 v47, v47, s40, v234\n"
    "v_mfma_f32_16x16x32_bf16 v[108:111], a[228:231], a[60:63], v[108:111]\n"
    "v_exp_f32 v15, v15\n"
    "v_mfma_f32_16x16x32_bf16 v[108:111], a[232:235], a[64:67], v[108:111]\n"
    "v_exp_f32 v31, v31\n"
    "v_mfma_f32_16x16x32_bf16 v[108:111], a[236:239], a[68:71], v[108:111]\n"
    "v_exp_f32 v47, v47\n"
    "v_mfma_f32_16x16x32_bf16 v[124:127], a[216:219], a[72:75], 0\n"
    "v_cvt_pk_bf16_f32 v58, v60, v61\n"
    "v_fma_f32 v63, v63, s40, v235\n"
    "v_mfma_f32_16x16x32_bf16 v[124:127], a[220:223], a[76:79], v[124:127]\n"
    "v_exp_f32 v63, v63\n"
    "v_mfma_f32_16x16x32_bf16 v[124:127], a[224:227], a[80:83], v[124:127]\n"
    "v_cvt_pk_bf16_f32 v11, v14, v15\n"
    "v_cvt_pk_bf16_f32 v27, v30, v31\n"
    "v_mfma_f32_16x16x32_bf16 v[124:127], a[228:231], a[84:87], v[124:127]\n"
    "v_cvt_pk_bf16_f32 v43, v46, v47\n"
    "v_cvt_pk_bf16_f32 v59, v62, v63\n"
    "v_mfma_f32_16x16x32_bf16 v[124:127], a[232:235], a[88:91], v[124:127]\n"
    "v_add_u32 v251, 0x400, v251\n"
    "v_mfma_f32_16x16x32_bf16 v[124:127], a[236:239], a[92:95], v[124:127]\n"
    "s_waitcnt lgkmcnt(0)\n"
    "s_cbranch_scc1 LSB6_%=\n"
    "v_mfma_f32_16x16x32_bf16 v[128:131], a[192:195], v[0:3], v[128:131]\n"
    "ds_read_b64_tr_b16 a[216:217], v227 offset:512\n"
    "v_max3_f32 v236, v64, v65, v66\n"
    "v_mfma_f32_16x16x32_bf16 v[128:131], a[196:199], v[8:11], v[128:131]\n"
    "s_mov_b32 m0, s55\n"
    "v_max3_f32 v237, v80, v81, v82\n"
    "v_mfma_f32_16x16x32_bf16 v[176:179], a[192:195], v[16:19], v[176:179]\n"
    "ds_read_b64_tr_b16 a[218:219], v227 offset:6656\n"
    "v_max3_f32 v238, v96, v97, v98\n"
    "v_mfma_f32_16x16x32_bf16 v[176:179], a[196:199], v[24:27], v[176:179]\n"
    "buffer_load_dwordx4 v228, s[20:23], s36 offen offset:0 nt lds\n"
    "v_mfma_f32_16x16x32_bf16 a[96:99], a[192:195], v[32:35], a[96:99]\n"
    "ds_read_b64_tr_b16 a[220:221], v227 offset:12800\n"
    "v_max3_f32 v239, v112, v113, v114\n"
    "v_mfma_f32_16x16x32_bf16 a[96:99], a[196:199], v[40:43], a[96:99]\n"
    "ds_read_b64_tr_b16 a[222:223], v227 offset:18944\n"
    "v_max3_f32 v236, v236, v67, v68\n"
    "v_mfma_f32_16x16x32_bf16 a[144:147], a[192:195], v[48:51], a[144:147]\n"
    "ds_read_b64_tr_b16 a[224:225], v226 offset:1024\n"
    "v_max3_f32 v237, v237, v83, v84\n"
    "v_mfma_f32_16x16x32_bf16 a[144:147], a[196:199], v[56:59], a[144:147]\n"
    "ds_read_b64_tr_b16 a[226:227], v226 offset:7168\n"
    "v_max3_f32 v238, v238, v99, v100\n"
    "v_mfma_f32_16x16x32_bf16 v[132:135], a[200:203], v[0:3], v[132:135]\n"
    "buffer_load_dwordx4 v229, s[20:23], s36 offen offset:1024 nt lds\n"
    "v_mfma_f32_16x16x32_bf16 v[132:135], a[204:207], v[8:11], v[132:135]\n"
    "ds_read_b64_tr_b16 a[228:229], v226 offset:13312\n"
    "v_max3_f32 v239, v239, v115, v116\n"
    "v_mfma_f32_16x16x32_bf16 v[180:183], a[200:203], v[16:19], v[180:183]\n"
    "ds_read_b64_tr_b16 a[230:231], v226 offset:19456\n"
    "v_max3_f32 v236, v236, v69, v70\n"
    "v_mfma_f32_16x16x32_bf16 v[180:183], a[204:207], v[24:27], v[180:183]\n"
    "s_mov_b32 m0, s56\n"
    "v_max3_f32 v237, v237, v85, v86\n"
    "v_mfma_f32_16x16x32_bf16 a[100:103], a[200:203], v[32:35], a[100:103]\n"
    "ds_read_b64_tr_b16 a[232:233], v227 offset:1024\n"
    "v_max3_f32 v238, v238, v101, v102\n"
    "v_mfma_f32_16x16x32_bf16 a[100:103], a[204:207], v[40:43], a[100:103]\n"
    "buffer_load_dwordx4 v228, s[20:23], s36 offen offset:128 nt lds\n"
    "ds_read_b64_tr_b16 a[234:235], v227 offset:7168\n"
    "v_mfma_f32_16x16x32_bf16 a[148:151], a[200:203], v[48:51], a[148:151]\n"
    "ds_read_b64_tr_b16 a[236:237], v227 offset:13312\n"
    "v_max3_f32 v239, v239, v117, v118\n"
    "v_mfma_f32_16x16x32_bf16 a[148:151], a[204:207], v[56:59], a[148:151]\n"
    "ds_read_b64_tr_b16 a[238:239], v227 offset:19456\n"
    "v_max3_f32 v236, v236, v71, v72\n"
    "v_mfma_f32_16x16x32_bf16 a[240:243], v[240:243], v[0:3], a[240:243]\n"
    "ds_read_b64_tr_b16 a[192:193], v226 offset:1536\n"
    "v_max3_f32 v237, v237, v87, v88\n"
    "v_mfma_f32_16x16x32_bf16 a[240:243], v[240:243], v[8:11], a[240:243]\n"
    "ds_read_b64_tr_b16 a[194:195], v226 offset:7680\n"
    "v_max3_f32 v238, v238, v103, v104\n"
    "v_mfma_f32_16x16x32_bf16 v[136:139], a[208:211], v[0:3], v[136:139]\n"
    "buffer_load_dwordx4 v229, s[20:23], s36 offen offset:1152 nt lds\n"
    "v_mfma_f32_16x16x32_bf16 v[136:139], a[212:215], v[8:11], v[136:139]\n"
    "ds_read_b64_tr_b16 a[196:197], v226 offset:13824\n"
    "v_max3_f32 v239, v239, v119, v120\n"
    "v_mfma_f32_16x16x32_bf16 v[184:187], a[208:211], v[16:19], v[184:187]\n"
    "ds_read_b64_tr_b16 a[198:199], v226 offset:19968\n"
    "v_max3_f32 v236, v236, v73, v74\n"
    "v_mfma_f32_16x16x32_bf16 v[184:187], a[212:215], v[24:27], v[184:187]\n"
    "s_mov_b32 m0, s57\n"
    "v_max3_f32 v237, v237, v89, v90\n"
    "v_mfma_f32_16x16x32_bf16 a[104:107], a[208:211], v[32:35], a[104:107]\n"
    "ds_read_b64_tr_b16 a[200:201], v227 offset:1536\n"
    "v_max3_f32 v238, v238, v105, v106\n"
    "v_mfma_f32_16x16x32_bf16 a[104:107], a[212:215], v[40:43], a[104:107]\n"
    "buffer_load_dwordx4 v228, s[20:23], s36 offen offset:256 nt lds\n"
    "v_mfma_f32_16x16x32_bf16 a[152:155], a[208:211], v[48:51], a[152:155]\n"
    "ds_read_b64_tr_b16 a[202:203], v227 offset:7680\n"
    "v_max3_f32 v239, v239, v121, v122\n"
    "v_mfma_f32_16x16x32_bf16 a[152:155], a[212:215], v[56:59], a[152:155]\n"
    "s_waitcnt lgkmcnt(6)\n"
    "v_max3_f32 v236, v236, v75, v76\n"
    "v_mfma_f32_16x16x32_bf16 v[140:143], a[216:219], v[0:3], v[140:143]\n"
    "ds_read_b64_tr_b16 a[204:205], v227 offset:13824\n"
    "v_max3_f32 v237, v237, v91, v92\n"
    "v_mfma_f32_16x16x32_bf16 v[140:143], a[220:223], v[8:11], v[140:143]\n"
    "ds_read_b64_tr_b16 a[206:207], v227 offset:19968\n"
    "v_max3_f32 v238, v238, v107, v108\n"
    "v_mfma_f32_16x16x32_bf16 v[188:191], a[216:219], v[16:19], v[188:191]\n"
    "buffer_load_dwordx4 v229, s[20:23], s36 offen offset:1280 nt lds\n"
    "v_mfma_f32_16x16x32_bf16 v[188:191], a[220:223], v[24:27], v[188:191]\n"
    "s_add_u32 s36, s36, 24576\n"
    "v_max3_f32 v239, v239, v123, v124\n"
    "v_mfma_f32_16x16x32_bf16 a[108:111], a[216:219], v[32:35], a[108:111]\n"
    "ds_read_b64_tr_b16 a[208:209], v226 offset:2048\n"
    "v_max3_f32 v236, v236, v77, v78\n"
    "v_mfma_f32_16x16x32_bf16 a[108:111], a[220:223], v[40:43], a[108:111]\n"
    "ds_read_b64_tr_b16 a[210:211], v226 offset:8192\n"
    "v_max3_f32 v237, v237, v93, v94\n"
    "v_mfma_f32_16x16x32_bf16 a[156:159], a[216:219], v[48:51], a[156:159]\n"
    "ds_read_b64_tr_b16 a[212:213], v226 offset:14336\n"
    "v_max3_f32 v238, v238, v109, v110\n"
    "v_mfma_f32_16x16x32_bf16 a[156:159], a[220:223], v[56:59], a[156:159]\n"
    "ds_read_b64_tr_b16 a[214:215], v226 offset:20480\n"
    "v_max3_f32 v239, v239, v125, v126\n"
    "v_mfma_f32_16x16x32_bf16 v[144:147], a[224:227], v[0:3], v[144:147]\n"
    "ds_read_b64_tr_b16 a[216:217], v227 offset:2048\n"
    "v_max_f32 v236, v236, v79\n"
    "v_mfma_f32_16x16x32_bf16 v[144:147], a[228:231], v[8:11], v[144:147]\n"
    "ds_read_b64_tr_b16 a[218:219], v227 offset:8192\n"
    "v_max_f32 v237, v237, v95\n"
    "v_mfma_f32_16x16x32_bf16 v[192:195], a[224:227], v[16:19], v[192:195]\n"
    "ds_read_b64_tr_b16 a[220:221], v227 offset:14336\n"
    "v_max_f32 v238, v238, v111\n"
    "v_mfma_f32_16x16x32_bf16 v[192:195], a[228:231], v[24:27], v[192:195]\n"
    "v_max_f32 v239, v239, v127\n"
    "v_fma_f32 v236, v236, s40, v232\n"
    "v_mfma_f32_16x16x32_bf16 a[112:115], a[224:227], v[32:35], a[112:115]\n"
    "ds_read_b64_tr_b16 a[222:223], v227 offset:20480\n"
    "v_fma_f32 v237, v237, s40, v233\n"
    "v_mfma_f32_16x16x32_bf16 a[112:115], a[228:231], v[40:43], a[112:115]\n"
    "v_fma_f32 v238, v238, s40, v234\n"
    "v_fma_f32 v239, v239, s40, v235\n"
    "v_mfma_f32_16x16x32_bf16 a[160:163], a[224:227], v[48:51], a[160:163]\n"
    "v_max3_f32 v231, v236, v237, v238\n"
    "v_max_f32 v231, v231, v239\n"
    "v_mfma_f32_16x16x32_bf16 a[160:163], a[228:231], v[56:59], a[160:163]\n"
    "v_cmp_lt_f32 vcc, s41, v231\n"
    "s_cbranch_vccnz LSRAo_%=\n"
    "LSRAbo_%=:\n"
    "v_mfma_f32_16x16x32_bf16 a[244:247], v[240:243], v[16:19], a[244:247]\n"
    "ds_read_b64_tr_b16 a[224:225], v226 offset:2560\n"
    "v_fma_f32 v64, v64, s40, v232\n"
    "v_mfma_f32_16x16x32_bf16 a[244:247], v[240:243], v[24:27], a[244:247]\n"
    "v_exp_f32 v64, v64\n"
    "v_mfma_f32_16x16x32_bf16 v[148:151], a[232:235], v[0:3], v[148:151]\n"
    "ds_read_b64_tr_b16 a[226:227], v226 offset:8704\n"
    "v_fma_f32 v80, v80, s40, v233\n"
    "v_mfma_f32_16x16x32_bf16 v[148:151], a[236:239], v[8:11], v[148:151]\n"
    "v_exp_f32 v80, v80\n"
    "v_mfma_f32_16x16x32_bf16 v[196:199], a[232:235], v[16:19], v[196:199]\n"
    "v_fma_f32 v96, v96, s40, v234\n"
    "v_fma_f32 v112, v112, s40, v235\n"
    "v_mfma_f32_16x16x32_bf16 v[196:199], a[236:239], v[24:27], v[196:199]\n"
    "ds_read_b64_tr_b16 a[228:229], v226 offset:14848\n"
    "v_fma_f32 v65, v65, s40, v232\n"
    "v_mfma_f32_16x16x32_bf16 a[116:119], a[232:235], v[32:35], a[116:119]\n"
    "v_exp_f32 v96, v96\n"
    "v_mfma_f32_16x16x32_bf16 a[116:119], a[236:239], v[40:43], a[116:119]\n"
    "v_exp_f32 v112, v112\n"
    "v_mfma_f32_16x16x32_bf16 a[164:167], a[232:235], v[48:51], a[164:167]\n"
    "ds_read_b64_tr_b16 a[230:231], v226 offset:20992\n"
    "v_fma_f32 v81, v81, s40, v233\n"
    "v_mfma_f32_16x16x32_bf16 a[164:167], a[236:239], v[56:59], a[164:167]\n"
    "s_waitcnt lgkmcnt(4)\n"
    "v_fma_f32 v97, v97, s40, v234\n"
    "v_mfma_f32_16x16x32_bf16 v[152:155], a[192:195], v[0:3], v[152:155]\n"
    "v_exp_f32 v65, v65\n"
    "v_mfma_f32_16x16x32_bf16 v[152:155], a[196:199], v[8:11], v[152:155]\n"
    "ds_read_b64_tr_b16 a[232:233], v227 offset:2560\n"
    "v_fma_f32 v113, v113, s40, v235\n"
    "v_mfma_f32_16x16x32_bf16 v[200:203], a[192:195], v[16:19], v[200:203]\n"
    "v_exp_f32 v81, v81\n"
    "v_mfma_f32_16x16x32_bf16 v[200:203], a[196:199], v[24:27], v[200:203]\n"
    "v_exp_f32 v97, v97\n"
    "v_mfma_f32_16x16x32_bf16 a[120:123], a[192:195], v[32:35], a[120:123]\n"
    "v_exp_f32 v113, v113\n"
    "v_mfma_f32_16x16x32_bf16 a[120:123], a[196:199], v[40:43], a[120:123]\n"
    "ds_read_b64_tr_b16 a[234:235], v227 offset:8704\n"
    "v_fma_f32 v66, v66, s40, v232\n"
    "v_mfma_f32_16x16x32_bf16 a[168:171], a[192:195], v[48:51], a[168:171]\n"
    "v_exp_f32 v66, v66\n"
    "v_mfma_f32_16x16x32_bf16 a[168:171], a[196:199], v[56:59], a[168:171]\n"
    "v_fma_f32 v82, v82, s40, v233\n"
    "v_fma_f32 v98, v98, s40, v234\n"
    "v_mfma_f32_16x16x32_bf16 v[156:159], a[200:203], v[0:3], v[156:159]\n"
    "v_exp_f32 v82, v82\n"
    "v_mfma_f32_16x16x32_bf16 v[156:159], a[204:207], v[8:11], v[156:159]\n"
    "v_exp_f32 v98, v98\n"
    "v_mfma_f32_16x16x32_bf16 v[204:207], a[200:203], v[16:19], v[204:207]\n"
    "ds_read_b64_tr_b16 a[236:237], v227 offset:14848\n"
    "v_fma_f32 v114, v114, s40, v235\n"
    "v_mfma_f32_16x16x32_bf16 v[204:207], a[204:207], v[24:27], v[204:207]\n"
    "ds_read_b128 v[244:247], v251 offset:8192\n"
    "v_fma_f32 v67, v67, s40, v232\n"
    "v_mfma_f32_16x16x32_bf16 a[124:127], a[200:203], v[32:35], a[124:127]\n"
    "v_exp_f32 v114, v114\n"
    "v_mfma_f32_16x16x32_bf16 a[124:127], a[204:207], v[40:43], a[124:127]\n"
    "v_exp_f32 v67, v67\n"
    "v_mfma_f32_16x16x32_bf16 a[172:175], a[200:203], v[48:51], a[172:175]\n"
    "v_fma_f32 v83, v83, s40, v233\n"
    "v_fma_f32 v99, v99, s40, v234\n"
    "v_mfma_f32_16x16x32_bf16 a[172:175], a[204:207], v[56:59], a[172:175]\n"
    "v_exp_f32 v83, v83\n"
    "v_mfma_f32_16x16x32_bf16 a[248:251], v[240:243], v[32:35], a[248:251]\n"
    "ds_read_b64_tr_b16 a[238:239], v227 offset:20992\n"
    "v_fma_f32 v115, v115, s40, v235\n"
    "v_mfma_f32_16x16x32_bf16 a[248:251], v[240:243], v[40:43], a[248:251]\n"
    "v_exp_f32 v99, v99\n"
    "v_mfma_f32_16x16x32_bf16 v[160:163], a[208:211], v[0:3], v[160:163]\n"
    "v_exp_f32 v115, v115\n"
    "v_mfma_f32_16x16x32_bf16 v[160:163], a[212:215], v[8:11], v[160:163]\n"
    "v_fma_f32 v68, v68, s40, v232\n"
    "v_fma_f32 v84, v84, s40, v233\n"
    "v_mfma_f32_16x16x32_bf16 v[208:211], a[208:211], v[16:19], v[208:211]\n"
    "v_exp_f32 v68, v68\n"
    "v_mfma_f32_16x16x32_bf16 v[208:211], a[212:215], v[24:27], v[208:211]\n"
    "v_exp_f32 v84, v84\n"
    "v_mfma_f32_16x16x32_bf16 a[128:131], a[208:211], v[32:35], a[128:131]\n"
    "v_fma_f32 v100, v100, s40, v234\n"
    "v_fma_f32 v116, v116, s40, v235\n"
    "v_mfma_f32_16x16x32_bf16 a[128:131], a[212:215], v[40:43], a[128:131]\n"
    "v_exp_f32 v100, v100\n"
    "v_mfma_f32_16x16x32_bf16 a[176:179], a[208:211], v[48:51], a[176:179]\n"
    "v_exp_f32 v116, v116\n"
    "v_mfma_f32_16x16x32_bf16 a[176:179], a[212:215], v[56:59], a[176:179]\n"
    "v_fma_f32 v69, v69, s40, v232\n"
    "v_fma_f32 v85, v85, s40, v233\n"
    "v_mfma_f32_16x16x32_bf16 v[164:167], a[216:219], v[0:3], v[164:167]\n"
    "v_exp_f32 v69, v69\n"
    "v_mfma_f32_16x16x32_bf16 v[164:167], a[220:223], v[8:11], v[164:167]\n"
    "s_waitcnt vmcnt(16) lgkmcnt(0)\n"
    "v_fma_f32 v101, v101, s40, v234\n"
    "v_mfma_f32_16x16x32_bf16 v[212:215], a[216:219], v[16:19], v[212:215]\n"
    "s_barrier\n"
    "v_fma_f32 v117, v117, s40, v235\n"
    "v_mfma_f32_16x16x32_bf16 v[212:215], a[220:223], v[24:27], v[212:215]\n"
    "ds_read_b128 a[192:195], v224 offset:0\n"
    "v_fma_f32 v70, v70, s40, v232\n"
    "v_mfma_f32_16x16x32_bf16 a[132:135], a[216:219], v[32:35], a[132:135]\n"
    "ds_read_b128 a[196:199], v225 offset:0\n"
    "v_fma_f32 v86, v86, s40, v233\n"
    "v_mfma_f32_16x16x32_bf16 a[132:135], a[220:223], v[40:43], a[132:135]\n"
    "ds_read_b128 a[200:203], v224 offset:8192\n"
    "v_fma_f32 v102, v102, s40, v234\n"
    "v_mfma_f32_16x16x32_bf16 a[180:183], a[216:219], v[48:51], a[180:183]\n"
    "ds_read_b128 a[204:207], v225 offset:8192\n"
    "v_fma_f32 v118, v118, s40, v235\n"
    "v_mfma_f32_16x16x32_bf16 a[180:183], a[220:223], v[56:59], a[180:183]\n"
    "buffer_store_dwordx4 v[244:247], v250, s[84:87], s78 offen offset:128 nt sc1\n"
    "v_mfma_f32_16x16x32_bf16 v[168:171], a[224:227], v[0:3], v[168:171]\n"
    "ds_read_b128 a[208:211], v224 offset:16384\n"
    "v_fma_f32 v71, v71, s40, v232\n"
    "v_mfma_f32_16x16x32_bf16 v[168:171], a[228:231], v[8:11], v[168:171]\n"
    "v_exp_f32 v85, v85\n"
    "v_mfma_f32_16x16x32_bf16 v[216:219], a[224:227], v[16:19], v[216:219]\n"
    "ds_read_b128 a[212:215], v225 offset:16384\n"
    "v_fma_f32 v87, v87, s40, v233\n"
    "v_mfma_f32_16x16x32_bf16 v[216:219], a[228:231], v[24:27], v[216:219]\n"
    "v_exp_f32 v101, v101\n"
    "v_mfma_f32_16x16x32_bf16 a[136:139], a[224:227], v[32:35], a[136:139]\n"
    "ds_read_b128 a[216:219], v224 offset:2048\n"
    "v_fma_f32 v103, v103, s40, v234\n"
    "v_mfma_f32_16x16x32_bf16 a[136:139], a[228:231], v[40:43], a[136:139]\n"
    "v_exp_f32 v117, v117\n"
    "v_mfma_f32_16x16x32_bf16 a[184:187], a[224:227], v[48:51], a[184:187]\n"
    "v_exp_f32 v70, v70\n"
    "v_mfma_f32_16x16x32_bf16 a[184:187], a[228:231], v[56:59], a[184:187]\n"
    "ds_read_b128 a[220:223], v225 offset:2048\n"
    "v_fma_f32 v119, v119, s40, v235\n"
    "v_mfma_f32_16x16x32_bf16 a[252:255], v[240:243], v[48:51], a[252:255]\n"
    "v_exp_f32 v86, v86\n"
    "v_mfma_f32_16x16x32_bf16 a[252:255], v[240:243], v[56:59], a[252:255]\n"
    "v_exp_f32 v102, v102\n"
    "v_mfma_f32_16x16x32_bf16 v[172:175], a[232:235], v[0:3], v[172:175]\n"
    "ds_read_b128 a[224:227], v224 offset:10240\n"
    "v_mfma_f32_16x16x32_bf16 v[172:175], a[236:239], v[8:11], v[172:175]\n"
    "v_exp_f32 v118, v118\n"
    "v_mfma_f32_16x16x32_bf16 v[220:223], a[232:235], v[16:19], v[220:223]\n"
    "v_exp_f32 v71, v71\n"
    "v_mfma_f32_16x16x32_bf16 v[220:223], a[236:239], v[24:27], v[220:223]\n"
    "v_exp_f32 v87, v87\n"
    "v_mfma_f32_16x16x32_bf16 a[140:143], a[232:235], v[32:35], a[140:143]\n"
    "v_exp_f32 v103, v103\n"
    "v_mfma_f32_16x16x32_bf16 a[140:143], a[236:239], v[40:43], a[140:143]\n"
    "ds_read_b128 a[228:231], v225 offset:10240\n"
    "v_mfma_f32_16x16x32_bf16 a[188:191], a[232:235], v[48:51], a[188:191]\n"
    "s_waitcnt lgkmcnt(4)\n"
    "v_exp_f32 v119, v119\n"
    "v_mfma_f32_16x16x32_bf16 a[188:191], a[236:239], v[56:59], a[188:191]\n"
    "s_branch LSLOOP_%=\n"
    "LSB6_%=:\n"
    "v_mfma_f32_16x16x32_bf16 v[128:131], a[192:195], v[0:3], v[128:131]\n"
    "ds_read_b64_tr_b16 a[216:217], v227 offset:512\n"
    "v_add_u32 v253, s75, v224\n"
    "v_max3_f32 v236, v64, v65, v66\n"
    "v_mfma_f32_16x16x32_bf16 v[128:131], a[196:199], v[8:11], v[128:131]\n"
    "s_mov_b32 m0, s55\n"
    "v_add_u32 v254, s75, v225\n"
    "v_max3_f32 v237, v80, v81, v82\n"
    "v_mfma_f32_16x16x32_bf16 v[176:179], a[192:195], v[16:19], v[176:179]\n"
    "ds_read_b64_tr_b16 a[218:219], v227 offset:6656\n"
    "v_max3_f32 v238, v96, v97, v98\n"
    "v_max3_f32 v239, v112, v113, v114\n"
    "v_max3_f32 v236, v236, v67, v68\n"
    "v_mfma_f32_16x16x32_bf16 v[176:179], a[196:199], v[24:27], v[176:179]\n"
    "buffer_load_dwordx4 v228, s[20:23], s36 offen offset:0 nt lds\n"
    "v_max3_f32 v237, v237, v83, v84\n"
    "v_max3_f32 v238, v238, v99, v100\n"
    "v_mfma_f32_16x16x32_bf16 a[96:99], a[192:195], v[32:35], a[96:99]\n"
    "ds_read_b64_tr_b16 a[220:221], v227 offset:12800\n"
    "v_max3_f32 v239, v239, v115, v116\n"
    "v_max3_f32 v236, v236, v69, v70\n"
    "v_mfma_f32_16x16x32_bf16 a[96:99], a[196:199], v[40:43], a[96:99]\n"
    "ds_read_b64_tr_b16 a[222:223], v227 offset:18944\n"
    "v_max3_f32 v237, v237, v85, v86\n"
    "v_max3_f32 v238, v238, v101, v102\n"
    "v_max3_f32 v239, v239, v117, v118\n"
    "v_mfma_f32_16x16x32_bf16 a[144:147], a[192:195], v[48:51], a[144:147]\n"
    "ds_read_b64_tr_b16 a[224:225], v226 offset:1024\n"
    "v_max3_f32 v236, v236, v71, v72\n"
    "v_max3_f32 v237, v237, v87, v88\n"
    "v_mfma_f32_16x16x32_bf16 a[144:147], a[196:199], v[56:59], a[144:147]\n"
    "ds_read_b64_tr_b16 a[226:227], v226 offset:7168\n"
    "v_max3_f32 v238, v238, v103, v104\n"
    "v_max3_f32 v239, v239, v119, v120\n"
    "v_max3_f32 v236, v236, v73, v74\n"
    "v_mfma_f32_16x16x32_bf16 v[132:135], a[200:203], v[0:3], v[132:135]\n"
    "buffer_load_dwordx4 v229, s[20:23], s36 offen offset:1024 nt lds\n"
    "v_max3_f32 v237, v237, v89, v90\n"
    "v_mfma_f32_16x16x32_bf16 v[132:135], a[204:207], v[8:11], v[132:135]\n"
    "ds_read_b64_tr_b16 a[228:229], v226 offset:13312\n"
    "ds_read_b128 v[244:247], v249 offset:11264\n"
    "v_max3_f32 v238, v238, v105, v106\n"
    "v_max3_f32 v239, v239, v121, v122\n"
    "v_mfma_f32_16x16x32_bf16 v[180:183], a[200:203], v[16:19], v[180:183]\n"
    "ds_read_b64_tr_b16 a[230:231], v226 offset:19456\n"
    "v_max3_f32 v236, v236, v75, v76\n"
    "v_max3_f32 v237, v237, v91, v92\n"
    "v_mfma_f32_16x16x32_bf16 v[180:183], a[204:207], v[24:27], v[180:183]\n"
    "s_mov_b32 m0, s56\n"
    "v_max3_f32 v238, v238, v107, v108\n"
    "v_max3_f32 v239, v239, v123, v124\n"
    "v_max3_f32 v236, v236, v77, v78\n"
    "v_mfma_f32_16x16x32_bf16 a[100:103], a[200:203], v[32:35], a[100:103]\n"
    "ds_read_b64_tr_b16 a[232:233], v227 offset:1024\n"
    "v_max3_f32 v237, v237, v93, v94\n"
    "v_max3_f32 v238, v238, v109, v110\n"
    "v_mfma_f32_16x16x32_bf16 a[100:103], a[204:207], v[40:43], a[100:103]\n"
    "buffer_load_dwordx4 v228, s[20:23], s36 offen offset:128 nt lds\n"
    "ds_read_b64_tr_b16 a[234:235], v227 offset:7168\n"
    "v_max3_f32 v239, v239, v125, v126\n"
    "v_max_f32 v236, v236, v79\n"
    "v_mfma_f32_16x16x32_bf16 a[148:151], a[200:203], v[48:51], a[148:151]\n"
    "ds_read_b64_tr_b16 a[236:237], v227 offset:13312\n"
    "v_max_f32 v237, v237, v95\n"
    "v_max_f32 v238, v238, v111\n"
    "v_mfma_f32_16x16x32_bf16 a[148:151], a[204:207], v[56:59], a[148:151]\n"
    "ds_read_b64_tr_b16 a[238:239], v227 offset:19456\n"
    "v_max_f32 v239, v239, v127\n"
    "v_fma_f32 v236, v236, s40, v232\n"
    "v_fma_f32 v237, v237, s40, v233\n"
    "v_mfma_f32_16x16x32_bf16 a[240:243], v[240:243], v[0:3], a[240:243]\n"
    "ds_read_b64_tr_b16 a[192:193], v226 offset:1536\n"
    "v_fma_f32 v238, v238, s40, v234\n"
    "v_fma_f32 v239, v239, s40, v235\n"
    "v_mfma_f32_16x16x32_bf16 a[240:243], v[240:243], v[8:11], a[240:243]\n"
    "ds_read_b64_tr_b16 a[194:195], v226 offset:7680\n"
    "v_max3_f32 v231, v236, v237, v238\n"
    "v_max_f32 v231, v231, v239\n"
    "v_cmp_lt_f32 vcc, s41, v231\n"
    "v_mfma_f32_16x16x32_bf16 v[136:139], a[208:211], v[0:3], v[136:139]\n"
    "buffer_load_dwordx4 v229, s[20:23], s36 offen offset:1152 nt lds\n"
    "s_cbranch_vccnz LSRAx_%=\n"
    "LSRAbx_%=:\n"
    "v_mfma_f32_16x16x32_bf16 v[136:139], a[212:215], v[8:11], v[136:139]\n"
    "ds_read_b64_tr_b16 a[196:197], v226 offset:13824\n"
    "v_fma_f32 v64, v64, s40, v232\n"
    "v_exp_f32 v64, v64\n"
    "v_mfma_f32_16x16x32_bf16 v[184:187], a[208:211], v[16:19], v[184:187]\n"
    "ds_read_b64_tr_b16 a[198:199], v226 offset:19968\n"
    "v_fma_f32 v80, v80, s40, v233\n"
    "v_fma_f32 v96, v96, s40, v234\n"
    "v_mfma_f32_16x16x32_bf16 v[184:187], a[212:215], v[24:27], v[184:187]\n"
    "s_mov_b32 m0, s57\n"
    "v_exp_f32 v80, v80\n"
    "v_fma_f32 v112, v112, s40, v235\n"
    "v_mfma_f32_16x16x32_bf16 a[104:107], a[208:211], v[32:35], a[104:107]\n"
    "ds_read_b64_tr_b16 a[200:201], v227 offset:1536\n"
    "v_exp_f32 v96, v96\n"
    "v_mfma_f32_16x16x32_bf16 a[104:107], a[212:215], v[40:43], a[104:107]\n"
    "buffer_load_dwordx4 v228, s[20:23], s36 offen offset:256 nt lds\n"
    "v_exp_f32 v112, v112\n"
    "v_mfma_f32_16x16x32_bf16 a[152:155], a[208:211], v[48:51], a[152:155]\n"
    "ds_read_b64_tr_b16 a[202:203], v227 offset:7680\n"
    "v_fma_f32 v65, v65, s40, v232\n"
    "v_fma_f32 v81, v81, s40, v233\n"
    "v_mfma_f32_16x16x32_bf16 a[152:155], a[212:215], v[56:59], a[152:155]\n"
    "s_waitcnt lgkmcnt(6)\n"
    "v_exp_f32 v65, v65\n"
    "v_fma_f32 v97, v97, s40, v234\n"
    "v_mfma_f32_16x16x32_bf16 v[140:143], a[216:219], v[0:3], v[140:143]\n"
    "ds_read_b64_tr_b16 a[204:205], v227 offset:13824\n"
    "buffer_store_dwordx4 v[244:247], v250, s[84:87], s42 offen offset:3200 nt sc1\n"
    "v_fma_f32 v113, v113, s40, v235\n"
    "v_mfma_f32_16x16x32_bf16 v[140:143], a[220:223], v[8:11], v[140:143]\n"
    "ds_read_b64_tr_b16 a[206:207], v227 offset:19968\n"
    "v_exp_f32 v81, v81\n"
    "v_fma_f32 v66, v66, s40, v232\n"
    "v_mfma_f32_16x16x32_bf16 v[188:191], a[216:219], v[16:19], v[188:191]\n"
    "buffer_load_dwordx4 v229, s[20:23], s36 offen offset:1280 nt lds\n"
    "v_fma_f32 v82, v82, s40, v233\n"
    "v_mfma_f32_16x16x32_bf16 v[188:191], a[220:223], v[24:27], v[188:191]\n"
    "s_add_u32 s36, s36, 24576\n"
    "v_exp_f32 v97, v97\n"
    "v_fma_f32 v98, v98, s40, v234\n"
    "v_mfma_f32_16x16x32_bf16 a[108:111], a[216:219], v[32:35], a[108:111]\n"
    "ds_read_b64_tr_b16 a[208:209], v226 offset:2048\n"
    "v_exp_f32 v113, v113\n"
    "v_mfma_f32_16x16x32_bf16 a[108:111], a[220:223], v[40:43], a[108:111]\n"
    "ds_read_b64_tr_b16 a[210:211], v226 offset:8192\n"
    "v_exp_f32 v66, v66\n"
    "v_fma_f32 v114, v114, s40, v235\n"
    "v_mfma_f32_16x16x32_bf16 a[156:159], a[216:219], v[48:51], a[156:159]\n"
    "ds_read_b64_tr_b16 a[212:213], v226 offset:14336\n"
    "v_exp_f32 v82, v82\n"
    "v_mfma_f32_16x16x32_bf16 a[156:159], a[220:223], v[56:59], a[156:159]\n"
    "ds_read_b64_tr_b16 a[214:215], v226 offset:20480\n"
    "v_exp_f32 v98, v98\n"
    "v_cvt_pk_bf16_f32 v64, v64, v65\n"
    "v_mfma_f32_16x16x32_bf16 v[144:147], a[224:227], v[0:3], v[144:147]\n"
    "ds_read_b64_tr_b16 a[216:217], v227 offset:2048\n"
    "v_exp_f32 v114, v114\n"
    "v_mfma_f32_16x16x32_bf16 v[144:147], a[228:231], v[8:11], v[144:147]\n"
    "ds_read_b64_tr_b16 a[218:219], v227 offset:8192\n"
    "v_fma_f32 v67, v67, s40, v232\n"
    "v_exp_f32 v67, v67\n"
    "v_mfma_f32_16x16x32_bf16 v[192:195], a[224:227], v[16:19], v[192:195]\n"
    "ds_read_b64_tr_b16 a[220:221], v227 offset:14336\n"
    "v_cvt_pk_bf16_f32 v80, v80, v81\n"
    "v_fma_f32 v83, v83, s40, v233\n"
    "v_mfma_f32_16x16x32_bf16 v[192:195], a[228:231], v[24:27], v[192:195]\n"
    "ds_read_b128 v[244:247], v249 offset:15360\n"
    "v_exp_f32 v83, v83\n"
    "v_cvt_pk_bf16_f32 v96, v96, v97\n"
    "v_mfma_f32_16x16x32_bf16 a[112:115], a[224:227], v[32:35], a[112:115]\n"
    "ds_read_b64_tr_b16 a[222:223], v227 offset:20480\n"
    "v_fma_f32 v99, v99, s40, v234\n"
    "v_cvt_pk_bf16_f32 v112, v112, v113\n"
    "v_mfma_f32_16x16x32_bf16 a[112:115], a[228:231], v[40:43], a[112:115]\n"
    "v_exp_f32 v99, v99\n"
    "v_fma_f32 v115, v115, s40, v235\n"
    "v_fma_f32 v68, v68, s40, v232\n"
    "v_mfma_f32_16x16x32_bf16 a[160:163], a[224:227], v[48:51], a[160:163]\n"
    "v_exp_f32 v115, v115\n"
    "v_fma_f32 v84, v84, s40, v233\n"
    "v_mfma_f32_16x16x32_bf16 a[160:163], a[228:231], v[56:59], a[160:163]\n"
    "v_exp_f32 v68, v68\n"
    "v_exp_f32 v84, v84\n"
    "v_mfma_f32_16x16x32_bf16 a[244:247], v[240:243], v[16:19], a[244:247]\n"
    "ds_read_b64_tr_b16 a[224:225], v226 offset:2560\n"
    "v_fma_f32 v100, v100, s40, v234\n"
    "v_fma_f32 v116, v116, s40, v235\n"
    "v_mfma_f32_16x16x32_bf16 a[244:247], v[240:243], v[24:27], a[244:247]\n"
    "v_exp_f32 v100, v100\n"
    "v_exp_f32 v116, v116\n"
    "v_mfma_f32_16x16x32_bf16 v[148:151], a[232:235], v[0:3], v[148:151]\n"
    "ds_read_b64_tr_b16 a[226:227], v226 offset:8704\n"
    "v_cvt_pk_bf16_f32 v65, v66, v67\n"
    "v_fma_f32 v69, v69, s40, v232\n"
    "v_mfma_f32_16x16x32_bf16 v[148:151], a[236:239], v[8:11], v[148:151]\n"
    "v_exp_f32 v69, v69\n"
    "v_cvt_pk_bf16_f32 v81, v82, v83\n"
    "v_fma_f32 v85, v85, s40, v233\n"
    "v_mfma_f32_16x16x32_bf16 v[196:199], a[232:235], v[16:19], v[196:199]\n"
    "v_exp_f32 v85, v85\n"
    "v_cvt_pk_bf16_f32 v97, v98, v99\n"
    "v_mfma_f32_16x16x32_bf16 v[196:199], a[236:239], v[24:27], v[196:199]\n"
    "ds_read_b64_tr_b16 a[228:229], v226 offset:14848\n"
    "v_fma_f32 v101, v101, s40, v234\n"
    "v_exp_f32 v101, v101\n"
    "v_mfma_f32_16x16x32_bf16 a[116:119], a[232:235], v[32:35], a[116:119]\n"
    "v_cvt_pk_bf16_f32 v113, v114, v115\n"
    "v_fma_f32 v117, v117, s40, v235\n"
    "v_fma_f32 v70, v70, s40, v232\n"
    "v_mfma_f32_16x16x32_bf16 a[116:119], a[236:239], v[40:43], a[116:119]\n"
    "v_exp_f32 v117, v117\n"
    "v_exp_f32 v70, v70\n"
    "v_mfma_f32_16x16x32_bf16 a[164:167], a[232:235], v[48:51], a[164:167]\n"
    "ds_read_b64_tr_b16 a[230:231], v226 offset:20992\n"
    "v_fma_f32 v86, v86, s40, v233\n"
    "v_fma_f32 v102, v102, s40, v234\n"
    "v_mfma_f32_16x16x32_bf16 a[164:167], a[236:239], v[56:59], a[164:167]\n"
    "s_waitcnt lgkmcnt(5)\n"
    "v_exp_f32 v86, v86\n"
    "v_fma_f32 v118, v118, s40, v235\n"
    "v_mfma_f32_16x16x32_bf16 v[152:155], a[192:195], v[0:3], v[152:155]\n"
    "buffer_store_dwordx4 v[244:247], v250, s[84:87], s44 offen offset:3200 nt sc1\n"
    "v_exp_f32 v102, v102\n"
    "v_mfma_f32_16x16x32_bf16 v[152:155], a[196:199], v[8:11], v[152:155]\n"
    "ds_read_b64_tr_b16 a[232:233], v227 offset:2560\n"
    "v_exp_f32 v118, v118\n"
    "v_mfma_f32_16x16x32_bf16 v[200:203], a[192:195], v[16:19], v[200:203]\n"
    "v_cvt_pk_bf16_f32 v66, v68, v69\n"
    "v_fma_f32 v71, v71, s40, v232\n"
    "v_exp_f32 v71, v71\n"
    "v_mfma_f32_16x16x32_bf16 v[200:203], a[196:199], v[24:27], v[200:203]\n"
    "v_cvt_pk_bf16_f32 v82, v84, v85\n"
    "v_fma_f32 v87, v87, s40, v233\n"
    "v_cvt_pk_bf16_f32 v98, v100, v101\n"
    "v_mfma_f32_16x16x32_bf16 a[120:123], a[192:195], v[32:35], a[120:123]\n"
    "s_waitcnt vmcnt(9)\n"
    "ds_read_b128 v[244:247], v249 offset:3072\n"
    "v_exp_f32 v87, v87\n"
    "v_mfma_f32_16x16x32_bf16 a[120:123], a[196:199], v[40:43], a[120:123]\n"
    "ds_read_b64_tr_b16 a[234:235], v227 offset:8704\n"
    "v_fma_f32 v103, v103, s40, v234\n"
    "v_cvt_pk_bf16_f32 v114, v116, v117\n"
    "v_mfma_f32_16x16x32_bf16 a[168:171], a[192:195], v[48:51], a[168:171]\n"
    "ds_read_b128 a[0:3], v253 offset:0\n"
    "v_exp_f32 v103, v103\n"
    "v_fma_f32 v119, v119, s40, v235\n"
    "v_mfma_f32_16x16x32_bf16 a[168:171], a[196:199], v[56:59], a[168:171]\n"
    "ds_read_b128 a[4:7], v254 offset:0\n"
    "v_exp_f32 v119, v119\n"
    "v_mfma_f32_16x16x32_bf16 v[156:159], a[200:203], v[0:3], v[156:159]\n"
    "v_fma_f32 v72, v72, s40, v232\n"
    "v_exp_f32 v72, v72\n"
    "v_fma_f32 v88, v88, s40, v233\n"
    "v_mfma_f32_16x16x32_bf16 v[156:159], a[204:207], v[8:11], v[156:159]\n"
    "v_exp_f32 v88, v88\n"
    "v_fma_f32 v104, v104, s40, v234\n"
    "v_mfma_f32_16x16x32_bf16 v[204:207], a[200:203], v[16:19], v[204:207]\n"
    "ds_read_b64_tr_b16 a[236:237], v227 offset:14848\n"
    "v_exp_f32 v104, v104\n"
    "v_fma_f32 v120, v120, s40, v235\n"
    "v_mfma_f32_16x16x32_bf16 v[204:207], a[204:207], v[24:27], v[204:207]\n"
    "ds_read_b128 a[24:27], v253 offset:2048\n"
    "v_exp_f32 v120, v120\n"
    "v_mfma_f32_16x16x32_bf16 a[124:127], a[200:203], v[32:35], a[124:127]\n"
    "v_cvt_pk_bf16_f32 v67, v70, v71\n"
    "v_fma_f32 v73, v73, s40, v232\n"
    "v_exp_f32 v73, v73\n"
    "v_mfma_f32_16x16x32_bf16 a[124:127], a[204:207], v[40:43], a[124:127]\n"
    "ds_read_b128 a[28:31], v254 offset:2048\n"
    "v_cvt_pk_bf16_f32 v83, v86, v87\n"
    "v_fma_f32 v89, v89, s40, v233\n"
    "v_mfma_f32_16x16x32_bf16 a[172:175], a[200:203], v[48:51], a[172:175]\n"
    "s_waitcnt lgkmcnt(6)\n"
    "v_exp_f32 v89, v89\n"
    "v_cvt_pk_bf16_f32 v99, v102, v103\n"
    "v_mfma_f32_16x16x32_bf16 a[172:175], a[204:207], v[56:59], a[172:175]\n"
    "buffer_store_dwordx4 v[244:247], v250, s[84:87], s42 offen offset:3328 nt sc1\n"
    "v_fma_f32 v105, v105, s40, v234\n"
    "v_mfma_f32_16x16x32_bf16 a[248:251], v[240:243], v[32:35], a[248:251]\n"
    "ds_read_b64_tr_b16 a[238:239], v227 offset:20992\n"
    "v_exp_f32 v105, v105\n"
    "v_cvt_pk_bf16_f32 v115, v118, v119\n"
    "v_mfma_f32_16x16x32_bf16 a[248:251], v[240:243], v[40:43], a[248:251]\n"
    "ds_read_b128 a[48:51], v253 offset:4096\n"
    "v_fma_f32 v121, v121, s40, v235\n"
    "v_fma_f32 v74, v74, s40, v232\n"
    "v_mfma_f32_16x16x32_bf16 v[160:163], a[208:211], v[0:3], v[160:163]\n"
    "ds_read_b128 a[52:55], v254 offset:4096\n"
    "v_exp_f32 v121, v121\n"
    "v_fma_f32 v90, v90, s40, v233\n"
    "v_mfma_f32_16x16x32_bf16 v[160:163], a[212:215], v[8:11], v[160:163]\n"
    "ds_read_b128 v[244:247], v249 offset:7168\n"
    "v_exp_f32 v74, v74\n"
    "v_mfma_f32_16x16x32_bf16 v[208:211], a[208:211], v[16:19], v[208:211]\n"
    "v_exp_f32 v90, v90\n"
    "v_fma_f32 v106, v106, s40, v234\n"
    "v_fma_f32 v122, v122, s40, v235\n"
    "v_mfma_f32_16x16x32_bf16 v[208:211], a[212:215], v[24:27], v[208:211]\n"
    "ds_read_b128 a[72:75], v253 offset:6144\n"
    "v_exp_f32 v106, v106\n"
    "v_mfma_f32_16x16x32_bf16 a[128:131], a[208:211], v[32:35], a[128:131]\n"
    "v_exp_f32 v122, v122\n"
    "v_cvt_pk_bf16_f32 v72, v72, v73\n"
    "v_fma_f32 v75, v75, s40, v232\n"
    "v_mfma_f32_16x16x32_bf16 a[128:131], a[212:215], v[40:43], a[128:131]\n"
    "v_exp_f32 v75, v75\n"
    "v_cvt_pk_bf16_f32 v88, v88, v89\n"
    "v_mfma_f32_16x16x32_bf16 a[176:179], a[208:211], v[48:51], a[176:179]\n"
    "ds_read_b128 a[76:79], v254 offset:6144\n"
    "v_fma_f32 v91, v91, s40, v233\n"
    "v_exp_f32 v91, v91\n"
    "v_mfma_f32_16x16x32_bf16 a[176:179], a[212:215], v[56:59], a[176:179]\n"
    "v_cvt_pk_bf16_f32 v104, v104, v105\n"
    "v_fma_f32 v107, v107, s40, v234\n"
    "v_cvt_pk_bf16_f32 v120, v120, v121\n"
    "v_mfma_f32_16x16x32_bf16 v[164:167], a[216:219], v[0:3], v[164:167]\n"
    "v_exp_f32 v107, v107\n"
    "v_fma_f32 v123, v123, s40, v235\n"
    "v_fma_f32 v76, v76, s40, v232\n"
    "v_mfma_f32_16x16x32_bf16 v[164:167], a[220:223], v[8:11], v[164:167]\n"
    "s_waitcnt vmcnt(12) lgkmcnt(5)\n"
    "ds_read_b128 a[8:11], v253 offset:8192\n"
    "v_fma_f32 v92, v92, s40, v233\n"
    "v_mfma_f32_16x16x32_bf16 v[212:215], a[216:219], v[16:19], v[212:215]\n"
    "s_barrier\n"
    "v_exp_f32 v123, v123\n"
    "v_fma_f32 v108, v108, s40, v234\n"
    "v_mfma_f32_16x16x32_bf16 v[212:215], a[220:223], v[24:27], v[212:215]\n"
    "ds_read_b64_tr_b16 a[192:193], v226 offset:24576\n"
    "ds_read_b128 a[12:15], v254 offset:8192\n"
    "s_waitcnt lgkmcnt(5)\n"
    "v_fma_f32 v124, v124, s40, v235\n"
    "v_mfma_f32_16x16x32_bf16 a[132:135], a[216:219], v[32:35], a[132:135]\n"
    "ds_read_b64_tr_b16 a[194:195], v226 offset:30720\n"
    "buffer_store_dwordx4 v[244:247], v250, s[84:87], s44 offen offset:3328 nt sc1\n"
    "v_exp_f32 v76, v76\n"
    "v_mfma_f32_16x16x32_bf16 a[132:135], a[220:223], v[40:43], a[132:135]\n"
    "ds_read_b64_tr_b16 a[196:197], v226 offset:36864\n"
    "v_exp_f32 v92, v92\n"
    "v_mfma_f32_16x16x32_bf16 a[180:183], a[216:219], v[48:51], a[180:183]\n"
    "ds_read_b64_tr_b16 a[198:199], v226 offset:43008\n"
    "ds_read_b128 a[32:35], v253 offset:10240\n"
    "v_exp_f32 v108, v108\n"
    "v_mfma_f32_16x16x32_bf16 a[180:183], a[220:223], v[56:59], a[180:183]\n"
    "ds_read_b64_tr_b16 a[200:201], v227 offset:24576\n"
    "v_exp_f32 v124, v124\n"
    "v_mfma_f32_16x16x32_bf16 v[168:171], a[224:227], v[0:3], v[168:171]\n"
    "ds_read_b64_tr_b16 a[202:203], v227 offset:30720\n"
    "v_cvt_pk_bf16_f32 v73, v74, v75\n"
    "v_fma_f32 v77, v77, s40, v232\n"
    "v_cvt_pk_bf16_f32 v89, v90, v91\n"
    "v_mfma_f32_16x16x32_bf16 v[168:171], a[228:231], v[8:11], v[168:171]\n"
    "ds_read_b64_tr_b16 a[204:205], v227 offset:36864\n"
    "ds_read_b128 a[36:39], v254 offset:10240\n"
    "v_fma_f32 v93, v93, s40, v233\n"
    "v_mfma_f32_16x16x32_bf16 v[216:219], a[224:227], v[16:19], v[216:219]\n"
    "ds_read_b64_tr_b16 a[206:207], v227 offset:43008\n"
    "v_exp_f32 v77, v77\n"
    "v_cvt_pk_bf16_f32 v105, v106, v107\n"
    "v_mfma_f32_16x16x32_bf16 v[216:219], a[228:231], v[24:27], v[216:219]\n"
    "ds_read_b64_tr_b16 a[208:209], v226 offset:25088\n"
    "v_exp_f32 v93, v93\n"
    "v_mfma_f32_16x16x32_bf16 a[136:139], a[224:227], v[32:35], a[136:139]\n"
    "ds_read_b64_tr_b16 a[210:211], v226 offset:31232\n"
    "ds_read_b128 a[56:59], v253 offset:12288\n"
    "v_fma_f32 v109, v109, s40, v234\n"
    "v_cvt_pk_bf16_f32 v121, v122, v123\n"
    "v_mfma_f32_16x16x32_bf16 a[136:139], a[228:231], v[40:43], a[136:139]\n"
    "ds_read_b64_tr_b16 a[212:213], v226 offset:37376\n"
    "v_exp_f32 v109, v109\n"
    "v_mfma_f32_16x16x32_bf16 a[184:187], a[224:227], v[48:51], a[184:187]\n"
    "ds_read_b64_tr_b16 a[214:215], v226 offset:43520\n"
    "v_fma_f32 v125, v125, s40, v235\n"
    "v_exp_f32 v125, v125\n"
    "v_mfma_f32_16x16x32_bf16 a[184:187], a[228:231], v[56:59], a[184:187]\n"
    "ds_read_b128 a[60:63], v254 offset:12288\n"
    "v_fma_f32 v78, v78, s40, v232\n"
    "v_fma_f32 v94, v94, s40, v233\n"
    "v_mfma_f32_16x16x32_bf16 a[252:255], v[240:243], v[48:51], a[252:255]\n"
    "v_exp_f32 v78, v78\n"
    "v_exp_f32 v94, v94\n"
    "v_mfma_f32_16x16x32_bf16 a[252:255], v[240:243], v[56:59], a[252:255]\n"
    "ds_read_b128 a[80:83], v253 offset:14336\n"
    "v_fma_f32 v110, v110, s40, v234\n"
    "v_fma_f32 v126, v126, s40, v235\n"
    "v_mfma_f32_16x16x32_bf16 v[172:175], a[232:235], v[0:3], v[172:175]\n"
    "v_exp_f32 v110, v110\n"
    "v_exp_f32 v126, v126\n"
    "v_mfma_f32_16x16x32_bf16 v[172:175], a[236:239], v[8:11], v[172:175]\n"
    "v_cvt_pk_bf16_f32 v74, v76, v77\n"
    "v_fma_f32 v79, v79, s40, v232\n"
    "v_exp_f32 v79, v79\n"
    "v_mfma_f32_16x16x32_bf16 v[220:223], a[232:235], v[16:19], v[220:223]\n"
    "ds_read_b128 a[84:87], v254 offset:14336\n"
    "v_cvt_pk_bf16_f32 v90, v92, v93\n"
    "v_fma_f32 v95, v95, s40, v233\n"
    "v_mfma_f32_16x16x32_bf16 v[220:223], a[236:239], v[24:27], v[220:223]\n"
    "v_exp_f32 v95, v95\n"
    "v_cvt_pk_bf16_f32 v106, v108, v109\n"
    "v_mfma_f32_16x16x32_bf16 a[140:143], a[232:235], v[32:35], a[140:143]\n"
    "v_fma_f32 v111, v111, s40, v234\n"
    "v_exp_f32 v111, v111\n"
    "v_cvt_pk_bf16_f32 v122, v124, v125\n"
    "v_mfma_f32_16x16x32_bf16 a[140:143], a[236:239], v[40:43], a[140:143]\n"
    "v_fma_f32 v127, v127, s40, v235\n"
    "v_exp_f32 v127, v127\n"
    "v_cvt_pk_bf16_f32 v75, v78, v79\n"
    "v_mfma_f32_16x16x32_bf16 a[188:191], a[232:235], v[48:51], a[188:191]\n"
    "v_cvt_pk_bf16_f32 v91, v94, v95\n"
    "v_cvt_pk_bf16_f32 v107, v110, v111\n"
    "v_cvt_pk_bf16_f32 v123, v126, v127\n"
    "v_mfma_f32_16x16x32_bf16 a[188:191], a[236:239], v[56:59], a[188:191]\n"
    "s_cbranch_vccnz LSRBd_%=\n"
    "LSRBbd_%=:\n"
    "v_mfma_f32_16x16x32_bf16 a[240:243], v[240:243], v[64:67], a[240:243]\n"
    "ds_read_b64_tr_b16 a[216:217], v227 offset:25088\n"
    "s_waitcnt lgkmcnt(4)\n"
    "v_mfma_f32_16x16x32_bf16 a[240:243], v[240:243], v[72:75], a[240:243]\n"
    "ds_read_b64_tr_b16 a[218:219], v227 offset:31232\n"
    "s_add_u32 m0, s49, -256\n"
    "v_mfma_f32_16x16x32_bf16 a[244:247], v[240:243], v[80:83], a[244:247]\n"
    "ds_read_b64_tr_b16 a[220:221], v227 offset:37376\n"
    "buffer_load_dwordx4 v228, s[28:31], 0 offen offset:256 nt lds\n"
    "v_mfma_f32_16x16x32_bf16 a[244:247], v[240:243], v[88:91], a[244:247]\n"
    "ds_read_b64_tr_b16 a[222:223], v227 offset:43520\n"
    "v_xor_b32 v245, 32, v248\n"
    "v_mfma_f32_16x16x32_bf16 a[248:251], v[240:243], v[96:99], a[248:251]\n"
    "ds_read_b64_tr_b16 a[224:225], v226 offset:25600\n"
    "buffer_load_dwordx4 v229, s[28:31], 0 offen offset:1280 nt lds\n"
    "v_mfma_f32_16x16x32_bf16 a[248:251], v[240:243], v[104:107], a[248:251]\n"
    "ds_read_b64_tr_b16 a[226:227], v226 offset:31744\n"
    "s_add_u32 m0, s49, 1792\n"
    "v_mfma_f32_16x16x32_bf16 a[252:255], v[240:243], v[112:115], a[252:255]\n"
    "ds_read_b64_tr_b16 a[228:229], v226 offset:37888\n"
    "v_xor_b32 v246, 64, v248\n"
    "v_mfma_f32_16x16x32_bf16 a[252:255], v[240:243], v[120:123], a[252:255]\n"
    "ds_read_b64_tr_b16 a[230:231], v226 offset:44032\n"
    "v_xor_b32 v247, 96, v248\n"
    "v_mfma_f32_16x16x32_bf16 v[128:131], a[192:195], v[64:67], v[128:131]\n"
    "buffer_load_dwordx4 v228, s[28:31], s42 offen offset:256 nt lds\n"
    "v_mfma_f32_16x16x32_bf16 v[128:131], a[196:199], v[72:75], v[128:131]\n"
    "ds_read_b64_tr_b16 a[232:233], v227 offset:25600\n"
    "v_accvgpr_read_b32 v236, a240\n"
    "v_mfma_f32_16x16x32_bf16 v[176:179], a[192:195], v[80:83], v[176:179]\n"
    "buffer_load_dwordx4 v229, s[28:31], s42 offen offset:1280 nt lds\n"
    "v_mfma_f32_16x16x32_bf16 v[176:179], a[196:199], v[88:91], v[176:179]\n"
    "ds_read_b64_tr_b16 a[234:235], v227 offset:31744\n"
    "v_accvgpr_read_b32 v237, a244\n"
    "v_mfma_f32_16x16x32_bf16 a[96:99], a[192:195], v[96:99], a[96:99]\n"
    "s_add_u32 m0, s49, 3840\n"
    "v_accvgpr_read_b32 v238, a248\n"
    "v_mfma_f32_16x16x32_bf16 a[96:99], a[196:199], v[104:107], a[96:99]\n"
    "ds_read_b64_tr_b16 a[236:237], v227 offset:37888\n"
    "v_accvgpr_read_b32 v239, a252\n"
    "v_mfma_f32_16x16x32_bf16 a[144:147], a[192:195], v[112:115], a[144:147]\n"
    "buffer_load_dwordx4 v228, s[28:31], s43 offen offset:256 nt lds\n"
    "v_mfma_f32_16x16x32_bf16 a[144:147], a[196:199], v[120:123], a[144:147]\n"
    "v_rcp_f32 v236, v236\n"
    "v_mfma_f32_16x16x32_bf16 v[132:135], a[200:203], v[64:67], v[132:135]\n"
    "ds_read_b64_tr_b16 a[238:239], v227 offset:44032\n"
    "buffer_load_dwordx4 v229, s[28:31], s43 offen offset:1280 nt lds\n"
    "v_mfma_f32_16x16x32_bf16 v[132:135], a[204:207], v[72:75], v[132:135]\n"
    "ds_read_b64_tr_b16 a[192:193], v226 offset:26112\n"
    "s_add_u32 m0, s49, 5888\n"
    "v_mfma_f32_16x16x32_bf16 v[180:183], a[200:203], v[80:83], v[180:183]\n"
    "ds_read_b64_tr_b16 a[194:195], v226 offset:32256\n"
    "v_mul_f32 v128, v128, v236\n"
    "v_mfma_f32_16x16x32_bf16 v[180:183], a[204:207], v[88:91], v[180:183]\n"
    "ds_read_b64_tr_b16 a[196:197], v226 offset:38400\n"
    "v_mul_f32 v129, v129, v236\n"
    "v_mfma_f32_16x16x32_bf16 a[100:103], a[200:203], v[96:99], a[100:103]\n"
    "ds_read_b64_tr_b16 a[198:199], v226 offset:44544\n"
    "v_mul_f32 v130, v130, v236\n"
    "v_mfma_f32_16x16x32_bf16 a[100:103], a[204:207], v[104:107], a[100:103]\n"
    "buffer_load_dwordx4 v228, s[28:31], s44 offen offset:256 nt lds\n"
    "v_mfma_f32_16x16x32_bf16 a[148:151], a[200:203], v[112:115], a[148:151]\n"
    "v_rcp_f32 v237, v237\n"
    "v_mfma_f32_16x16x32_bf16 a[148:151], a[204:207], v[120:123], a[148:151]\n"
    "ds_read_b64_tr_b16 a[200:201], v227 offset:26112\n"
    "v_mul_f32 v176, v176, v237\n"
    "v_mfma_f32_16x16x32_bf16 v[136:139], a[208:211], v[64:67], v[136:139]\n"
    "ds_read_b64_tr_b16 a[202:203], v227 offset:32256\n"
    "v_mul_f32 v177, v177, v237\n"
    "v_mfma_f32_16x16x32_bf16 v[136:139], a[212:215], v[72:75], v[136:139]\n"
    "buffer_load_dwordx4 v229, s[28:31], s44 offen offset:1280 nt lds\n"
    "v_mfma_f32_16x16x32_bf16 v[184:187], a[208:211], v[80:83], v[184:187]\n"
    "ds_read_b64_tr_b16 a[204:205], v227 offset:38400\n"
    "v_mul_f32 v178, v178, v237\n"
    "v_mfma_f32_16x16x32_bf16 v[184:187], a[212:215], v[88:91], v[184:187]\n"
    "v_rcp_f32 v238, v238\n"
    "v_mfma_f32_16x16x32_bf16 a[104:107], a[208:211], v[96:99], a[104:107]\n"
    "ds_read_b64_tr_b16 a[206:207], v227 offset:44544\n"
    "v_mul_f32 v131, v131, v236\n"
    "v_mfma_f32_16x16x32_bf16 a[104:107], a[212:215], v[104:107], a[104:107]\n"
    "v_rcp_f32 v239, v239\n"
    "v_mfma_f32_16x16x32_bf16 a[152:155], a[208:211], v[112:115], a[152:155]\n"
    "v_mul_f32 v179, v179, v237\n"
    "v_cvt_pk_bf16_f32 v128, v128, v129\n"
    "v_mfma_f32_16x16x32_bf16 a[152:155], a[212:215], v[120:123], a[152:155]\n"
    "s_waitcnt lgkmcnt(8)\n"
    "v_cvt_pk_bf16_f32 v176, v176, v177\n"
    "v_mfma_f32_16x16x32_bf16 v[140:143], a[216:219], v[64:67], v[140:143]\n"
    "ds_read_b64_tr_b16 a[208:209], v226 offset:26624\n"
    "v_cvt_pk_bf16_f32 v129, v130, v131\n"
    "v_mfma_f32_16x16x32_bf16 v[140:143], a[220:223], v[72:75], v[140:143]\n"
    "ds_write_b64 v248, v[128:129] offset:8192\n"
    "v_mfma_f32_16x16x32_bf16 v[188:191], a[216:219], v[80:83], v[188:191]\n"
    "ds_read_b64_tr_b16 a[210:211], v226 offset:32768\n"
    "v_cvt_pk_bf16_f32 v177, v178, v179\n"
    "v_mfma_f32_16x16x32_bf16 v[188:191], a[220:223], v[88:91], v[188:191]\n"
    "ds_write_b64 v248, v[176:177] offset:10240\n"
    "v_mfma_f32_16x16x32_bf16 a[108:111], a[216:219], v[96:99], a[108:111]\n"
    "v_accvgpr_read_b32 v68, a96\n"
    "v_accvgpr_read_b32 v76, a144\n"
    "v_mfma_f32_16x16x32_bf16 a[108:111], a[220:223], v[104:107], a[108:111]\n"
    "ds_read_b64_tr_b16 a[212:213], v226 offset:38912\n"
    "v_accvgpr_read_b32 v69, a97\n"
    "v_mfma_f32_16x16x32_bf16 a[156:159], a[216:219], v[112:115], a[156:159]\n"
    "v_accvgpr_read_b32 v77, a145\n"
    "v_accvgpr_read_b32 v70, a98\n"
    "v_mfma_f32_16x16x32_bf16 a[156:159], a[220:223], v[120:123], a[156:159]\n"
    "v_accvgpr_read_b32 v78, a146\n"
    "v_accvgpr_read_b32 v71, a99\n"
    "v_mfma_f32_16x16x32_bf16 v[144:147], a[224:227], v[64:67], v[144:147]\n"
    "ds_read_b64_tr_b16 a[214:215], v226 offset:45056\n"
    "v_accvgpr_read_b32 v79, a147\n"
    "v_mfma_f32_16x16x32_bf16 v[144:147], a[228:231], v[72:75], v[144:147]\n"
    "ds_read_b64_tr_b16 a[216:217], v227 offset:26624\n"
    "v_mul_f32 v68, v68, v238\n"
    "v_mfma_f32_16x16x32_bf16 v[192:195], a[224:227], v[80:83], v[192:195]\n"
    "ds_read_b64_tr_b16 a[218:219], v227 offset:32768\n"
    "v_mul_f32 v76, v76, v239\n"
    "v_mfma_f32_16x16x32_bf16 v[192:195], a[228:231], v[88:91], v[192:195]\n"
    "ds_read_b64_tr_b16 a[220:221], v227 offset:38912\n"
    "v_mul_f32 v69, v69, v238\n"
    "v_mfma_f32_16x16x32_bf16 a[112:115], a[224:227], v[96:99], a[112:115]\n"
    "ds_read_b64_tr_b16 a[222:223], v227 offset:45056\n"
    "v_mul_f32 v77, v77, v239\n"
    "v_mfma_f32_16x16x32_bf16 a[112:115], a[228:231], v[104:107], a[112:115]\n"
    "v_mul_f32 v70, v70, v238\n"
    "v_mul_f32 v78, v78, v239\n"
    "v_mfma_f32_16x16x32_bf16 a[160:163], a[224:227], v[112:115], a[160:163]\n"
    "v_mul_f32 v71, v71, v238\n"
    "v_mul_f32 v79, v79, v239\n"
    "v_mfma_f32_16x16x32_bf16 a[160:163], a[228:231], v[120:123], a[160:163]\n"
    "ds_read_b64_tr_b16 a[224:225], v226 offset:27136\n"
    "v_cvt_pk_bf16_f32 v68, v68, v69\n"
    "v_mfma_f32_16x16x32_bf16 v[148:151], a[232:235], v[64:67], v[148:151]\n"
    "v_cvt_pk_bf16_f32 v76, v76, v77\n"
    "v_cvt_pk_bf16_f32 v69, v70, v71\n"
    "v_mfma_f32_16x16x32_bf16 v[148:151], a[236:239], v[72:75], v[148:151]\n"
    "ds_read_b64_tr_b16 a[226:227], v226 offset:33280\n"
    "v_cvt_pk_bf16_f32 v77, v78, v79\n"
    "v_mfma_f32_16x16x32_bf16 v[196:199], a[232:235], v[80:83], v[196:199]\n"
    "ds_write_b64 v248, v[68:69] offset:12288\n"
    "v_mfma_f32_16x16x32_bf16 v[196:199], a[236:239], v[88:91], v[196:199]\n"
    "ds_read_b64_tr_b16 a[228:229], v226 offset:39424\n"
    "v_mul_f32 v132, v132, v236\n"
    "v_mfma_f32_16x16x32_bf16 a[116:119], a[232:235], v[96:99], a[116:119]\n"
    "ds_write_b64 v248, v[76:77] offset:14336\n"
    "v_mfma_f32_16x16x32_bf16 a[116:119], a[236:239], v[104:107], a[116:119]\n"
    "ds_read_b64_tr_b16 a[230:231], v226 offset:45568\n"
    "v_mul_f32 v180, v180, v237\n"
    "v_mfma_f32_16x16x32_bf16 a[164:167], a[232:235], v[112:115], a[164:167]\n"
    "v_mul_f32 v133, v133, v236\n"
    "v_mul_f32 v181, v181, v237\n"
    "v_mfma_f32_16x16x32_bf16 a[164:167], a[236:239], v[120:123], a[164:167]\n"
    "s_waitcnt lgkmcnt(10)\n"
    "v_mul_f32 v134, v134, v236\n"
    "v_mfma_f32_16x16x32_bf16 v[152:155], a[192:195], v[64:67], v[152:155]\n"
    "ds_read_b64_tr_b16 a[232:233], v227 offset:27136\n"
    "v_mul_f32 v182, v182, v237\n"
    "v_mfma_f32_16x16x32_bf16 v[152:155], a[196:199], v[72:75], v[152:155]\n"
    "v_mul_f32 v135, v135, v236\n"
    "v_mul_f32 v183, v183, v237\n"
    "v_mfma_f32_16x16x32_bf16 v[200:203], a[192:195], v[80:83], v[200:203]\n"
    "s_mov_b32 m0, s58\n"
    "v_cvt_pk_bf16_f32 v132, v132, v133\n"
    "v_mfma_f32_16x16x32_bf16 v[200:203], a[196:199], v[88:91], v[200:203]\n"
    "ds_read_b64_tr_b16 a[234:235], v227 offset:33280\n"
    "v_cvt_pk_bf16_f32 v180, v180, v181\n"
    "v_mfma_f32_16x16x32_bf16 a[120:123], a[192:195], v[96:99], a[120:123]\n"
    "buffer_load_dwordx4 v230, s[24:27], s37 offen offset:0 nt lds\n"
    "v_mfma_f32_16x16x32_bf16 a[120:123], a[196:199], v[104:107], a[120:123]\n"
    "v_cvt_pk_bf16_f32 v133, v134, v135\n"
    "v_cvt_pk_bf16_f32 v181, v182, v183\n"
    "v_mfma_f32_16x16x32_bf16 a[168:171], a[192:195], v[112:115], a[168:171]\n"
    "ds_write_b64 v245, v[132:133] offset:8192\n"
    "v_mfma_f32_16x16x32_bf16 a[168:171], a[196:199], v[120:123], a[168:171]\n"
    "ds_read_b64_tr_b16 a[236:237], v227 offset:39424\n"
    "v_accvgpr_read_b32 v84, a100\n"
    "v_mfma_f32_16x16x32_bf16 v[156:159], a[200:203], v[64:67], v[156:159]\n"
    "ds_write_b64 v245, v[180:181] offset:10240\n"
    "v_mfma_f32_16x16x32_bf16 v[156:159], a[204:207], v[72:75], v[156:159]\n"
    "buffer_load_dwordx4 v230, s[24:27], s37 offen offset:3072 nt lds\n"
    "v_mfma_f32_16x16x32_bf16 v[204:207], a[200:203], v[80:83], v[204:207]\n"
    "v_accvgpr_read_b32 v92, a148\n"
    "v_accvgpr_read_b32 v85, a101\n"
    "v_mfma_f32_16x16x32_bf16 v[204:207], a[204:207], v[88:91], v[204:207]\n"
    "v_accvgpr_read_b32 v93, a149\n"
    "v_accvgpr_read_b32 v86, a102\n"
    "v_mfma_f32_16x16x32_bf16 a[124:127], a[200:203], v[96:99], a[124:127]\n"
    "ds_read_b64_tr_b16 a[238:239], v227 offset:45568\n"
    "v_accvgpr_read_b32 v94, a150\n"
    "v_mfma_f32_16x16x32_bf16 a[124:127], a[204:207], v[104:107], a[124:127]\n"
    "s_mov_b32 m0, s59\n"
    "v_accvgpr_read_b32 v87, a103\n"
    "v_mfma_f32_16x16x32_bf16 a[172:175], a[200:203], v[112:115], a[172:175]\n"
    "buffer_load_dwordx4 v230, s[24:27], s37 offen offset:128 nt lds\n"
    "v_mfma_f32_16x16x32_bf16 a[172:175], a[204:207], v[120:123], a[172:175]\n"
    "v_accvgpr_read_b32 v95, a151\n"
    "v_mul_f32 v84, v84, v238\n"
    "v_mfma_f32_16x16x32_bf16 v[160:163], a[208:211], v[64:67], v[160:163]\n"
    "v_mul_f32 v92, v92, v239\n"
    "v_mul_f32 v85, v85, v238\n"
    "v_mfma_f32_16x16x32_bf16 v[160:163], a[212:215], v[72:75], v[160:163]\n"
    "v_mul_f32 v93, v93, v239\n"
    "v_mul_f32 v86, v86, v238\n"
    "v_mfma_f32_16x16x32_bf16 v[208:211], a[208:211], v[80:83], v[208:211]\n"
    "v_mul_f32 v94, v94, v239\n"
    "v_mul_f32 v87, v87, v238\n"
    "v_mfma_f32_16x16x32_bf16 v[208:211], a[212:215], v[88:91], v[208:211]\n"
    "buffer_load_dwordx4 v230, s[24:27], s37 offen offset:3200 nt lds\n"
    "v_mfma_f32_16x16x32_bf16 a[128:131], a[208:211], v[96:99], a[128:131]\n"
    "v_mul_f32 v95, v95, v239\n"
    "v_cvt_pk_bf16_f32 v84, v84, v85\n"
    "v_mfma_f32_16x16x32_bf16 a[128:131], a[212:215], v[104:107], a[128:131]\n"
    "v_cvt_pk_bf16_f32 v92, v92, v93\n"
    "v_cvt_pk_bf16_f32 v85, v86, v87\n"
    "v_mfma_f32_16x16x32_bf16 a[176:179], a[208:211], v[112:115], a[176:179]\n"
    "s_mov_b32 m0, s60\n"
    "v_cvt_pk_bf16_f32 v93, v94, v95\n"
    "v_mfma_f32_16x16x32_bf16 a[176:179], a[212:215], v[120:123], a[176:179]\n"
    "s_waitcnt vmcnt(32) lgkmcnt(0)\n"
    "v_mul_f32 v136, v136, v236\n"
    "v_mfma_f32_16x16x32_bf16 v[164:167], a[216:219], v[64:67], v[164:167]\n"
    "s_barrier\n"
    "buffer_load_dwordx4 v230, s[24:27], s37 offen offset:256 nt lds\n"
    "v_mfma_f32_16x16x32_bf16 v[164:167], a[220:223], v[72:75], v[164:167]\n"
    "ds_read_b128 a[192:195], v224 offset:0\n"
    "v_mul_f32 v184, v184, v237\n"
    "v_mfma_f32_16x16x32_bf16 v[212:215], a[216:219], v[80:83], v[212:215]\n"
    "ds_read_b128 a[196:199], v225 offset:0\n"
    "v_mul_f32 v137, v137, v236\n"
    "v_mfma_f32_16x16x32_bf16 v[212:215], a[220:223], v[88:91], v[212:215]\n"
    "ds_read_b128 a[200:203], v224 offset:2048\n"
    "v_mul_f32 v185, v185, v237\n"
    "v_mfma_f32_16x16x32_bf16 a[132:135], a[216:219], v[96:99], a[132:135]\n"
    "ds_read_b128 a[204:207], v225 offset:2048\n"
    "v_mul_f32 v138, v138, v236\n"
    "v_mfma_f32_16x16x32_bf16 a[132:135], a[220:223], v[104:107], a[132:135]\n"
    "buffer_load_dwordx4 v230, s[24:27], s37 offen offset:3328 nt lds\n"
    "v_mfma_f32_16x16x32_bf16 a[180:183], a[216:219], v[112:115], a[180:183]\n"
    "ds_read_b128 a[208:211], v224 offset:4096\n"
    "v_mul_f32 v186, v186, v237\n"
    "v_mfma_f32_16x16x32_bf16 a[180:183], a[220:223], v[120:123], a[180:183]\n"
    "s_add_u32 s37, s37, 24576\n"
    "v_mul_f32 v139, v139, v236\n"
    "v_mfma_f32_16x16x32_bf16 v[168:171], a[224:227], v[64:67], v[168:171]\n"
    "ds_read_b128 a[212:215], v225 offset:4096\n"
    "v_mul_f32 v187, v187, v237\n"
    "v_mfma_f32_16x16x32_bf16 v[168:171], a[228:231], v[72:75], v[168:171]\n"
    "ds_write_b64 v245, v[84:85] offset:12288\n"
    "v_mfma_f32_16x16x32_bf16 v[216:219], a[224:227], v[80:83], v[216:219]\n"
    "v_cvt_pk_bf16_f32 v136, v136, v137\n"
    "v_cvt_pk_bf16_f32 v184, v184, v185\n"
    "v_mfma_f32_16x16x32_bf16 v[216:219], a[228:231], v[88:91], v[216:219]\n"
    "ds_write_b64 v245, v[92:93] offset:14336\n"
    "v_mfma_f32_16x16x32_bf16 a[136:139], a[224:227], v[96:99], a[136:139]\n"
    "v_cvt_pk_bf16_f32 v137, v138, v139\n"
    "v_cvt_pk_bf16_f32 v185, v186, v187\n"
    "v_mfma_f32_16x16x32_bf16 a[136:139], a[228:231], v[104:107], a[136:139]\n"
    "ds_write_b64 v246, v[136:137] offset:8192\n"
    "v_mfma_f32_16x16x32_bf16 a[184:187], a[224:227], v[112:115], a[184:187]\n"
    "ds_read_b128 a[216:219], v224 offset:6144\n"
    "v_accvgpr_read_b32 v100, a104\n"
    "v_mfma_f32_16x16x32_bf16 a[184:187], a[228:231], v[120:123], a[184:187]\n"
    "ds_write_b64 v246, v[184:185] offset:10240\n"
    "v_mfma_f32_16x16x32_bf16 v[172:175], a[232:235], v[64:67], v[172:175]\n"
    "v_accvgpr_read_b32 v108, a152\n"
    "v_accvgpr_read_b32 v101, a105\n"
    "v_mfma_f32_16x16x32_bf16 v[172:175], a[236:239], v[72:75], v[172:175]\n"
    "v_accvgpr_read_b32 v109, a153\n"
    "v_accvgpr_read_b32 v102, a106\n"
    "v_mfma_f32_16x16x32_bf16 v[220:223], a[232:235], v[80:83], v[220:223]\n"
    "v_accvgpr_read_b32 v110, a154\n"
    "v_accvgpr_read_b32 v103, a107\n"
    "v_mfma_f32_16x16x32_bf16 v[220:223], a[236:239], v[88:91], v[220:223]\n"
    "v_accvgpr_read_b32 v111, a155\n"
    "v_mul_f32 v100, v100, v238\n"
    "v_mfma_f32_16x16x32_bf16 a[140:143], a[232:235], v[96:99], a[140:143]\n"
    "s_cmp_eq_u32 s46, 0\n"
    "v_mul_f32 v108, v108, v239\n"
    "v_mfma_f32_16x16x32_bf16 a[140:143], a[236:239], v[104:107], a[140:143]\n"
    "ds_read_b128 a[220:223], v225 offset:6144\n"
    "v_mul_f32 v101, v101, v238\n"
    "v_mfma_f32_16x16x32_bf16 a[188:191], a[232:235], v[112:115], a[188:191]\n"
    "s_waitcnt lgkmcnt(6)\n"
    "v_mul_f32 v109, v109, v239\n"
    "v_mfma_f32_16x16x32_bf16 a[188:191], a[236:239], v[120:123], a[188:191]\n"
    "s_cbranch_scc1 LSA0X_%=\n"
    "s_nop 7\n"
    "v_mul_f32 v102, v102, v238\n"
    "v_mul_f32 v110, v110, v239\n"
    "v_mul_f32 v103, v103, v238\n"
    "v_mul_f32 v111, v111, v239\n"
    "v_cvt_pk_bf16_f32 v100, v100, v101\n"
    "v_cvt_pk_bf16_f32 v108, v108, v109\n"
    "v_cvt_pk_bf16_f32 v101, v102, v103\n"
    "v_cvt_pk_bf16_f32 v109, v110, v111\n"
    "ds_write_b64 v246, v[100:101] offset:12288\n"
    "ds_write_b64 v246, v[108:109] offset:14336\n"
    "v_mul_f32 v140, v140, v236\n"
    "v_mul_f32 v188, v188, v237\n"
    "v_mul_f32 v141, v141, v236\n"
    "v_mul_f32 v189, v189, v237\n"
    "v_mul_f32 v142, v142, v236\n"
    "v_mul_f32 v190, v190, v237\n"
    "v_mul_f32 v143, v143, v236\n"
    "v_mul_f32 v191, v191, v237\n"
    "v_cvt_pk_bf16_f32 v140, v140, v141\n"
    "v_cvt_pk_bf16_f32 v188, v188, v189\n"
    "v_cvt_pk_bf16_f32 v141, v142, v143\n"
    "v_cvt_pk_bf16_f32 v189, v190, v191\n"
    "ds_write_b64 v247, v[140:141] offset:8192\n"
    "ds_write_b64 v247, v[188:189] offset:10240\n"
    "v_accvgpr_read_b32 v116, a108\n"
    "v_accvgpr_read_b32 v124, a156\n"
    "v_accvgpr_read_b32 v117, a109\n"
    "v_accvgpr_read_b32 v125, a157\n"
    "v_accvgpr_read_b32 v118, a110\n"
    "v_accvgpr_read_b32 v126, a158\n"
    "v_accvgpr_read_b32 v119, a111\n"
    "v_accvgpr_read_b32 v127, a159\n"
    "v_mul_f32 v116, v116, v238\n"
    "v_mul_f32 v124, v124, v239\n"
    "v_mul_f32 v117, v117, v238\n"
    "v_mul_f32 v125, v125, v239\n"
    "v_mul_f32 v118, v118, v238\n"
    "v_mul_f32 v126, v126, v239\n"
    "v_mul_f32 v119, v119, v238\n"
    "v_mul_f32 v127, v127, v239\n"
    "v_cvt_pk_bf16_f32 v116, v116, v117\n"
    "v_cvt_pk_bf16_f32 v124, v124, v125\n"
    "v_cvt_pk_bf16_f32 v117, v118, v119\n"
    "v_cvt_pk_bf16_f32 v125, v126, v127\n"
    "ds_write_b64 v247, v[116:117] offset:12288\n"
    "ds_write_b64 v247, v[124:125] offset:14336\n"
    "ds_read_b128 a[96:99], v249 offset:8192\n"
    "ds_read_b128 a[100:103], v249 offset:9216\n"
    "ds_read_b128 a[104:107], v249 offset:10240\n"
    "ds_read_b128 a[108:111], v249 offset:11264\n"
    "ds_read_b128 a[144:147], v249 offset:12288\n"
    "ds_read_b128 a[148:151], v249 offset:13312\n"
    "ds_read_b128 a[152:155], v249 offset:14336\n"
    "ds_read_b128 a[156:159], v249 offset:15360\n"
    "v_mul_f32 v144, v144, v236\n"
    "v_mul_f32 v192, v192, v237\n"
    "v_mul_f32 v145, v145, v236\n"
    "v_mul_f32 v193, v193, v237\n"
    "v_mul_f32 v146, v146, v236\n"
    "v_mul_f32 v194, v194, v237\n"
    "v_mul_f32 v147, v147, v236\n"
    "v_mul_f32 v195, v195, v237\n"
    "v_cvt_pk_bf16_f32 v144, v144, v145\n"
    "v_cvt_pk_bf16_f32 v192, v192, v193\n"
    "v_cvt_pk_bf16_f32 v145, v146, v147\n"
    "v_cvt_pk_bf16_f32 v193, v194, v195\n"
    "ds_write_b64 v248, v[144:145] offset:8192\n"
    "ds_write_b64 v248, v[192:193] offset:10240\n"
    "s_waitcnt lgkmcnt(6)\n"
    "buffer_store_dwordx4 a[96:99], v250, s[80:83], 0 offen offset:0 nt sc1\n"
    "buffer_store_dwordx4 a[100:103], v250, s[80:83], 0 offen offset:3072 nt sc1\n"
    "buffer_store_dwordx4 a[104:107], v250, s[80:83], s42 offen offset:0 nt sc1\n"
    "buffer_store_dwordx4 a[108:111], v250, s[80:83], s42 offen offset:3072 nt sc1\n"
    "s_waitcnt lgkmcnt(2)\n"
    "buffer_store_dwordx4 a[144:147], v250, s[80:83], s43 offen offset:0 nt sc1\n"
    "buffer_store_dwordx4 a[148:151], v250, s[80:83], s43 offen offset:3072 nt sc1\n"
    "buffer_store_dwordx4 a[152:155], v250, s[80:83], s44 offen offset:0 nt sc1\n"
    "buffer_store_dwordx4 a[156:159], v250, s[80:83], s44 offen offset:3072 nt sc1\n"
    "v_accvgpr_read_b32 v68, a112\n"
    "v_accvgpr_read_b32 v76, a160\n"
    "v_accvgpr_read_b32 v69, a113\n"
    "v_accvgpr_read_b32 v77, a161\n"
    "v_accvgpr_read_b32 v70, a114\n"
    "v_accvgpr_read_b32 v78, a162\n"
    "v_accvgpr_read_b32 v71, a115\n"
    "v_accvgpr_read_b32 v79, a163\n"
    "v_mul_f32 v68, v68, v238\n"
    "v_mul_f32 v76, v76, v239\n"
    "v_mul_f32 v69, v69, v238\n"
    "v_mul_f32 v77, v77, v239\n"
    "v_mul_f32 v70, v70, v238\n"
    "v_mul_f32 v78, v78, v239\n"
    "v_mul_f32 v71, v71, v238\n"
    "v_mul_f32 v79, v79, v239\n"
    "v_cvt_pk_bf16_f32 v68, v68, v69\n"
    "v_cvt_pk_bf16_f32 v76, v76, v77\n"
    "v_cvt_pk_bf16_f32 v69, v70, v71\n"
    "v_cvt_pk_bf16_f32 v77, v78, v79\n"
    "ds_write_b64 v248, v[68:69] offset:12288\n"
    "ds_write_b64 v248, v[76:77] offset:14336\n"
    "v_mul_f32 v148, v148, v236\n"
    "v_mul_f32 v196, v196, v237\n"
    "v_mul_f32 v149, v149, v236\n"
    "v_mul_f32 v197, v197, v237\n"
    "v_mul_f32 v150, v150, v236\n"
    "v_mul_f32 v198, v198, v237\n"
    "v_mul_f32 v151, v151, v236\n"
    "v_mul_f32 v199, v199, v237\n"
    "v_cvt_pk_bf16_f32 v148, v148, v149\n"
    "v_cvt_pk_bf16_f32 v196, v196, v197\n"
    "v_cvt_pk_bf16_f32 v149, v150, v151\n"
    "v_cvt_pk_bf16_f32 v197, v198, v199\n"
    "ds_write_b64 v245, v[148:149] offset:8192\n"
    "ds_write_b64 v245, v[196:197] offset:10240\n"
    "v_accvgpr_read_b32 v84, a116\n"
    "v_accvgpr_read_b32 v92, a164\n"
    "v_accvgpr_read_b32 v85, a117\n"
    "v_accvgpr_read_b32 v93, a165\n"
    "v_accvgpr_read_b32 v86, a118\n"
    "v_accvgpr_read_b32 v94, a166\n"
    "v_accvgpr_read_b32 v87, a119\n"
    "v_accvgpr_read_b32 v95, a167\n"
    "v_mul_f32 v84, v84, v238\n"
    "v_mul_f32 v92, v92, v239\n"
    "v_mul_f32 v85, v85, v238\n"
    "v_mul_f32 v93, v93, v239\n"
    "v_mul_f32 v86, v86, v238\n"
    "v_mul_f32 v94, v94, v239\n"
    "v_mul_f32 v87, v87, v238\n"
    "v_mul_f32 v95, v95, v239\n"
    "v_cvt_pk_bf16_f32 v84, v84, v85\n"
    "v_cvt_pk_bf16_f32 v92, v92, v93\n"
    "v_cvt_pk_bf16_f32 v85, v86, v87\n"
    "v_cvt_pk_bf16_f32 v93, v94, v95\n"
    "ds_write_b64 v245, v[84:85] offset:12288\n"
    "ds_write_b64 v245, v[92:93] offset:14336\n"
    "v_mul_f32 v152, v152, v236\n"
    "v_mul_f32 v200, v200, v237\n"
    "v_mul_f32 v153, v153, v236\n"
    "v_mul_f32 v201, v201, v237\n"
    "v_mul_f32 v154, v154, v236\n"
    "v_mul_f32 v202, v202, v237\n"
    "v_mul_f32 v155, v155, v236\n"
    "v_mul_f32 v203, v203, v237\n"
    "v_cvt_pk_bf16_f32 v152, v152, v153\n"
    "v_cvt_pk_bf16_f32 v200, v200, v201\n"
    "v_cvt_pk_bf16_f32 v153, v154, v155\n"
    "v_cvt_pk_bf16_f32 v201, v202, v203\n"
    "ds_write_b64 v246, v[152:153] offset:8192\n"
    "ds_write_b64 v246, v[200:201] offset:10240\n"
    "v_accvgpr_read_b32 v100, a120\n"
    "v_accvgpr_read_b32 v108, a168\n"
    "v_accvgpr_read_b32 v101, a121\n"
    "v_accvgpr_read_b32 v109, a169\n"
    "v_accvgpr_read_b32 v102, a122\n"
    "v_accvgpr_read_b32 v110, a170\n"
    "v_accvgpr_read_b32 v103, a123\n"
    "v_accvgpr_read_b32 v111, a171\n"
    "v_mul_f32 v100, v100, v238\n"
    "v_mul_f32 v108, v108, v239\n"
    "v_mul_f32 v101, v101, v238\n"
    "v_mul_f32 v109, v109, v239\n"
    "v_mul_f32 v102, v102, v238\n"
    "v_mul_f32 v110, v110, v239\n"
    "v_mul_f32 v103, v103, v238\n"
    "v_mul_f32 v111, v111, v239\n"
    "v_cvt_pk_bf16_f32 v100, v100, v101\n"
    "v_cvt_pk_bf16_f32 v108, v108, v109\n"
    "v_cvt_pk_bf16_f32 v101, v102, v103\n"
    "v_cvt_pk_bf16_f32 v109, v110, v111\n"
    "ds_write_b64 v246, v[100:101] offset:12288\n"
    "ds_write_b64 v246, v[108:109] offset:14336\n"
    "v_mul_f32 v156, v156, v236\n"
    "v_mul_f32 v204, v204, v237\n"
    "v_mul_f32 v157, v157, v236\n"
    "v_mul_f32 v205, v205, v237\n"
    "v_mul_f32 v158, v158, v236\n"
    "v_mul_f32 v206, v206, v237\n"
    "v_mul_f32 v159, v159, v236\n"
    "v_mul_f32 v207, v207, v237\n"
    "v_cvt_pk_bf16_f32 v156, v156, v157\n"
    "v_cvt_pk_bf16_f32 v204, v204, v205\n"
    "v_cvt_pk_bf16_f32 v157, v158, v159\n"
    "v_cvt_pk_bf16_f32 v205, v206, v207\n"
    "ds_write_b64 v247, v[156:157] offset:8192\n"
    "ds_write_b64 v247, v[204:205] offset:10240\n"
    "v_accvgpr_read_b32 v116, a124\n"
    "v_accvgpr_read_b32 v124, a172\n"
    "v_accvgpr_read_b32 v117, a125\n"
    "v_accvgpr_read_b32 v125, a173\n"
    "v_accvgpr_read_b32 v118, a126\n"
    "v_accvgpr_read_b32 v126, a174\n"
    "v_accvgpr_read_b32 v119, a127\n"
    "v_accvgpr_read_b32 v127, a175\n"
    "v_mul_f32 v116, v116, v238\n"
    "v_mul_f32 v124, v124, v239\n"
    "v_mul_f32 v117, v117, v238\n"
    "v_mul_f32 v125, v125, v239\n"
    "v_mul_f32 v118, v118, v238\n"
    "v_mul_f32 v126, v126, v239\n"
    "v_mul_f32 v119, v119, v238\n"
    "v_mul_f32 v127, v127, v239\n"
    "v_cvt_pk_bf16_f32 v116, v116, v117\n"
    "v_cvt_pk_bf16_f32 v124, v124, v125\n"
    "v_cvt_pk_bf16_f32 v117, v118, v119\n"
    "v_cvt_pk_bf16_f32 v125, v126, v127\n"
    "ds_write_b64 v247, v[116:117] offset:12288\n"
    "ds_write_b64 v247, v[124:125] offset:14336\n"
    "ds_read_b128 a[112:115], v249 offset:8192\n"
    "ds_read_b128 a[116:119], v249 offset:9216\n"
    "ds_read_b128 a[120:123], v249 offset:10240\n"
    "ds_read_b128 a[124:127], v249 offset:11264\n"
    "ds_read_b128 a[160:163], v249 offset:12288\n"
    "ds_read_b128 a[164:167], v249 offset:13312\n"
    "ds_read_b128 a[168:171], v249 offset:14336\n"
    "ds_read_b128 a[172:175], v249 offset:15360\n"
    "v_mul_f32 v160, v160, v236\n"
    "v_mul_f32 v208, v208, v237\n"
    "v_mul_f32 v161, v161, v236\n"
    "v_mul_f32 v209, v209, v237\n"
    "v_mul_f32 v162, v162, v236\n"
    "v_mul_f32 v210, v210, v237\n"
    "v_mul_f32 v163, v163, v236\n"
    "v_mul_f32 v211, v211, v237\n"
    "v_cvt_pk_bf16_f32 v160, v160, v161\n"
    "v_cvt_pk_bf16_f32 v208, v208, v209\n"
    "v_cvt_pk_bf16_f32 v161, v162, v163\n"
    "v_cvt_pk_bf16_f32 v209, v210, v211\n"
    "ds_write_b64 v248, v[160:161] offset:0\n"
    "ds_write_b64 v248, v[208:209] offset:2048\n"
    "s_waitcnt lgkmcnt(6)\n"
    "buffer_store_dwordx4 a[112:115], v250, s[80:83], 0 offen offset:128 nt sc1\n"
    "buffer_store_dwordx4 a[116:119], v250, s[80:83], 0 offen offset:3200 nt sc1\n"
    "buffer_store_dwordx4 a[120:123], v250, s[80:83], s42 offen offset:128 nt sc1\n"
    "buffer_store_dwordx4 a[124:127], v250, s[80:83], s42 offen offset:3200 nt sc1\n"
    "s_waitcnt lgkmcnt(2)\n"
    "buffer_store_dwordx4 a[160:163], v250, s[80:83], s43 offen offset:128 nt sc1\n"
    "buffer_store_dwordx4 a[164:167], v250, s[80:83], s43 offen offset:3200 nt sc1\n"
    "buffer_store_dwordx4 a[168:171], v250, s[80:83], s44 offen offset:128 nt sc1\n"
    "buffer_store_dwordx4 a[172:175], v250, s[80:83], s44 offen offset:3200 nt sc1\n"
    "v_accvgpr_read_b32 v68, a128\n"
    "v_accvgpr_read_b32 v76, a176\n"
    "v_accvgpr_read_b32 v69, a129\n"
    "v_accvgpr_read_b32 v77, a177\n"
    "v_accvgpr_read_b32 v70, a130\n"
    "v_accvgpr_read_b32 v78, a178\n"
    "v_accvgpr_read_b32 v71, a131\n"
    "v_accvgpr_read_b32 v79, a179\n"
    "v_mul_f32 v68, v68, v238\n"
    "v_mul_f32 v76, v76, v239\n"
    "v_mul_f32 v69, v69, v238\n"
    "v_mul_f32 v77, v77, v239\n"
    "v_mul_f32 v70, v70, v238\n"
    "v_mul_f32 v78, v78, v239\n"
    "v_mul_f32 v71, v71, v238\n"
    "v_mul_f32 v79, v79, v239\n"
    "v_cvt_pk_bf16_f32 v68, v68, v69\n"
    "v_cvt_pk_bf16_f32 v76, v76, v77\n"
    "v_cvt_pk_bf16_f32 v69, v70, v71\n"
    "v_cvt_pk_bf16_f32 v77, v78, v79\n"
    "ds_write_b64 v248, v[68:69] offset:4096\n"
    "ds_write_b64 v248, v[76:77] offset:6144\n"
    "v_mul_f32 v164, v164, v236\n"
    "v_mul_f32 v212, v212, v237\n"
    "v_mul_f32 v165, v165, v236\n"
    "v_mul_f32 v213, v213, v237\n"
    "v_mul_f32 v166, v166, v236\n"
    "v_mul_f32 v214, v214, v237\n"
    "v_mul_f32 v167, v167, v236\n"
    "v_mul_f32 v215, v215, v237\n"
    "v_cvt_pk_bf16_f32 v164, v164, v165\n"
    "v_cvt_pk_bf16_f32 v212, v212, v213\n"
    "v_cvt_pk_bf16_f32 v165, v166, v167\n"
    "v_cvt_pk_bf16_f32 v213, v214, v215\n"
    "ds_write_b64 v245, v[164:165] offset:0\n"
    "ds_write_b64 v245, v[212:213] offset:2048\n"
    "v_accvgpr_read_b32 v84, a132\n"
    "v_accvgpr_read_b32 v92, a180\n"
    "v_accvgpr_read_b32 v85, a133\n"
    "v_accvgpr_read_b32 v93, a181\n"
    "v_accvgpr_read_b32 v86, a134\n"
    "v_accvgpr_read_b32 v94, a182\n"
    "v_accvgpr_read_b32 v87, a135\n"
    "v_accvgpr_read_b32 v95, a183\n"
    "v_mul_f32 v84, v84, v238\n"
    "v_mul_f32 v92, v92, v239\n"
    "v_mul_f32 v85, v85, v238\n"
    "v_mul_f32 v93, v93, v239\n"
    "v_mul_f32 v86, v86, v238\n"
    "v_mul_f32 v94, v94, v239\n"
    "v_mul_f32 v87, v87, v238\n"
    "v_mul_f32 v95, v95, v239\n"
    "v_cvt_pk_bf16_f32 v84, v84, v85\n"
    "v_cvt_pk_bf16_f32 v92, v92, v93\n"
    "v_cvt_pk_bf16_f32 v85, v86, v87\n"
    "v_cvt_pk_bf16_f32 v93, v94, v95\n"
    "ds_write_b64 v245, v[84:85] offset:4096\n"
    "ds_write_b64 v245, v[92:93] offset:6144\n"
    "v_mul_f32 v168, v168, v236\n"
    "v_mul_f32 v216, v216, v237\n"
    "v_mul_f32 v169, v169, v236\n"
    "v_mul_f32 v217, v217, v237\n"
    "v_mul_f32 v170, v170, v236\n"
    "v_mul_f32 v218, v218, v237\n"
    "v_mul_f32 v171, v171, v236\n"
    "v_mul_f32 v219, v219, v237\n"
    "v_cvt_pk_bf16_f32 v168, v168, v169\n"
    "v_cvt_pk_bf16_f32 v216, v216, v217\n"
    "v_cvt_pk_bf16_f32 v169, v170, v171\n"
    "v_cvt_pk_bf16_f32 v217, v218, v219\n"
    "ds_write_b64 v246, v[168:169] offset:0\n"
    "ds_write_b64 v246, v[216:217] offset:2048\n"
    "v_accvgpr_read_b32 v100, a136\n"
    "v_accvgpr_read_b32 v108, a184\n"
    "v_accvgpr_read_b32 v101, a137\n"
    "v_accvgpr_read_b32 v109, a185\n"
    "v_accvgpr_read_b32 v102, a138\n"
    "v_accvgpr_read_b32 v110, a186\n"
    "v_accvgpr_read_b32 v103, a139\n"
    "v_accvgpr_read_b32 v111, a187\n"
    "v_mul_f32 v100, v100, v238\n"
    "v_mul_f32 v108, v108, v239\n"
    "v_mul_f32 v101, v101, v238\n"
    "v_mul_f32 v109, v109, v239\n"
    "v_mul_f32 v102, v102, v238\n"
    "v_mul_f32 v110, v110, v239\n"
    "v_mul_f32 v103, v103, v238\n"
    "v_mul_f32 v111, v111, v239\n"
    "v_cvt_pk_bf16_f32 v100, v100, v101\n"
    "v_cvt_pk_bf16_f32 v108, v108, v109\n"
    "v_cvt_pk_bf16_f32 v101, v102, v103\n"
    "v_cvt_pk_bf16_f32 v109, v110, v111\n"
    "ds_write_b64 v246, v[100:101] offset:4096\n"
    "ds_write_b64 v246, v[108:109] offset:6144\n"
    "v_mul_f32 v172, v172, v236\n"
    "v_mul_f32 v220, v220, v237\n"
    "v_mul_f32 v173, v173, v236\n"
    "v_mul_f32 v221, v221, v237\n"
    "v_mul_f32 v174, v174, v236\n"
    "v_mul_f32 v222, v222, v237\n"
    "v_mul_f32 v175, v175, v236\n"
    "v_mul_f32 v223, v223, v237\n"
    "v_cvt_pk_bf16_f32 v172, v172, v173\n"
    "v_cvt_pk_bf16_f32 v220, v220, v221\n"
    "v_cvt_pk_bf16_f32 v173, v174, v175\n"
    "v_cvt_pk_bf16_f32 v221, v222, v223\n"
    "ds_write_b64 v247, v[172:173] offset:0\n"
    "ds_write_b64 v247, v[220:221] offset:2048\n"
    "v_accvgpr_read_b32 v116, a140\n"
    "v_accvgpr_read_b32 v124, a188\n"
    "v_accvgpr_read_b32 v117, a141\n"
    "v_accvgpr_read_b32 v125, a189\n"
    "v_accvgpr_read_b32 v118, a142\n"
    "v_accvgpr_read_b32 v126, a190\n"
    "v_accvgpr_read_b32 v119, a143\n"
    "v_accvgpr_read_b32 v127, a191\n"
    "v_mul_f32 v116, v116, v238\n"
    "v_mul_f32 v124, v124, v239\n"
    "v_mul_f32 v117, v117, v238\n"
    "v_mul_f32 v125, v125, v239\n"
    "v_mul_f32 v118, v118, v238\n"
    "v_mul_f32 v126, v126, v239\n"
    "v_mul_f32 v119, v119, v238\n"
    "v_mul_f32 v127, v127, v239\n"
    "v_cvt_pk_bf16_f32 v116, v116, v117\n"
    "v_cvt_pk_bf16_f32 v124, v124, v125\n"
    "v_cvt_pk_bf16_f32 v117, v118, v119\n"
    "v_cvt_pk_bf16_f32 v125, v126, v127\n"
    "ds_write_b64 v247, v[116:117] offset:4096\n"
    "ds_write_b64 v247, v[124:125] offset:6144\n"
    "ds_read_b128 a[128:131], v249 offset:0\n"
    "ds_read_b128 a[132:135], v249 offset:1024\n"
    "ds_read_b128 a[136:139], v249 offset:2048\n"
    "ds_read_b128 a[140:143], v249 offset:3072\n"
    "ds_read_b128 a[176:179], v249 offset:4096\n"
    "ds_read_b128 a[180:183], v249 offset:5120\n"
    "ds_read_b128 a[184:187], v249 offset:6144\n"
    "ds_read_b128 a[188:191], v249 offset:7168\n"
    "s_waitcnt lgkmcnt(4)\n"
    "buffer_store_dwordx4 a[128:131], v250, s[80:83], 0 offen offset:256 nt sc1\n"
    "buffer_store_dwordx4 a[132:135], v250, s[80:83], 0 offen offset:3328 nt sc1\n"
    "buffer_store_dwordx4 a[136:139], v250, s[80:83], s42 offen offset:256 nt sc1\n"
    "buffer_store_dwordx4 a[140:143], v250, s[80:83], s42 offen offset:3328 nt sc1\n"
    "s_waitcnt lgkmcnt(0)\n"
    "buffer_store_dwordx4 a[176:179], v250, s[80:83], s43 offen offset:256 nt sc1\n"
    "buffer_store_dwordx4 a[180:183], v250, s[80:83], s43 offen offset:3328 nt sc1\n"
    "buffer_store_dwordx4 a[184:187], v250, s[80:83], s44 offen offset:256 nt sc1\n"
    "buffer_store_dwordx4 a[188:191], v250, s[80:83], s44 offen offset:3328 nt sc1\n"
    "s_branch LEND_%=\n"
    "LSRAf_%=:\n"
    "s_call_b64 s[50:51], LSRABODY_%=\n"
    "s_branch LSRAbf_%=\n"
    "LSRAe_%=:\n"
    "s_call_b64 s[50:51], LSRABODY_%=\n"
    "s_branch LSRAbe_%=\n"
    "LSRAo_%=:\n"
    "s_call_b64 s[50:51], LSRABODY_%=\n"
    "s_branch LSRAbo_%=\n"
    "LSRAx_%=:\n"
    "s_call_b64 s[50:51], LSRABODY_%=\n"
    "s_branch LSRAbx_%=\n"
    "LSRABODY_%=:\n"
    "v_mov_b32 v231, v236\n"
    "v_mov_b32 v255, v237\n"
    "s_nop 1\n"
    "v_permlane16_swap_b32 v236, v231\n"
    "v_permlane16_swap_b32 v237, v255\n"
    "s_nop 1\n"
    "v_max_f32 v236, v236, v231\n"
    "v_max_f32 v237, v237, v255\n"
    "v_mov_b32 v231, v236\n"
    "v_mov_b32 v255, v237\n"
    "s_nop 1\n"
    "v_permlane32_swap_b32 v236, v231\n"
    "v_permlane32_swap_b32 v237, v255\n"
    "s_nop 1\n"
    "v_max_f32 v236, v236, v231\n"
    "v_max_f32 v237, v237, v255\n"
    "v_max_f32 v236, 0, v236\n"
    "v_max_f32 v237, 0, v237\n"
    "v_sub_f32 v231, v232, v236\n"
    "v_sub_f32 v255, v233, v237\n"
    "v_sub_f32 v236, v231, v232\n"
    "v_sub_f32 v237, v255, v233\n"
    "v_exp_f32 v236, v236\n"
    "v_exp_f32 v237, v237\n"
    "v_mov_b32 v232, v231\n"
    "v_mov_b32 v233, v255\n"
    "v_mov_b32 v231, v238\n"
    "v_mov_b32 v255, v239\n"
    "s_nop 1\n"
    "v_permlane16_swap_b32 v238, v231\n"
    "v_permlane16_swap_b32 v239, v255\n"
    "s_nop 1\n"
    "v_max_f32 v238, v238, v231\n"
    "v_max_f32 v239, v239, v255\n"
    "v_mov_b32 v231, v238\n"
    "v_mov_b32 v255, v239\n"
    "s_nop 1\n"
    "v_permlane32_swap_b32 v238, v231\n"
    "v_permlane32_swap_b32 v239, v255\n"
    "s_nop 1\n"
    "v_max_f32 v238, v238, v231\n"
    "v_max_f32 v239, v239, v255\n"
    "v_max_f32 v238, 0, v238\n"
    "v_max_f32 v239, 0, v239\n"
    "v_sub_f32 v231, v234, v238\n"
    "v_sub_f32 v255, v235, v239\n"
    "v_sub_f32 v238, v231, v234\n"
    "v_sub_f32 v239, v255, v235\n"
    "v_exp_f32 v238, v238\n"
    "v_exp_f32 v239, v239\n"
    "v_mov_b32 v234, v231\n"
    "v_mov_b32 v235, v255\n"
    "s_nop 1\n"
    "s_setpc_b64 s[50:51]\n"
    "LSRBe_%=:\n"
    "s_call_b64 s[50:51], LSRBODY_%=\n"
    "s_branch LSRBbe_%=\n"
    "LSRBo_%=:\n"
    "s_call_b64 s[50:51], LSRBODY_%=\n"
    "s_branch LSRBbo_%=\n"
    "LSRBd_%=:\n"
    "s_call_b64 s[50:51], LSRBODY_%=\n"
    "s_branch LSRBbd_%=\n"
    "LSRBODY_%=:\n"
    "s_nop 15\n"
    "v_mul_f32 v128, v128, v236\n"
    "v_mul_f32 v129, v129, v236\n"
    "v_mul_f32 v130, v130, v236\n"
    "v_mul_f32 v131, v131, v236\n"
    "v_mul_f32 v132, v132, v236\n"
    "v_mul_f32 v133, v133, v236\n"
    "v_mul_f32 v134, v134, v236\n"
    "v_mul_f32 v135, v135, v236\n"
    "v_mul_f32 v136, v136, v236\n"
    "v_mul_f32 v137, v137, v236\n"
    "v_mul_f32 v138, v138, v236\n"
    "v_mul_f32 v139, v139, v236\n"
    "v_mul_f32 v140, v140, v236\n"
    "v_mul_f32 v141, v141, v236\n"
    "v_mul_f32 v142, v142, v236\n"
    "v_mul_f32 v143, v143, v236\n"
    "v_mul_f32 v144, v144, v236\n"
    "v_mul_f32 v145, v145, v236\n"
    "v_mul_f32 v146, v146, v236\n"
    "v_mul_f32 v147, v147, v236\n"
    "v_mul_f32 v148, v148, v236\n"
    "v_mul_f32 v149, v149, v236\n"
    "v_mul_f32 v150, v150, v236\n"
    "v_mul_f32 v151, v151, v236\n"
    "v_mul_f32 v152, v152, v236\n"
    "v_mul_f32 v153, v153, v236\n"
    "v_mul_f32 v154, v154, v236\n"
    "v_mul_f32 v155, v155, v236\n"
    "v_mul_f32 v156, v156, v236\n"
    "v_mul_f32 v157, v157, v236\n"
    "v_mul_f32 v158, v158, v236\n"
    "v_mul_f32 v159, v159, v236\n"
    "v_mul_f32 v160, v160, v236\n"
    "v_mul_f32 v161, v161, v236\n"
    "v_mul_f32 v162, v162, v236\n"
    "v_mul_f32 v163, v163, v236\n"
    "v_mul_f32 v164, v164, v236\n"
    "v_mul_f32 v165, v165, v236\n"
    "v_mul_f32 v166, v166, v236\n"
    "v_mul_f32 v167, v167, v236\n"
    "v_mul_f32 v168, v168, v236\n"
    "v_mul_f32 v169, v169, v236\n"
    "v_mul_f32 v170, v170, v236\n"
    "v_mul_f32 v171, v171, v236\n"
    "v_mul_f32 v172, v172, v236\n"
    "v_mul_f32 v173, v173, v236\n"
    "v_mul_f32 v174, v174, v236\n"
    "v_mul_f32 v175, v175, v236\n"
    "v_mul_f32 v176, v176, v237\n"
    "v_mul_f32 v177, v177, v237\n"
    "v_mul_f32 v178, v178, v237\n"
    "v_mul_f32 v179, v179, v237\n"
    "v_mul_f32 v180, v180, v237\n"
    "v_mul_f32 v181, v181, v237\n"
    "v_mul_f32 v182, v182, v237\n"
    "v_mul_f32 v183, v183, v237\n"
    "v_mul_f32 v184, v184, v237\n"
    "v_mul_f32 v185, v185, v237\n"
    "v_mul_f32 v186, v186, v237\n"
    "v_mul_f32 v187, v187, v237\n"
    "v_mul_f32 v188, v188, v237\n"
    "v_mul_f32 v189, v189, v237\n"
    "v_mul_f32 v190, v190, v237\n"
    "v_mul_f32 v191, v191, v237\n"
    "v_mul_f32 v192, v192, v237\n"
    "v_mul_f32 v193, v193, v237\n"
    "v_mul_f32 v194, v194, v237\n"
    "v_mul_f32 v195, v195, v237\n"
    "v_mul_f32 v196, v196, v237\n"
    "v_mul_f32 v197, v197, v237\n"
    "v_mul_f32 v198, v198, v237\n"
    "v_mul_f32 v199, v199, v237\n"
    "v_mul_f32 v200, v200, v237\n"
    "v_mul_f32 v201, v201, v237\n"
    "v_mul_f32 v202, v202, v237\n"
    "v_mul_f32 v203, v203, v237\n"
    "v_mul_f32 v204, v204, v237\n"
    "v_mul_f32 v205, v205, v237\n"
    "v_mul_f32 v206, v206, v237\n"
    "v_mul_f32 v207, v207, v237\n"
    "v_mul_f32 v208, v208, v237\n"
    "v_mul_f32 v209, v209, v237\n"
    "v_mul_f32 v210, v210, v237\n"
    "v_mul_f32 v211, v211, v237\n"
    "v_mul_f32 v212, v212, v237\n"
    "v_mul_f32 v213, v213, v237\n"
    "v_mul_f32 v214, v214, v237\n"
    "v_mul_f32 v215, v215, v237\n"
    "v_mul_f32 v216, v216, v237\n"
    "v_mul_f32 v217, v217, v237\n"
    "v_mul_f32 v218, v218, v237\n"
    "v_mul_f32 v219, v219, v237\n"
    "v_mul_f32 v220, v220, v237\n"
    "v_mul_f32 v221, v221, v237\n"
    "v_mul_f32 v222, v222, v237\n"
    "v_mul_f32 v223, v223, v237\n"
    "v_accvgpr_read_b32 v244, a96\n"
    "v_accvgpr_read_b32 v245, a97\n"
    "v_accvgpr_read_b32 v246, a98\n"
    "v_accvgpr_read_b32 v247, a99\n"
    "v_mul_f32 v244, v244, v238\n"
    "v_mul_f32 v245, v245, v238\n"
    "v_mul_f32 v246, v246, v238\n"
    "v_mul_f32 v247, v247, v238\n"
    "v_accvgpr_write_b32 a96, v244\n"
    "v_accvgpr_write_b32 a97, v245\n"
    "v_accvgpr_write_b32 a98, v246\n"
    "v_accvgpr_write_b32 a99, v247\n"
    "v_accvgpr_read_b32 v244, a100\n"
    "v_accvgpr_read_b32 v245, a101\n"
    "v_accvgpr_read_b32 v246, a102\n"
    "v_accvgpr_read_b32 v247, a103\n"
    "v_mul_f32 v244, v244, v238\n"
    "v_mul_f32 v245, v245, v238\n"
    "v_mul_f32 v246, v246, v238\n"
    "v_mul_f32 v247, v247, v238\n"
    "v_accvgpr_write_b32 a100, v244\n"
    "v_accvgpr_write_b32 a101, v245\n"
    "v_accvgpr_write_b32 a102, v246\n"
    "v_accvgpr_write_b32 a103, v247\n"
    "v_accvgpr_read_b32 v244, a104\n"
    "v_accvgpr_read_b32 v245, a105\n"
    "v_accvgpr_read_b32 v246, a106\n"
    "v_accvgpr_read_b32 v247, a107\n"
    "v_mul_f32 v244, v244, v238\n"
    "v_mul_f32 v245, v245, v238\n"
    "v_mul_f32 v246, v246, v238\n"
    "v_mul_f32 v247, v247, v238\n"
    "v_accvgpr_write_b32 a104, v244\n"
    "v_accvgpr_write_b32 a105, v245\n"
    "v_accvgpr_write_b32 a106, v246\n"
    "v_accvgpr_write_b32 a107, v247\n"
    "v_accvgpr_read_b32 v244, a108\n"
    "v_accvgpr_read_b32 v245, a109\n"
    "v_accvgpr_read_b32 v246, a110\n"
    "v_accvgpr_read_b32 v247, a111\n"
    "v_mul_f32 v244, v244, v238\n"
    "v_mul_f32 v245, v245, v238\n"
    "v_mul_f32 v246, v246, v238\n"
    "v_mul_f32 v247, v247, v238\n"
    "v_accvgpr_write_b32 a108, v244\n"
    "v_accvgpr_write_b32 a109, v245\n"
    "v_accvgpr_write_b32 a110, v246\n"
    "v_accvgpr_write_b32 a111, v247\n"
    "v_accvgpr_read_b32 v244, a112\n"
    "v_accvgpr_read_b32 v245, a113\n"
    "v_accvgpr_read_b32 v246, a114\n"
    "v_accvgpr_read_b32 v247, a115\n"
    "v_mul_f32 v244, v244, v238\n"
    "v_mul_f32 v245, v245, v238\n"
    "v_mul_f32 v246, v246, v238\n"
    "v_mul_f32 v247, v247, v238\n"
    "v_accvgpr_write_b32 a112, v244\n"
    "v_accvgpr_write_b32 a113, v245\n"
    "v_accvgpr_write_b32 a114, v246\n"
    "v_accvgpr_write_b32 a115, v247\n"
    "v_accvgpr_read_b32 v244, a116\n"
    "v_accvgpr_read_b32 v245, a117\n"
    "v_accvgpr_read_b32 v246, a118\n"
    "v_accvgpr_read_b32 v247, a119\n"
    "v_mul_f32 v244, v244, v238\n"
    "v_mul_f32 v245, v245, v238\n"
    "v_mul_f32 v246, v246, v238\n"
    "v_mul_f32 v247, v247, v238\n"
    "v_accvgpr_write_b32 a116, v244\n"
    "v_accvgpr_write_b32 a117, v245\n"
    "v_accvgpr_write_b32 a118, v246\n"
    "v_accvgpr_write_b32 a119, v247\n"
    "v_accvgpr_read_b32 v244, a120\n"
    "v_accvgpr_read_b32 v245, a121\n"
    "v_accvgpr_read_b32 v246, a122\n"
    "v_accvgpr_read_b32 v247, a123\n"
    "v_mul_f32 v244, v244, v238\n"
    "v_mul_f32 v245, v245, v238\n"
    "v_mul_f32 v246, v246, v238\n"
    "v_mul_f32 v247, v247, v238\n"
    "v_accvgpr_write_b32 a120, v244\n"
    "v_accvgpr_write_b32 a121, v245\n"
    "v_accvgpr_write_b32 a122, v246\n"
    "v_accvgpr_write_b32 a123, v247\n"
    "v_accvgpr_read_b32 v244, a124\n"
    "v_accvgpr_read_b32 v245, a125\n"
    "v_accvgpr_read_b32 v246, a126\n"
    "v_accvgpr_read_b32 v247, a127\n"
    "v_mul_f32 v244, v244, v238\n"
    "v_mul_f32 v245, v245, v238\n"
    "v_mul_f32 v246, v246, v238\n"
    "v_mul_f32 v247, v247, v238\n"
    "v_accvgpr_write_b32 a124, v244\n"
    "v_accvgpr_write_b32 a125, v245\n"
    "v_accvgpr_write_b32 a126, v246\n"
    "v_accvgpr_write_b32 a127, v247\n"
    "v_accvgpr_read_b32 v244, a128\n"
    "v_accvgpr_read_b32 v245, a129\n"
    "v_accvgpr_read_b32 v246, a130\n"
    "v_accvgpr_read_b32 v247, a131\n"
    "v_mul_f32 v244, v244, v238\n"
    "v_mul_f32 v245, v245, v238\n"
    "v_mul_f32 v246, v246, v238\n"
    "v_mul_f32 v247, v247, v238\n"
    "v_accvgpr_write_b32 a128, v244\n"
    "v_accvgpr_write_b32 a129, v245\n"
    "v_accvgpr_write_b32 a130, v246\n"
    "v_accvgpr_write_b32 a131, v247\n"
    "v_accvgpr_read_b32 v244, a132\n"
    "v_accvgpr_read_b32 v245, a133\n"
    "v_accvgpr_read_b32 v246, a134\n"
    "v_accvgpr_read_b32 v247, a135\n"
    "v_mul_f32 v244, v244, v238\n"
    "v_mul_f32 v245, v245, v238\n"
    "v_mul_f32 v246, v246, v238\n"
    "v_mul_f32 v247, v247, v238\n"
    "v_accvgpr_write_b32 a132, v244\n"
    "v_accvgpr_write_b32 a133, v245\n"
    "v_accvgpr_write_b32 a134, v246\n"
    "v_accvgpr_write_b32 a135, v247\n"
    "v_accvgpr_read_b32 v244, a136\n"
    "v_accvgpr_read_b32 v245, a137\n"
    "v_accvgpr_read_b32 v246, a138\n"
    "v_accvgpr_read_b32 v247, a139\n"
    "v_mul_f32 v244, v244, v238\n"
    "v_mul_f32 v245, v245, v238\n"
    "v_mul_f32 v246, v246, v238\n"
    "v_mul_f32 v247, v247, v238\n"
    "v_accvgpr_write_b32 a136, v244\n"
    "v_accvgpr_write_b32 a137, v245\n"
    "v_accvgpr_write_b32 a138, v246\n"
    "v_accvgpr_write_b32 a139, v247\n"
    "v_accvgpr_read_b32 v244, a140\n"
    "v_accvgpr_read_b32 v245, a141\n"
    "v_accvgpr_read_b32 v246, a142\n"
    "v_accvgpr_read_b32 v247, a143\n"
    "v_mul_f32 v244, v244, v238\n"
    "v_mul_f32 v245, v245, v238\n"
    "v_mul_f32 v246, v246, v238\n"
    "v_mul_f32 v247, v247, v238\n"
    "v_accvgpr_write_b32 a140, v244\n"
    "v_accvgpr_write_b32 a141, v245\n"
    "v_accvgpr_write_b32 a142, v246\n"
    "v_accvgpr_write_b32 a143, v247\n"
    "v_accvgpr_read_b32 v244, a144\n"
    "v_accvgpr_read_b32 v245, a145\n"
    "v_accvgpr_read_b32 v246, a146\n"
    "v_accvgpr_read_b32 v247, a147\n"
    "v_mul_f32 v244, v244, v239\n"
    "v_mul_f32 v245, v245, v239\n"
    "v_mul_f32 v246, v246, v239\n"
    "v_mul_f32 v247, v247, v239\n"
    "v_accvgpr_write_b32 a144, v244\n"
    "v_accvgpr_write_b32 a145, v245\n"
    "v_accvgpr_write_b32 a146, v246\n"
    "v_accvgpr_write_b32 a147, v247\n"
    "v_accvgpr_read_b32 v244, a148\n"
    "v_accvgpr_read_b32 v245, a149\n"
    "v_accvgpr_read_b32 v246, a150\n"
    "v_accvgpr_read_b32 v247, a151\n"
    "v_mul_f32 v244, v244, v239\n"
    "v_mul_f32 v245, v245, v239\n"
    "v_mul_f32 v246, v246, v239\n"
    "v_mul_f32 v247, v247, v239\n"
    "v_accvgpr_write_b32 a148, v244\n"
    "v_accvgpr_write_b32 a149, v245\n"
    "v_accvgpr_write_b32 a150, v246\n"
    "v_accvgpr_write_b32 a151, v247\n"
    "v_accvgpr_read_b32 v244, a152\n"
    "v_accvgpr_read_b32 v245, a153\n"
    "v_accvgpr_read_b32 v246, a154\n"
    "v_accvgpr_read_b32 v247, a155\n"
    "v_mul_f32 v244, v244, v239\n"
    "v_mul_f32 v245, v245, v239\n"
    "v_mul_f32 v246, v246, v239\n"
    "v_mul_f32 v247, v247, v239\n"
    "v_accvgpr_write_b32 a152, v244\n"
    "v_accvgpr_write_b32 a153, v245\n"
    "v_accvgpr_write_b32 a154, v246\n"
    "v_accvgpr_write_b32 a155, v247\n"
    "v_accvgpr_read_b32 v244, a156\n"
    "v_accvgpr_read_b32 v245, a157\n"
    "v_accvgpr_read_b32 v246, a158\n"
    "v_accvgpr_read_b32 v247, a159\n"
    "v_mul_f32 v244, v244, v239\n"
    "v_mul_f32 v245, v245, v239\n"
    "v_mul_f32 v246, v246, v239\n"
    "v_mul_f32 v247, v247, v239\n"
    "v_accvgpr_write_b32 a156, v244\n"
    "v_accvgpr_write_b32 a157, v245\n"
    "v_accvgpr_write_b32 a158, v246\n"
    "v_accvgpr_write_b32 a159, v247\n"
    "v_accvgpr_read_b32 v244, a160\n"
    "v_accvgpr_read_b32 v245, a161\n"
    "v_accvgpr_read_b32 v246, a162\n"
    "v_accvgpr_read_b32 v247, a163\n"
    "v_mul_f32 v244, v244, v239\n"
    "v_mul_f32 v245, v245, v239\n"
    "v_mul_f32 v246, v246, v239\n"
    "v_mul_f32 v247, v247, v239\n"
    "v_accvgpr_write_b32 a160, v244\n"
    "v_accvgpr_write_b32 a161, v245\n"
    "v_accvgpr_write_b32 a162, v246\n"
    "v_accvgpr_write_b32 a163, v247\n"
    "v_accvgpr_read_b32 v244, a164\n"
    "v_accvgpr_read_b32 v245, a165\n"
    "v_accvgpr_read_b32 v246, a166\n"
    "v_accvgpr_read_b32 v247, a167\n"
    "v_mul_f32 v244, v244, v239\n"
    "v_mul_f32 v245, v245, v239\n"
    "v_mul_f32 v246, v246, v239\n"
    "v_mul_f32 v247, v247, v239\n"
    "v_accvgpr_write_b32 a164, v244\n"
    "v_accvgpr_write_b32 a165, v245\n"
    "v_accvgpr_write_b32 a166, v246\n"
    "v_accvgpr_write_b32 a167, v247\n"
    "v_accvgpr_read_b32 v244, a168\n"
    "v_accvgpr_read_b32 v245, a169\n"
    "v_accvgpr_read_b32 v246, a170\n"
    "v_accvgpr_read_b32 v247, a171\n"
    "v_mul_f32 v244, v244, v239\n"
    "v_mul_f32 v245, v245, v239\n"
    "v_mul_f32 v246, v246, v239\n"
    "v_mul_f32 v247, v247, v239\n"
    "v_accvgpr_write_b32 a168, v244\n"
    "v_accvgpr_write_b32 a169, v245\n"
    "v_accvgpr_write_b32 a170, v246\n"
    "v_accvgpr_write_b32 a171, v247\n"
    "v_accvgpr_read_b32 v244, a172\n"
    "v_accvgpr_read_b32 v245, a173\n"
    "v_accvgpr_read_b32 v246, a174\n"
    "v_accvgpr_read_b32 v247, a175\n"
    "v_mul_f32 v244, v244, v239\n"
    "v_mul_f32 v245, v245, v239\n"
    "v_mul_f32 v246, v246, v239\n"
    "v_mul_f32 v247, v247, v239\n"
    "v_accvgpr_write_b32 a172, v244\n"
    "v_accvgpr_write_b32 a173, v245\n"
    "v_accvgpr_write_b32 a174, v246\n"
    "v_accvgpr_write_b32 a175, v247\n"
    "v_accvgpr_read_b32 v244, a176\n"
    "v_accvgpr_read_b32 v245, a177\n"
    "v_accvgpr_read_b32 v246, a178\n"
    "v_accvgpr_read_b32 v247, a179\n"
    "v_mul_f32 v244, v244, v239\n"
    "v_mul_f32 v245, v245, v239\n"
    "v_mul_f32 v246, v246, v239\n"
    "v_mul_f32 v247, v247, v239\n"
    "v_accvgpr_write_b32 a176, v244\n"
    "v_accvgpr_write_b32 a177, v245\n"
    "v_accvgpr_write_b32 a178, v246\n"
    "v_accvgpr_write_b32 a179, v247\n"
    "v_accvgpr_read_b32 v244, a180\n"
    "v_accvgpr_read_b32 v245, a181\n"
    "v_accvgpr_read_b32 v246, a182\n"
    "v_accvgpr_read_b32 v247, a183\n"
    "v_mul_f32 v244, v244, v239\n"
    "v_mul_f32 v245, v245, v239\n"
    "v_mul_f32 v246, v246, v239\n"
    "v_mul_f32 v247, v247, v239\n"
    "v_accvgpr_write_b32 a180, v244\n"
    "v_accvgpr_write_b32 a181, v245\n"
    "v_accvgpr_write_b32 a182, v246\n"
    "v_accvgpr_write_b32 a183, v247\n"
    "v_accvgpr_read_b32 v244, a184\n"
    "v_accvgpr_read_b32 v245, a185\n"
    "v_accvgpr_read_b32 v246, a186\n"
    "v_accvgpr_read_b32 v247, a187\n"
    "v_mul_f32 v244, v244, v239\n"
    "v_mul_f32 v245, v245, v239\n"
    "v_mul_f32 v246, v246, v239\n"
    "v_mul_f32 v247, v247, v239\n"
    "v_accvgpr_write_b32 a184, v244\n"
    "v_accvgpr_write_b32 a185, v245\n"
    "v_accvgpr_write_b32 a186, v246\n"
    "v_accvgpr_write_b32 a187, v247\n"
    "v_accvgpr_read_b32 v244, a188\n"
    "v_accvgpr_read_b32 v245, a189\n"
    "v_accvgpr_read_b32 v246, a190\n"
    "v_accvgpr_read_b32 v247, a191\n"
    "v_mul_f32 v244, v244, v239\n"
    "v_mul_f32 v245, v245, v239\n"
    "v_mul_f32 v246, v246, v239\n"
    "v_mul_f32 v247, v247, v239\n"
    "v_accvgpr_write_b32 a188, v244\n"
    "v_accvgpr_write_b32 a189, v245\n"
    "v_accvgpr_write_b32 a190, v246\n"
    "v_accvgpr_write_b32 a191, v247\n"
    "v_accvgpr_read_b32 v244, a240\n"
    "v_accvgpr_read_b32 v245, a244\n"
    "v_accvgpr_read_b32 v246, a248\n"
    "v_accvgpr_read_b32 v247, a252\n"
    "v_mul_f32 v244, v244, v236\n"
    "v_mul_f32 v245, v245, v237\n"
    "v_mul_f32 v246, v246, v238\n"
    "v_mul_f32 v247, v247, v239\n"
    "v_accvgpr_write_b32 a240, v244\n"
    "v_accvgpr_write_b32 a244, v245\n"
    "v_accvgpr_write_b32 a248, v246\n"
    "v_accvgpr_write_b32 a252, v247\n"
    "s_nop 3\n"
    "s_setpc_b64 s[50:51]\n"
    "LEND_%=:\n"
    :
    : "s"(__builtin_amdgcn_kernarg_segment_ptr()), "s"(bid), "s"(wid), "s"(ldsb)
    : "s4", "s5", "s6", "s7", "s8", "s9", "s10", "s11", "s12", "s13", "s14", "s15", "s16", "s17", "s18", "s19", "s20", "s21", "s22", "s23", "s24", "s25", "s26", "s27", "s28", "s29", "s30", "s31", "s36", "s37", "s38", "s39", "s40", "s41", "s42", "s43", "s44", "s45", "s46", "s47", "s48", "s49", "s50", "s51", "s52", "s53", "s54", "s55", "s56", "s57", "s58", "s59", "s60", "s61", "s62", "s63", "s64", "s65", "s66", "s67", "s68", "s69", "s70", "s71", "s72", "s73", "s74", "s75", "s76", "s77", "s78", "s79", "s80", "s81", "s82", "s83", "s84", "s85", "s86", "s87", "s88", "s89", "s90", "s91", "s92", "s93", "s94", "s95", "vcc", "scc", "memory", "v0", "v1", "v2", "v3", "v4", "v5", "v6", "v7", "v8", "v9", "v10", "v11", "v12", "v13", "v14", "v15", "v16", "v17", "v18", "v19", "v20", "v21", "v22", "v23", "v24", "v25", "v26", "v27", "v28", "v29", "v30", "v31", "v32", "v33", "v34", "v35", "v36", "v37", "v38", "v39", "v40", "v41", "v42", "v43", "v44", "v45", "v46", "v47", "v48", "v49", "v50", "v51", "v52", "v53", "v54", "v55", "v56", "v57", "v58", "v59", "v60", "v61", "v62", "v63", "v64", "v65", "v66", "v67", "v68", "v69", "v70", "v71", "v72", "v73", "v74", "v75", "v76", "v77", "v78", "v79", "v80", "v81", "v82", "v83", "v84", "v85", "v86", "v87", "v88", "v89", "v90", "v91", "v92", "v93", "v94", "v95", "v96", "v97", "v98", "v99", "v100", "v101", "v102", "v103", "v104", "v105", "v106", "v107", "v108", "v109", "v110", "v111", "v112", "v113", "v114", "v115", "v116", "v117", "v118", "v119", "v120", "v121", "v122", "v123", "v124", "v125", "v126", "v127", "v128", "v129", "v130", "v131", "v132", "v133", "v134", "v135", "v136", "v137", "v138", "v139", "v140", "v141", "v142", "v143", "v144", "v145", "v146", "v147", "v148", "v149", "v150", "v151", "v152", "v153", "v154", "v155", "v156", "v157", "v158", "v159", "v160", "v161", "v162", "v163", "v164", "v165", "v166", "v167", "v168", "v169", "v170", "v171", "v172", "v173", "v174", "v175", "v176", "v177", "v178", "v179", "v180", "v181", "v182", "v183", "v184", "v185", "v186", "v187", "v188", "v189", "v190", "v191", "v192", "v193", "v194", "v195", "v196", "v197", "v198", "v199", "v200", "v201", "v202", "v203", "v204", "v205", "v206", "v207", "v208", "v209", "v210", "v211", "v212", "v213", "v214", "v215", "v216", "v217", "v218", "v219", "v220", "v221", "v222", "v223", "v224", "v225", "v226", "v227", "v228", "v229", "v230", "v231", "v232", "v233", "v234", "v235", "v236", "v237", "v238", "v239", "v240", "v241", "v242", "v243", "v244", "v245", "v246", "v247", "v248", "v249", "v250", "v251", "v252", "v253", "v254", "v255", "a0", "a1", "a2", "a3", "a4", "a5", "a6", "a7", "a8", "a9", "a10", "a11", "a12", "a13", "a14", "a15", "a16", "a17", "a18", "a19", "a20", "a21", "a22", "a23", "a24", "a25", "a26", "a27", "a28", "a29", "a30", "a31", "a32", "a33", "a34", "a35", "a36", "a37", "a38", "a39", "a40", "a41", "a42", "a43", "a44", "a45", "a46", "a47", "a48", "a49", "a50", "a51", "a52", "a53", "a54", "a55", "a56", "a57", "a58", "a59", "a60", "a61", "a62", "a63", "a64", "a65", "a66", "a67", "a68", "a69", "a70", "a71", "a72", "a73", "a74", "a75", "a76", "a77", "a78", "a79", "a80", "a81", "a82", "a83", "a84", "a85", "a86", "a87", "a88", "a89", "a90", "a91", "a92", "a93", "a94", "a95", "a96", "a97", "a98", "a99", "a100", "a101", "a102", "a103", "a104", "a105", "a106", "a107", "a108", "a109", "a110", "a111", "a112", "a113", "a114", "a115", "a116", "a117", "a118", "a119", "a120", "a121", "a122", "a123", "a124", "a125", "a126", "a127", "a128", "a129", "a130", "a131", "a132", "a133", "a134", "a135", "a136", "a137", "a138", "a139", "a140", "a141", "a142", "a143", "a144", "a145", "a146", "a147", "a148", "a149", "a150", "a151", "a152", "a153", "a154", "a155", "a156", "a157", "a158", "a159", "a160", "a161", "a162", "a163", "a164", "a165", "a166", "a167", "a168", "a169", "a170", "a171", "a172", "a173", "a174", "a175", "a176", "a177", "a178", "a179", "a180", "a181", "a182", "a183", "a184", "a185", "a186", "a187", "a188", "a189", "a190", "a191", "a192", "a193", "a194", "a195", "a196", "a197", "a198", "a199", "a200", "a201", "a202", "a203", "a204", "a205", "a206", "a207", "a208", "a209", "a210", "a211", "a212", "a213", "a214", "a215", "a216", "a217", "a218", "a219", "a220", "a221", "a222", "a223", "a224", "a225", "a226", "a227", "a228", "a229", "a230", "a231", "a232", "a233", "a234", "a235", "a236", "a237", "a238", "a239", "a240", "a241", "a242", "a243", "a244", "a245", "a246", "a247", "a248", "a249", "a250", "a251", "a252", "a253", "a254", "a255");
}

extern "C" void kernel_launch(void* const* d_in, const int* in_sizes, int n_in, void* d_out, int out_size, void* d_ws, size_t ws_size, hipStream_t stream) {
  hipLaunchKernelGGL(attn_fwd, dim3(256), dim3(256), 0, stream, d_in[0], d_in[1], d_in[2], d_out, d_ws);
}
```
